```python
import torch

def _softmax(x, axis=-1):
    return torch.softmax(x, dim=axis)

def _gelu(x):
    return torch.nn.functional.gelu(x, approximate='tanh')

def _silu(x):
    return torch.nn.functional.silu(x)

def _sigmoid(x):
    return torch.sigmoid(x)

def _layernorm(x, axis=-1, eps=1e-05):
    m = x.mean(dim=axis, keepdim=True)
    v = x.var(dim=axis, keepdim=True, unbiased=False)
    return (x - m) / torch.sqrt(v + eps)

def _scatter_add(tgt, idx, src, axis=0):
    return tgt.clone().index_add_(axis, idx, src)

def _segment_sum(src, seg, n):
    out = torch.zeros((n,) + src.shape[1:], dtype=src.dtype)
    return out.index_add_(0, seg, src)

def _pad(x, pw):
    flat = tuple((v for pair in reversed(pw) for v in pair))
    return torch.nn.functional.pad(x, flat)
kernel_name = 'adaLN_cond__sliding_block_cached__dense_activation_silu__decode'

def setup_inputs():

    _w = lambda a: (lambda t: t.to(torch.bfloat16) if t.is_floating_point() else t)(torch.as_tensor(a))

    def _A_setup():
        B, Sq, D = (8, 1, 640)
        Hq, Hkv, Sk, Dh = (32, 32, 4096, 128)
        assert Hq % Hkv == 0, (Hq, Hkv)
        return {'x': _w((lambda *s: torch.randn(*s, dtype=torch.bfloat16) * 0.1)(B, Sq, D)), 'k_cache': _w((lambda *s: torch.randn(*s, dtype=torch.bfloat16) * 0.8)(B, Hkv, Sk, Dh)), 'v_cache': _w((lambda *s: torch.randn(*s, dtype=torch.bfloat16) * 3.2)(B, Hkv, Sk, Dh)), 'wq': _w((lambda *s: torch.randn(*s, dtype=torch.bfloat16) * 0.8)(D, Hq * Dh) * D ** (-0.5)), 'wo': _w((lambda *s: torch.randn(*s, dtype=torch.bfloat16) * 3.2)(Hq * Dh, D) * (Hq * Dh) ** (-0.5))}
    _A_in = _A_setup()

    def _B_setup():
        B, S, D, F = (8, 1, 640, 1728)
        return {'x': _w((lambda *s: torch.randn(*s, dtype=torch.bfloat16) * 0.1)(B, S, D)), 'w1': _w((lambda *s: torch.randn(*s, dtype=torch.bfloat16) * 3.2)(D, F) * D ** (-0.5)), 'w2': _w((lambda *s: torch.randn(*s, dtype=torch.bfloat16) * 3.2)(F, D) * F ** (-0.5))}
    _B_in = _B_setup()
    return {'A_x': _A_in['x'], 'A_k_cache': _A_in['k_cache'], 'A_v_cache': _A_in['v_cache'], 'A_wq': _A_in['wq'], 'A_wo': _A_in['wo'], 'B_x': _B_in['x'], 'B_w1': _B_in['w1'], 'B_w2': _B_in['w2'], 'c': _w((lambda *s: torch.randn(*s, dtype=torch.bfloat16) * 0.1)(8, 640) * 8.0), 'w_mod': _w((lambda *s: torch.randn(*s, dtype=torch.bfloat16) * 0.1)(640, 6 * 640) * 640 ** (-0.5) * 8.0)}

def reference(A_x, A_k_cache, A_v_cache, A_wq, A_wo, B_x, B_w1, B_w2, c, w_mod):

    def _A_ref(x, k_cache, v_cache, wq, wo):
        B, Sq, D = x.shape
        Hq, Dh = (32, 128)
        Hkv, Sk = (k_cache.shape[1], k_cache.shape[2])
        W = min(4096, max(256, Sk // 4))
        rep = Hq // Hkv
        q = (lambda x, *d: x.permute(*d))((x @ wq).reshape(B, Sq, Hq, Dh), 0, 2, 1, 3)
        k = (lambda x, n, axis: torch.repeat_interleave(x, n, dim=axis))(k_cache[:, :, Sk - W:, :], rep, axis=1)
        v = (lambda x, n, axis: torch.repeat_interleave(x, n, dim=axis))(v_cache[:, :, Sk - W:, :], rep, axis=1)
        scores = torch.einsum('bhqd,bhkd->bhqk', q, k) * Dh ** (-0.5)
        p = _softmax(scores, axis=-1)
        o = torch.einsum('bhqk,bhkd->bhqd', p, v)
        return (lambda x, *d: x.permute(*d))(o, 0, 2, 1, 3).reshape(B, Sq, Hq * Dh) @ wo

    def _B_ref(x, w1, w2):
        h = torch.einsum('bsd,df->bsf', x, w1)
        h = _silu(h)
        out = torch.einsum('bsf,fd->bsd', h, w2)
        return out
    x = A_x
    mod = (_silu(c) @ w_mod).reshape(c.shape[0], 6, 1, -1)
    sa, ca, ga, sb, cb, gb = (mod[:, 0].clone(), mod[:, 1].clone(), mod[:, 2].clone(), mod[:, 3].clone(), mod[:, 4].clone(), mod[:, 5].clone())
    h = _layernorm(x, axis=-1) * (1 + ca) + sa
    x = x + ga * _A_ref(h, A_k_cache, A_v_cache, A_wq, A_wo)
    h = _layernorm(x, axis=-1) * (1 + cb) + sb
    x = x + gb * _B_ref(h, B_w1, B_w2)
    return x
```

```cpp
#include <hip/hip_runtime.h>
#include <hip/hip_bf16.h>
#include <stdint.h>

typedef unsigned short u16;
typedef unsigned v4u __attribute__((ext_vector_type(4)));

__device__ __forceinline__ float bf2f(u16 b) { return __uint_as_float((unsigned)b << 16); }
__device__ __forceinline__ float rbf(float f) {
  unsigned u = __float_as_uint(f); u += 0x7FFFu + ((u >> 16) & 1u); return __uint_as_float(u & 0xffff0000u);
}
__device__ __forceinline__ u16 f2bf(float f) { unsigned u = __float_as_uint(f); u += 0x7FFFu + ((u >> 16) & 1u); return (u16)(u >> 16); }

constexpr int B = 8, D = 640, H = 32, SK = 4096, DH = 128, W = 1024, F = 1728;

__global__ void k_gemv(const float* act, const u16* w, float* out, int K, int N) {
  __shared__ float red[4][8][64];
  int n = blockIdx.x * 64 + (threadIdx.x & 63);
  int ks = threadIdx.x >> 6;
  float acc[8];
  for (int b = 0; b < 8; ++b) acc[b] = 0.f;
  if (n < N) {
    for (int k = ks; k < K; k += 4) {
      float wv = bf2f(w[(size_t)k * N + n]);
      for (int b = 0; b < 8; ++b) acc[b] += act[b * K + k] * wv;
    }
  }
  for (int b = 0; b < 8; ++b) red[ks][b][threadIdx.x & 63] = acc[b];
  __syncthreads();
  if (ks == 0 && n < N) {
    for (int b = 0; b < 8; ++b) {
      float s = red[0][b][threadIdx.x] + red[1][b][threadIdx.x] + red[2][b][threadIdx.x] + red[3][b][threadIdx.x];
      out[b * N + n] = rbf(s);
    }
  }
}

__global__ void k_silu_c(const u16* c, float* sc) {
  int i = blockIdx.x * blockDim.x + threadIdx.x;
  if (i < B * D) { float v = bf2f(c[i]); sc[i] = rbf(v / (1.f + __expf(-v))); }
}

__global__ void k_ln(const float* xin, const float* mod, int soff, int coff, float* h) {
  int b = blockIdx.x;
  __shared__ float red[2][10];
  int t = threadIdx.x;
  float x = xin[b * D + t];
  float s = x;
  for (int m = 32; m >= 1; m >>= 1) s += __shfl_xor(s, m);
  if ((t & 63) == 0) red[0][t >> 6] = s;
  __syncthreads();
  float tot = 0; for (int i = 0; i < 10; ++i) tot += red[0][i];
  float meanf = tot / D;
  float mean = rbf(meanf);
  float d = x - meanf; float q = d * d;
  for (int m = 32; m >= 1; m >>= 1) q += __shfl_xor(q, m);
  if ((t & 63) == 0) red[1][t >> 6] = q;
  __syncthreads();
  float tq = 0; for (int i = 0; i < 10; ++i) tq += red[1][i];
  float var = rbf(tq / D);
  float den = rbf(sqrtf(rbf(var + 1e-5f)));
  float ln = rbf(rbf(x - mean) / den);
  float c1 = rbf(1.f + mod[b * 3840 + coff + t]);
  h[b * D + t] = rbf(rbf(ln * c1) + mod[b * 3840 + soff + t]);
}

__global__ void k_cvt(const u16* x, float* o, int n) { int i = blockIdx.x * blockDim.x + threadIdx.x; if (i < n) o[i] = bf2f(x[i]); }

__global__ void __launch_bounds__(256) k_attn(const float* q, const u16* kc, const u16* vc, float* o) {
  int bh = blockIdx.x;
  int b = bh >> 5, h = bh & 31;
  __shared__ float sq[DH];
  __shared__ float sc[W];
  __shared__ float red[8];
  __shared__ float oacc[4][DH];
  int t = threadIdx.x;
  if (t < DH) sq[t] = q[b * 4096 + h * DH + t];
  __syncthreads();
  const u16* kb = kc + ((size_t)bh * SK + (SK - W)) * DH;
  const u16* vb = vc + ((size_t)bh * SK + (SK - W)) * DH;
  int lane = t & 63, wv = t >> 6;
  int r = lane >> 4, c = lane & 15;
  float qv[8]; for (int j = 0; j < 8; ++j) qv[j] = sq[c * 8 + j];
  for (int k0 = wv * 4; k0 < W; k0 += 16) {
    int k = k0 + r;
    v4u kv = *(const v4u*)(kb + (size_t)k * DH + c * 8);
    float s = 0;
    for (int j = 0; j < 4; ++j) { s += qv[2 * j] * __uint_as_float(kv[j] << 16); s += qv[2 * j + 1] * __uint_as_float(kv[j] & 0xffff0000u); }
    s += __shfl_xor(s, 1); s += __shfl_xor(s, 2); s += __shfl_xor(s, 4); s += __shfl_xor(s, 8);
    if (c == 0) sc[k] = rbf(rbf(s) * 0.08838834764831845f);
  }
  __syncthreads();
  float m = -INFINITY;
  for (int k = t; k < W; k += 256) m = fmaxf(m, sc[k]);
  for (int o2 = 32; o2 >= 1; o2 >>= 1) m = fmaxf(m, __shfl_xor(m, o2));
  if (lane == 0) red[wv] = m;
  __syncthreads();
  m = fmaxf(fmaxf(red[0], red[1]), fmaxf(red[2], red[3]));
  float s = 0;
  for (int k = t; k < W; k += 256) { float e = __expf(sc[k] - m); sc[k] = e; s += e; }
  for (int o2 = 32; o2 >= 1; o2 >>= 1) s += __shfl_xor(s, o2);
  if (lane == 0) red[4 + wv] = s;
  __syncthreads();
  s = red[4] + red[5] + red[6] + red[7];
  float inv = 1.f / s;
  float acc[8]; for (int j = 0; j < 8; ++j) acc[j] = 0;
  for (int k0 = wv * 4; k0 < W; k0 += 16) {
    int k = k0 + r;
    v4u vv = *(const v4u*)(vb + (size_t)k * DH + c * 8);
    float p = rbf(sc[k] * inv);
    for (int j = 0; j < 4; ++j) { acc[2 * j] += p * __uint_as_float(vv[j] << 16); acc[2 * j + 1] += p * __uint_as_float(vv[j] & 0xffff0000u); }
  }
  for (int j = 0; j < 8; ++j) { acc[j] += __shfl_xor(acc[j], 16); acc[j] += __shfl_xor(acc[j], 32); }
  if (r == 0) for (int j = 0; j < 8; ++j) oacc[wv][c * 8 + j] = acc[j];
  __syncthreads();
  if (t < DH) o[b * 4096 + h * DH + t] = rbf(oacc[0][t] + oacc[1][t] + oacc[2][t] + oacc[3][t]);
}

__global__ void k_resid(const float* x, const float* a, const float* mod, int goff, float* y, u16* yo) {
  int i = blockIdx.x * blockDim.x + threadIdx.x;
  if (i < B * D) { int b = i / D, d = i % D; float r = rbf(x[i] + rbf(mod[b * 3840 + goff + d] * a[i])); if (y) y[i] = r; if (yo) yo[i] = f2bf(r); }
}
__global__ void k_silu(float* a, int n) { int i = blockIdx.x * blockDim.x + threadIdx.x; if (i < n) { float v = a[i]; a[i] = rbf(v / (1.f + __expf(-v))); } }

extern "C" void kernel_launch(void* const* d_in, const int* in_sizes, int n_in,
                              void* d_out, int out_size, void* d_ws, size_t ws_size,
                              hipStream_t stream) {
  const u16* x = (const u16*)d_in[0]; const u16* kc = (const u16*)d_in[1]; const u16* vc = (const u16*)d_in[2];
  const u16* wq = (const u16*)d_in[3]; const u16* wo = (const u16*)d_in[4];
  const u16* w1 = (const u16*)d_in[6]; const u16* w2 = (const u16*)d_in[7];
  const u16* c = (const u16*)d_in[8]; const u16* wm = (const u16*)d_in[9];
  float* ws = (float*)d_ws;
  float* sc = ws; float* mod = ws + 8192; float* xf = mod + 8 * 3840; float* h = xf + 8192; float* q = h + 8192; float* o = q + 32768;
  float* at = o + 32768; float* x1 = at + 8192; float* h2 = x1 + 8192; float* hid = h2 + 8192; float* mo = hid + 16384;
  k_silu_c<<<20, 256, 0, stream>>>(c, sc);
  k_cvt<<<20, 256, 0, stream>>>(x, xf, B * D);
  k_gemv<<<60, 256, 0, stream>>>(sc, wm, mod, 640, 3840);
  k_ln<<<8, 640, 0, stream>>>(xf, mod, 0, 640, h);
  k_gemv<<<64, 256, 0, stream>>>(h, wq, q, 640, 4096);
  k_attn<<<256, 256, 0, stream>>>(q, kc, vc, o);
  k_gemv<<<10, 256, 0, stream>>>(o, wo, at, 4096, 640);
  k_resid<<<20, 256, 0, stream>>>(xf, at, mod, 1280, x1, nullptr);
  k_ln<<<8, 640, 0, stream>>>(x1, mod, 1920, 2560, h2);
  k_gemv<<<27, 256, 0, stream>>>(h2, w1, hid, 640, 1728);
  k_silu<<<54, 256, 0, stream>>>(hid, 8 * 1728);
  k_gemv<<<10, 256, 0, stream>>>(hid, w2, mo, 1728, 640);
  k_resid<<<20, 256, 0, stream>>>(x1, mo, mod, 3200, nullptr, (u16*)d_out);
}
```

```cpp
#include <hip/hip_runtime.h>
#include <hip/hip_bf16.h>
#include <stdint.h>
#include <type_traits>

typedef unsigned v4u __attribute__((ext_vector_type(4)));
typedef unsigned v2u __attribute__((ext_vector_type(2)));
typedef float f4 __attribute__((ext_vector_type(4)));
typedef __bf16 bf8 __attribute__((ext_vector_type(8)));
typedef __bf16 bf2 __attribute__((ext_vector_type(2)));
typedef short s4 __attribute__((ext_vector_type(4)));
#define LAS __attribute__((address_space(3)))
typedef __amdgpu_buffer_rsrc_t rsrc_t;

constexpr int AUX_NT = 2, AUX_SC1 = 16;
constexpr unsigned OOB = 0x80000000u;

constexpr int WS_EPOCH = 0;
constexpr int WS_ALIVE = 4096;
constexpr int WS_AGP = 65536;
constexpr int WS_PM = 262144;
constexpr int WS_AG = 131072;
constexpr int WS_OUT1 = 1 << 20;
constexpr int WS_OUT2 = 2 << 20;
constexpr int WS_STAMP = 4 << 20;
constexpr int WS_BYTES = 8 << 20;

constexpr int L_WL = 0;
constexpr int L_SC = 20480;
constexpr int SC_STRIDE = 336;
constexpr int L_PART = L_SC + 2688;
constexpr int L_ST = L_PART + 4096;
constexpr int L_HB = L_ST + 64;
constexpr int L_SB = L_HB + 1280;
constexpr int L_CB = L_SB + 1280;
constexpr int L_GAB = L_CB + 1280;
constexpr int L_XR = L_GAB + 160;
constexpr int L_DUMMY = L_XR + 64;
constexpr int L_QP = L_DUMMY + 2048;
constexpr int L_OP = L_QP + 4096;
constexpr int L_ML = L_OP + 4096;
constexpr int L_H2 = L_ML + 64;
constexpr int L_HP = L_H2 + 1280;
constexpr int L_YP = L_HP + 4096;
constexpr int L_END = L_YP + 20480;
static_assert(L_HB % 16 == 0 && L_QP % 16 == 0 && L_YP % 16 == 0 && L_OP % 16 == 0 && L_HP % 16 == 0 && L_H2 % 16 == 0, "align");

#define NKB 20
#define RK 8
#define D_RS1 12
#define D_AG 12
#define D_RS2 12
#define POLL_SLEEP 1
#define STAMPS 0
constexpr unsigned SPIN_CAP = 1u << 15;

__device__ __forceinline__ float bf2f(unsigned b16) { return __uint_as_float(b16 << 16); }
__device__ __forceinline__ float bflo(unsigned d) { return __uint_as_float(d << 16); }
__device__ __forceinline__ float bfhi(unsigned d) { return __uint_as_float(d & 0xffff0000u); }
__device__ __forceinline__ unsigned cvtpk(float lo, float hi) {
  bf2 r = {(__bf16)lo, (__bf16)hi}; return __builtin_bit_cast(unsigned, r);
}
__device__ __forceinline__ float rbf(float f) { return bflo(cvtpk(f, 0.f)); }
template <int CTRL> __device__ __forceinline__ float dppf(float x) {
  return __builtin_bit_cast(float, __builtin_amdgcn_update_dpp(0, __builtin_bit_cast(int, x), CTRL, 0xf, 0xf, true));
}
template <int CTRL> __device__ __forceinline__ unsigned dppu(unsigned x) { return (unsigned)__builtin_amdgcn_update_dpp(0, (int)x, CTRL, 0xf, 0xf, true); }
constexpr int XOR1 = 0xB1, XOR2 = 0x4E, XOR7 = 0x141, XOR8 = 0x128, QB0 = 0x00, QB2 = 0xAA;
__device__ __forceinline__ float x16_sum(float x) { auto s = __builtin_amdgcn_permlane16_swap(__float_as_uint(x), __float_as_uint(x), false, false); return __uint_as_float(s[0]) + __uint_as_float(s[1]); }
__device__ __forceinline__ float x32_sum(float x) { auto s = __builtin_amdgcn_permlane32_swap(__float_as_uint(x), __float_as_uint(x), false, false); return __uint_as_float(s[0]) + __uint_as_float(s[1]); }
__device__ __forceinline__ float x16_max(float x) { auto s = __builtin_amdgcn_permlane16_swap(__float_as_uint(x), __float_as_uint(x), false, false); return fmaxf(__uint_as_float(s[0]), __uint_as_float(s[1])); }
__device__ __forceinline__ float x32_max(float x) { auto s = __builtin_amdgcn_permlane32_swap(__float_as_uint(x), __float_as_uint(x), false, false); return fmaxf(__uint_as_float(s[0]), __uint_as_float(s[1])); }
__device__ __forceinline__ float wave_sum(float x) {
  x += dppf<XOR1>(x); x += dppf<XOR2>(x); x += dppf<XOR7>(x); x += dppf<XOR8>(x); return x32_sum(x16_sum(x));
}
__device__ __forceinline__ float wave_max(float x) {
  x = fmaxf(x, dppf<XOR1>(x)); x = fmaxf(x, dppf<XOR2>(x)); x = fmaxf(x, dppf<XOR7>(x)); x = fmaxf(x, dppf<XOR8>(x)); return x32_max(x16_max(x));
}
__device__ __forceinline__ f4 mfma(v4u a, v4u b, f4 c) {
  return __builtin_amdgcn_mfma_f32_16x16x32_bf16(__builtin_bit_cast(bf8, a), __builtin_bit_cast(bf8, b), c, 0, 0, 0);
}
__device__ __forceinline__ float dot2(unsigned a, unsigned b, float c) {
  return __builtin_amdgcn_fdot2_f32_bf16(__builtin_bit_cast(bf2, a), __builtin_bit_cast(bf2, b), c, false);
}
__device__ __forceinline__ rsrc_t mkrs(const void* p, unsigned bytes) { return __builtin_amdgcn_make_buffer_rsrc((void*)p, 0, (int)bytes, 0x00020000); }
__device__ __forceinline__ v4u ldnt(rsrc_t r, unsigned voff, unsigned soff) { return __builtin_amdgcn_raw_buffer_load_b128(r, (int)voff, (int)soff, AUX_NT); }
#define AUX_W 0
#define NOW 0
__device__ __forceinline__ v4u ldw(rsrc_t r, unsigned voff, unsigned soff) { return __builtin_amdgcn_raw_buffer_load_b128(r, (int)(NOW ? OOB : voff), (int)soff, AUX_W); }
__device__ __forceinline__ v4u ldsc(rsrc_t r, unsigned voff) { return __builtin_amdgcn_raw_buffer_load_b128(r, (int)voff, 0, AUX_SC1); }
__device__ __forceinline__ void stsc(rsrc_t r, unsigned voff, v4u v) { __builtin_amdgcn_raw_buffer_store_b128(v, r, (int)voff, 0, AUX_SC1); }
__device__ __forceinline__ void stsc2(rsrc_t r, unsigned voff, v2u v) { __builtin_amdgcn_raw_buffer_store_b64(v, r, (int)voff, 0, AUX_SC1); }
#define WAITVM(n) asm volatile("s_waitcnt vmcnt(%0)" ::"n"(n) : "memory")
#define BAR() asm volatile("s_waitcnt lgkmcnt(0)\n\ts_barrier" ::: "memory")
#define SB() __builtin_amdgcn_sched_barrier(0)
#define FENCE() asm volatile("" ::: "memory")

template <typename T> __device__ __forceinline__ T lds_ld(LAS unsigned char* L, int off) { return *(LAS T*)(L + off); }
template <typename T> __device__ __forceinline__ void lds_st(LAS unsigned char* L, int off, T v) { *(LAS T*)(L + off) = v; }

template <int I, int N, typename F> __device__ __forceinline__ void sfor(F&& f) {
  if constexpr (I < N) { f(std::integral_constant<int, I>{}); sfor<I + 1, N>(f); }
}
#define AINL __attribute__((always_inline))

struct Sel { unsigned s[2][4]; };
__device__ __forceinline__ v4u diagA(unsigned X, const Sel& S, int par) {
  v4u a;
#pragma unroll
  for (int d = 0; d < 4; ++d) a[d] = __builtin_amdgcn_perm(X, X, S.s[par][d]);
  return a;
}
__device__ __forceinline__ v4u tag4(f4 v, unsigned T) {
  v4u r;
#pragma unroll
  for (int e = 0; e < 4; ++e) r[e] = ((__float_as_uint(v[e]) + 0x80u) & 0xffffff00u) | ((T >> (8 * e)) & 0xffu);
  return r;
}
__device__ __forceinline__ bool tagok4(v4u v, unsigned T) {
  const unsigned t = (v.x & 0xffu) | ((v.y & 0xffu) << 8) | ((v.z & 0xffu) << 16) | (v.w << 24);
  return t == T;
}

#define STAMP(n) do { } while (0)

#define KVW 4
constexpr int NPOST = (32 - NKB) + 32 + 40;
struct It { int kind, idx; };
constexpr It wsel(int n) { return n < 20 ? It{2, n} : (n < 30 ? It{3, n - 20} : It{4, n - 30}); }
constexpr It post_item(int m) {
  constexpr int NK = 32 - NKB;
  if (m < 2 * NK) return (m & 1) ? wsel(m >> 1) : It{0, NKB + (m >> 1)};
  const int r = m - 2 * NK, nw = 40 - NK;
  if (r < 2 * nw) return (r & 1) ? wsel(NK + (r >> 1)) : It{1, r >> 1};
  return It{1, r - nw};
}
constexpr int NKV = (32 - NKB) + 32;
constexpr int post_kvpos(int k) { int n = -1; for (int m = 0; m < NPOST; ++m) { if (post_item(m).kind < 2) { ++n; if (n == k) return m; } } return NPOST; }
constexpr int post_kvno(int m) { int n = 0; for (int i = 0; i < m; ++i) if (post_item(i).kind < 2) ++n; return n; }
constexpr int NPRE = 20 + NKB;
constexpr It pre_item(int m) {
  constexpr int NI = (20 < NKB ? 20 : NKB);
  if (m < 2 * NI) return (m & 1) ? It{0, m >> 1} : It{2, m >> 1};
  return (20 > NKB) ? It{2, m - NI} : It{0, m - NI};
}
constexpr int pre_kvpos(int k) { int n = -1; for (int m = 0; m < NPRE; ++m) { if (pre_item(m).kind == 0) { ++n; if (n == k) return m; } } return NPRE; }

__global__ void __launch_bounds__(512, 2)
fused_kernel(const void* __restrict__ px, const void* __restrict__ pk, const void* __restrict__ pv, const void* __restrict__ pwq,
             const void* __restrict__ pwo, const void* __restrict__ pw1, const void* __restrict__ pw2, const void* __restrict__ pc,
             const void* __restrict__ pwm, void* __restrict__ pout, void* __restrict__ pws) {
  __shared__ __attribute__((aligned(16))) unsigned char smem[L_END];
  LAS unsigned char* L = (LAS unsigned char*)smem;
  const int t = threadIdx.x, lane = t & 63, w = __builtin_amdgcn_readfirstlane(t >> 6);
  const int wg = blockIdx.x, xg = wg & 7, e8 = wg >> 3, b = wg >> 5, h = xg * 4 + (e8 & 3);
  const int g = lane >> 4, c = lane & 15;
  const bool mlp = h < 27;
  STAMP(0);
  const rsrc_t rsWS = mkrs(pws, WS_BYTES);
  const rsrc_t rsK = mkrs((const char*)pk + ((size_t)(b * 32 + h) * 4096 + 3072) * 256, 262144);
  const rsrc_t rsV = mkrs((const char*)pv + ((size_t)(b * 32 + h) * 4096 + 3072) * 256, 262144);
  const rsrc_t rsWQ = mkrs(pwq, 640 * 4096 * 2), rsWO = mkrs(pwo, 4096 * 640 * 2), rsW1 = mkrs(pw1, 640 * 1728 * 2), rsW2 = mkrs(pw2, 1728 * 640 * 2);
  const rsrc_t rsWM = mkrs(pwm, 640 * 3840 * 2), rsC = mkrs(pc, 10240), rsX = mkrs(px, 10240), rsOUT = mkrs(pout, 10240);

  const bool role = wg < 240;
  const int ta = wg % 60, kq = wg / 60;
  const unsigned T = *(const __attribute__((address_space(4))) unsigned*)((const char*)pws + WS_EPOCH) + 1u;
  v4u cv0 = ldnt(rsC, (role && t < 160) ? (t / 20) * 1280 + kq * 320 + (t % 20) * 16 : OOB, 0);
  const unsigned wmoff = (unsigned)(160 * kq + 8 * w + (lane >> 3)) * 7680u + ta * 128 + (lane & 7) * 16;
  v4u wm0 = ldnt(rsWM, role ? wmoff : OOB, 0);
  v4u wm1 = ldnt(rsWM, role ? wmoff : OOB, 64 * 7680);
  v4u wm2 = ldnt(rsWM, (role && w < 4) ? wmoff : OOB, 128 * 7680);
  v4u xa = ldnt(rsX, w == 6 ? b * 1280 + lane * 16 : OOB, 0);
  unsigned xb = __builtin_amdgcn_raw_buffer_load_b32(rsX, w == 6 ? b * 1280 + 1024 + lane * 4 : OOB, 0, AUX_NT);
  v2u xown = __builtin_amdgcn_raw_buffer_load_b64(rsX, (w == 6 && lane < 5) ? b * 1280 + 40 * h + lane * 8 : OOB, 0, AUX_NT);
  SB();
  v4u WB[20], KB[NKB];
  const unsigned offWQ = (unsigned)(80 * w + 20 * g) * 8192u + h * 256 + c * 16;
  const unsigned offKV = w * 1024 + lane * 16;
  auto pre_issue = [&](auto mc) AINL {
    constexpr It it = pre_item(decltype(mc)::value);
    if constexpr (it.kind == 0) KB[it.idx] = ldnt(rsK, offKV, it.idx * 8192); else WB[it.idx] = ldw(rsWQ, offWQ, it.idx * 8192);
  };
  constexpr int NINIT = pre_kvpos(KVW + 1);
  sfor<0, NINIT>(pre_issue);
  SB();
  STAMP(1);
  {
    auto silu8 = [&](v4u v) AINL {
      v4u r;
#pragma unroll
      for (int d = 0; d < 4; ++d) { const float a0 = bflo(v[d]), a1 = bfhi(v[d]); r[d] = cvtpk(a0 / (1.f + __expf(-a0)), a1 / (1.f + __expf(-a1))); }
      return r;
    };
    if (t < 160) lds_st<v4u>(L, L_SC + (t / 20) * SC_STRIDE + (t % 20) * 16, silu8(cv0));
    lds_st<v4u>(L, L_WL + w * 1024 + lane * 16, wm0);
    lds_st<v4u>(L, L_WL + (w + 8) * 1024 + lane * 16, wm1);
    if (w < 4) lds_st<v4u>(L, L_WL + (w + 16) * 1024 + lane * 16, wm2);
    if (w == 6) {
      if (lane < 5) lds_st<v2u>(L, L_XR + lane * 8, xown);
      float xv[10];
#pragma unroll
      for (int d = 0; d < 4; ++d) { xv[2 * d] = bflo(xa[d]); xv[2 * d + 1] = bfhi(xa[d]); }
      xv[8] = bflo(xb); xv[9] = bfhi(xb);
      float s = 0.f;
#pragma unroll
      for (int d = 0; d < 10; ++d) s += xv[d];
      const float mean = wave_sum(s) * (1.f / 640.f);
      float q2 = 0.f;
#pragma unroll
      for (int d = 0; d < 10; ++d) { const float dd = xv[d] - mean; q2 += dd * dd; }
      const float var = rbf(wave_sum(q2) * (1.f / 640.f));
      if (lane == 0) lds_st<v2u>(L, L_ST, v2u{__float_as_uint(rbf(mean)), __float_as_uint(rbf(sqrtf(rbf(var + 1e-5f))))});
    }
  }
  BAR();
  STAMP(2);
  {
    f4 acc = {0.f, 0.f, 0.f, 0.f};
    const int tl = w & 3, s0_ = (w >> 2) * 3;
    const int trbase = L_WL + (8 * g + ((lane & 15) >> 2)) * 128 + tl * 32 + (lane & 3) * 8;
#pragma unroll
    for (int n = 0; n < 3; ++n) {
      if (n < 2 || w < 4) {
        const int s = s0_ + n;
        const v4u a = lds_ld<v4u>(L, L_SC + (c & 7) * SC_STRIDE + s * 64 + g * 16);
        const s4 b0 = __builtin_amdgcn_ds_read_tr16_b64_v4i16((LAS s4*)(L + trbase + s * 4096));
        const s4 b1 = __builtin_amdgcn_ds_read_tr16_b64_v4i16((LAS s4*)(L + trbase + s * 4096 + 512));
        const v2u u0 = __builtin_bit_cast(v2u, b0), u1 = __builtin_bit_cast(v2u, b1);
        acc = mfma(a, v4u{u0.x, u0.y, u1.x, u1.y}, acc);
      }
    }
    if (g < 2) {
#pragma unroll
      for (int r = 0; r < 4; ++r) lds_st<float>(L, L_PART + (w >> 2) * 2048 + (4 * g + r) * 256 + (16 * tl + c) * 4, acc[r]);
    }
  }
  BAR();
  if (t < 128 && role) {
    const f4 s = lds_ld<f4>(L, L_PART + t * 16) + lds_ld<f4>(L, L_PART + 2048 + t * 16);
    stsc(rsWS, WS_PM + (((t >> 4) * 4 + kq) * 3840 + ta * 64) * 4 + (t & 15) * 16, tag4(s, T));
  }
  {
    const v4u clr = tag4(f4{0.f, 0.f, 0.f, 0.f}, T - 1u);
    if (t < 256) stsc(rsWS, WS_OUT1 + wg * 4096 + t * 16, clr); else stsc(rsWS, WS_OUT2 + wg * 4096 + (t - 256) * 16, clr);
    if (w == 5) {
      if (lane < 8) stsc(rsWS, WS_AG + (b * 32 + h) * 128 + lane * 16, clr);
      else if (lane < 24) stsc(rsWS, WS_AGP + (b * 32 + h) * 256 + (lane - 8) * 16, clr);
      else if (lane == 24) stsc2(rsWS, WS_ALIVE + wg * 8, v2u{T, T});
    }
  }
  STAMP(3);
  unsigned roff[4]; v4u rv[4];
  {
    const int sec = lane / 5, k5 = lane % 5;
#pragma unroll
    for (int q = 0; q < 4; ++q) roff[q] = (w == 5 && lane < 30) ? WS_PM + ((b * 4 + q) * 3840 + sec * 640 + 20 * h + 4 * k5) * 4 : OOB;
  }
  unsigned goff[3]; int gdst;
  {
    gdst = w == 0 ? L_HB : (w == 1 ? L_SB : (w == 2 ? L_CB : L_DUMMY));
#pragma unroll
    for (int n = 0; n < 3; ++n) {
      const int idx = 64 * n + lane;
      goff[n] = (w < 3 && (w == 0 || mlp) && idx < 160) ? WS_AGP + (b * 32 + idx / 5) * 256 + (5 * w + idx % 5) * 16 : OOB;
      if (w == 4 && wg == 0 && idx < 128) goff[n] = WS_ALIVE + idx * 16;
    }
  }
  v4u gv[3];
  constexpr int RPOS = pre_kvpos(RK) > NINIT ? pre_kvpos(RK) : NINIT;
  constexpr int RCHK = pre_kvpos(RK + KVW + 1);
  constexpr int GPOS = pre_kvpos(NKB - KVW - 1);
  static_assert(RCHK < GPOS, "stage R before stage G");
  sfor<NINIT, NPRE>([&](auto mc) AINL {
    constexpr int m = decltype(mc)::value;
    constexpr It it = pre_item(m);
    if constexpr (it.kind == 0) {
      constexpr int o = pre_kvpos(it.idx - KVW);
      WAITVM(m - 1 - o + (m > GPOS && o <= GPOS ? 3 : 0) + (m > RPOS && o <= RPOS ? 4 : 0));
    }
    pre_issue(mc);
    if constexpr (m == RPOS) {
#pragma unroll
      for (int q = 0; q < 4; ++q) rv[q] = ldsc(rsWS, roff[q]);
    }
    if constexpr (m == RCHK) {
      if (w == 5) {
        unsigned spins = 0;
        for (;;) {
          bool ok = true;
#pragma unroll
          for (int q = 0; q < 4; ++q) ok = ok && (roff[q] == OOB || tagok4(rv[q], T));
          if (__all(ok)) break;
          if (++spins > SPIN_CAP) break;
          __builtin_amdgcn_s_sleep(2);
          FENCE();
#pragma unroll
          for (int q = 0; q < 4; ++q) { const bool k1 = tagok4(rv[q], T); const v4u nv = ldsc(rsWS, k1 ? OOB : roff[q]); rv[q] = k1 ? rv[q] : nv; }
        }
        STAMP(14);
        f4 mv;
#pragma unroll
        for (int e = 0; e < 4; ++e) mv[e] = rbf((__uint_as_float(rv[0][e] & 0xffffff00u) + __uint_as_float(rv[1][e] & 0xffffff00u)) +
                                                (__uint_as_float(rv[2][e] & 0xffffff00u) + __uint_as_float(rv[3][e] & 0xffffff00u)));
        const v2u xr = lds_ld<v2u>(L, L_XR + (lane % 5) * 8), stt = lds_ld<v2u>(L, L_ST);
        const float xx[4] = {bflo(xr.x), bfhi(xr.x), bflo(xr.y), bfhi(xr.y)};
        v4u msg;
#pragma unroll
        for (int e = 0; e < 4; ++e) {
          const float ca = __uint_as_float(__builtin_amdgcn_ds_bpermute(((lane + 5) & 63) * 4, (int)__float_as_uint(mv[e])));
          const float ln = rbf(rbf(xx[e] - __uint_as_float(stt.x)) / __uint_as_float(stt.y));
          const float hv = rbf(rbf(ln * rbf(1.f + ca)) + mv[e]);
          const float o = lane < 5 ? hv : (lane >= 20 ? rbf(1.f + mv[e]) : mv[e]);
          msg[e] = (__float_as_uint(o) & 0xffff0000u) | ((T >> (8 * e)) & 0xffu);
        }
        if (lane < 5) stsc(rsWS, WS_AGP + (b * 32 + h) * 256 + lane * 16, msg);
        else if (lane >= 15 && lane < 25) stsc(rsWS, WS_AGP + (b * 32 + h) * 256 + (lane - 10) * 16, msg);
        else if (lane >= 10 && lane < 15) lds_st<f4>(L, L_GAB + (lane - 10) * 16, mv);
        else if (lane >= 25 && lane < 30) lds_st<f4>(L, L_GAB + 80 + (lane - 25) * 16, mv);
      }
    }
    if constexpr (m == GPOS) {
#pragma unroll
      for (int n = 0; n < 3; ++n) gv[n] = ldsc(rsWS, goff[n]);
    }
    SB();
  });
  STAMP(4);
  {
    const bool alive = w == 4;
    unsigned spins = 0;
    for (;;) {
      bool ok = true;
#pragma unroll
      for (int n = 0; n < 3; ++n) ok = ok && (goff[n] == OOB || (alive ? (gv[n].y == T && gv[n].w == T) : tagok4(gv[n], T)));
      if (__all(ok)) break;
      if (++spins > SPIN_CAP) break;
      __builtin_amdgcn_s_sleep(2);
      FENCE();
#pragma unroll
      for (int n = 0; n < 3; ++n) gv[n] = ldsc(rsWS, goff[n]);
    }
#pragma unroll
    for (int n = 0; n < 3; ++n) {
      const int idx = 64 * n + lane;
      if (goff[n] != OOB && !alive) lds_st<v2u>(L, gdst + (20 * (idx / 5) + 4 * (idx % 5)) * 2,
                                                v2u{__builtin_amdgcn_perm(gv[n].y, gv[n].x, 0x07060302u), __builtin_amdgcn_perm(gv[n].w, gv[n].z, 0x07060302u)});
    }
    if (alive && wg == 0 && lane == 0) __builtin_amdgcn_raw_buffer_store_b32(T, rsWS, WS_EPOCH, 0, AUX_SC1);
  }
  BAR();
  STAMP(5);
  Sel S;
  {
    const int e = c & 7;
#pragma unroll
    for (int par = 0; par < 2; ++par)
#pragma unroll
      for (int d = 0; d < 4; ++d) {
        const unsigned lo = (unsigned)(2 * par) | ((unsigned)(2 * par + 1) << 8);
        S.s[par][d] = (d == (e >> 1)) ? ((e & 1) ? (0x0c0cu | (lo << 16)) : (0x0c0c0000u | lo)) : 0x0c0c0c0cu;
      }
  }
  unsigned qr[4];
  {
    unsigned X[10];
#pragma unroll
    for (int d = 0; d < 5; ++d) { const v2u u = lds_ld<v2u>(L, L_HB + 160 * w + 40 * g + d * 8); X[2 * d] = u.x; X[2 * d + 1] = u.y; }
    f4 a0 = {0.f, 0.f, 0.f, 0.f}, a1 = {0.f, 0.f, 0.f, 0.f};
#pragma unroll
    for (int p = 0; p < 20; p += 2) { a0 = mfma(diagA(X[p >> 1], S, 0), WB[p], a0); a1 = mfma(diagA(X[p >> 1], S, 1), WB[p + 1], a1); }
    a0 += a1;
    if (lane < 32) lds_st<f4>(L, L_QP + w * 512 + (8 * c + 4 * g) * 4, a0);
    BAR();
    float q0 = 0.f, q1 = 0.f;
#pragma unroll
    for (int ww = 0; ww < 8; ++ww) { const v2u u = lds_ld<v2u>(L, L_QP + ww * 512 + lane * 8); q0 += __uint_as_float(u.x); q1 += __uint_as_float(u.y); }
    const unsigned qp = cvtpk(q0, q1);
#pragma unroll
    for (int d = 0; d < 4; ++d) qr[d] = (unsigned)__builtin_amdgcn_ds_bpermute((4 * c + d) * 4, (int)qp);
  }
  STAMP(6);
  float xs[32];
  auto kdot = [&](v4u k) __attribute__((always_inline)) { return dot2(k.w, qr[3], dot2(k.z, qr[2], dot2(k.y, qr[1], dot2(k.x, qr[0], 0.f)))); };
  v4u WO[20], W1[10], W2[10], KR[32 - NKB > 0 ? 32 - NKB : 1], VR[KVW + 1];
  const unsigned offWO = (unsigned)(h * 128 + 16 * w + 4 * g) * 1280u + c * 16;
  const unsigned offW1 = mlp ? (unsigned)(80 * w + 40 * (c >> 3) + 10 * g) * 3456u + (64 * h + 8 * (c & 7)) * 2 : OOB;
  const unsigned offW2 = mlp ? (unsigned)(64 * h + 8 * w + 2 * g) * 1280u + c * 16 : OOB;
  auto post_issue = [&](auto mc) AINL {
    constexpr It it = post_item(decltype(mc)::value);
    if constexpr (it.kind == 0) KR[it.idx - NKB] = ldnt(rsK, offKV, it.idx * 8192);
    else if constexpr (it.kind == 1) VR[it.idx % (KVW + 1)] = ldnt(rsV, offKV, it.idx * 8192);
    else if constexpr (it.kind == 2) WO[it.idx] = ldw(rsWO, offWO, (it.idx / 5) * 1280 + (it.idx % 5) * 256);
    else if constexpr (it.kind == 3) W1[it.idx] = ldw(rsW1, offW1, it.idx * 3456);
    else W2[it.idx] = ldw(rsW2, offW2, (it.idx / 5) * 1280 + (it.idx % 5) * 256);
  };
  auto treduce = [&](auto bc) AINL {
    constexpr int B0 = decltype(bc)::value;
    float x[8];
#pragma unroll
    for (int i = 0; i < 8; ++i) { const float lo = xs[B0 + i] + dppf<XOR8>(xs[B0 + i]), hi = xs[B0 + i + 8] + dppf<XOR8>(xs[B0 + i + 8]); x[i] = (c & 8) ? hi : lo; }
#pragma unroll
    for (int i = 0; i < 4; ++i) { const float lo = x[i] + dppf<XOR7>(x[i]), hi = x[i + 4] + dppf<XOR7>(x[i + 4]); x[i] = (c & 4) ? hi : lo; }
#pragma unroll
    for (int i = 0; i < 2; ++i) { const float lo = x[i] + dppf<XOR2>(x[i]), hi = x[i + 2] + dppf<XOR2>(x[i + 2]); x[i] = (c & 2) ? hi : lo; }
    const float lo = x[0] + dppf<XOR1>(x[0]), hi = x[1] + dppf<XOR1>(x[1]);
    return (c & 1) ? hi : lo;
  };
  constexpr int PINIT = post_kvpos(KVW);
  sfor<0, PINIT>(post_issue);
  SB();
#pragma unroll
  for (int j = 0; j < NKB; ++j) xs[j] = kdot(KB[j]);
  float s0 = treduce(std::integral_constant<int, 0>{});
  SB();
  constexpr float C2 = 0.08838834764831845f * 1.4426950408889634f;
  float s1 = 0.f, mw = 0.f, fw = 0.f;
  unsigned ppk = 0;
  f4 oacc = {0.f, 0.f, 0.f, 0.f};
  auto process = [&](auto kc) AINL {
    constexpr It it = post_item(post_kvpos(decltype(kc)::value));
    if constexpr (it.kind == 0) {
      xs[it.idx] = kdot(KR[it.idx - NKB]);
      if constexpr (it.idx == 31) {
        s1 = treduce(std::integral_constant<int, 16>{});
        mw = wave_max(fmaxf(s0, s1));
        const float p0 = __builtin_amdgcn_exp2f((s0 - mw) * C2), p1 = __builtin_amdgcn_exp2f((s1 - mw) * C2);
        const float lw = wave_sum(p0 + p1);
        ppk = cvtpk(p0, p1);
        if (lane == 0) lds_st<v2u>(L, L_ML + w * 8, v2u{__float_as_uint(mw), __float_as_uint(lw)});
      }
    } else {
      const unsigned X = dppu<0x150 + (it.idx & 15)>(ppk);
      oacc = mfma(diagA(X, S, it.idx >> 4), VR[it.idx % (KVW + 1)], oacc);
      if constexpr (it.idx == 8) {
        BAR();
        float M = -INFINITY;
        v2u ml[8];
#pragma unroll
        for (int ww = 0; ww < 8; ++ww) { ml[ww] = lds_ld<v2u>(L, L_ML + ww * 8); M = fmaxf(M, __uint_as_float(ml[ww].x)); }
        float Ls = 0.f;
#pragma unroll
        for (int ww = 0; ww < 8; ++ww) Ls += __builtin_amdgcn_exp2f((__uint_as_float(ml[ww].x) - M) * C2) * __uint_as_float(ml[ww].y);
        fw = __builtin_amdgcn_exp2f((mw - M) * C2) / Ls;
      }
    }
  };
  sfor<PINIT, NPOST>([&](auto mc) AINL {
    constexpr int m = decltype(mc)::value;
    constexpr It it = post_item(m);
    if constexpr (it.kind < 2) {
      constexpr int k = post_kvno(m);
      WAITVM(m - 1 - post_kvpos(k - KVW));
      post_issue(mc);
      process(std::integral_constant<int, k - KVW>{});
    } else post_issue(mc);
    SB();
  });
  sfor<NKV - KVW, NKV>([&](auto kc) AINL { process(kc); SB(); });
  STAMP(7);
  if (lane < 32) lds_st<f4>(L, L_OP + w * 512 + (8 * c + 4 * g) * 4, oacc * fw);
  BAR();
  {
    const int kap = (c >> 1) & 3, sig = (c & 1) + 2 * (c >> 3);
    const int ko = (16 * w + 4 * g + kap) * 4;
    float o = lds_ld<float>(L, L_OP + (2 * sig) * 512 + ko) + lds_ld<float>(L, L_OP + (2 * sig + 1) * 512 + ko);
    o += dppf<XOR1>(o); o += dppf<XOR8>(o);
    const unsigned P = cvtpk(dppf<QB0>(o), dppf<QB2>(o)), Q = dppu<XOR7>(P);
    const unsigned X0 = (c & 4) ? Q : P, X1 = (c & 4) ? P : Q;
    const v4u A0 = diagA(X0, S, 0), A1 = diagA(X0, S, 1), A2 = diagA(X1, S, 0), A3 = diagA(X1, S, 1);
#pragma unroll
    for (int cb = 0; cb < 5; ++cb) {
      f4 y = {0.f, 0.f, 0.f, 0.f};
      y = mfma(A0, WO[cb], y); y = mfma(A1, WO[5 + cb], y); y = mfma(A2, WO[10 + cb], y); y = mfma(A3, WO[15 + cb], y);
      if (lane < 32) lds_st<f4>(L, L_YP + w * 2560 + (128 * cb + 8 * c + 4 * g) * 4, y);
    }
  }
  BAR();
  STAMP(8);
  auto publish = [&](int wsoff) __attribute__((always_inline)) {
    const int k = t & 7, j = t >> 3;
    f4 s = {0.f, 0.f, 0.f, 0.f};
    if (k < 5) {
#pragma unroll
      for (int ww = 0; ww < 8; ++ww) s += lds_ld<f4>(L, L_YP + ww * 2560 + (20 * j + 4 * k) * 4);
    }
    stsc(rsWS, wsoff + t * 16, tag4(s, T));
  };
  float x1v[4] = {0.f, 0.f, 0.f, 0.f};
  auto rs_gather = [&](int wsbase, int nprod) __attribute__((always_inline)) -> f4 {
    const int pl = lane >> 3, k = lane & 7;
    unsigned off[4]; v4u v[4];
#pragma unroll
    for (int q = 0; q < 4; ++q) {
      const int u = 8 * q + pl;
      const int wgu = (b << 5) | ((u & 3) << 3) | (u >> 2);
      off[q] = (k < 5 && u < nprod) ? wsbase + wgu * 4096 + h * 128 + k * 16 : OOB;
      v[q] = ldsc(rsWS, off[q]);
    }
    unsigned spins = 0;
    for (;;) {
      bool all = true;
#pragma unroll
      for (int q = 0; q < 4; ++q) all = all && (off[q] == OOB || tagok4(v[q], T));
      if (__all(all)) break;
      if (++spins > SPIN_CAP) break;
      __builtin_amdgcn_s_sleep(POLL_SLEEP);
      FENCE();
#pragma unroll
      for (int q = 0; q < 4; ++q) { const bool ok = tagok4(v[q], T); const v4u nv = ldsc(rsWS, ok ? OOB : off[q]); v[q] = ok ? v[q] : nv; }
    }
    f4 s = {0.f, 0.f, 0.f, 0.f};
#pragma unroll
    for (int q = 0; q < 4; ++q)
#pragma unroll
      for (int e = 0; e < 4; ++e) s[e] += off[q] == OOB ? 0.f : __uint_as_float(v[q][e] & 0xffffff00u);
#pragma unroll
    for (int e = 0; e < 4; ++e) { float a = s[e]; a += dppf<XOR8>(a); s[e] = x32_sum(x16_sum(a)); }
    return s;
  };
  if (w < 4) publish(WS_OUT1 + wg * 4096);
  else if (w == 4) {
    const v2u xr = lds_ld<v2u>(L, L_XR + (lane & 7) * 8); const f4 gg = lds_ld<f4>(L, L_GAB + (lane & 7) * 16);
    unsigned off[3]; v4u v[3]; v2u sbv[3], cbv[3];
#pragma unroll
    for (int q = 0; q < 3; ++q) {
      const int idx = 64 * q + lane, jj = idx / 5, k = idx % 5;
      off[q] = (idx < 160 && mlp) ? WS_AG + (b * 32 + jj) * 128 + k * 16 : OOB;
      const int col = idx < 160 ? 20 * jj + 4 * k : 0;
      sbv[q] = lds_ld<v2u>(L, L_SB + col * 2); cbv[q] = lds_ld<v2u>(L, L_CB + col * 2);
    }
    __builtin_amdgcn_s_sleep(D_RS1);
    const f4 s = rs_gather(WS_OUT1, 32);
    const float xx[4] = {bflo(xr.x), bfhi(xr.x), bflo(xr.y), bfhi(xr.y)};
    v4u m;
#pragma unroll
    for (int e = 0; e < 4; ++e) { x1v[e] = rbf(xx[e] + rbf(gg[e] * rbf(s[e]))); m[e] = __float_as_uint(x1v[e]) | ((T >> (8 * e)) & 0xffu); }
    if (lane < 5) stsc(rsWS, WS_AG + (b * 32 + h) * 128 + lane * 16, m);
    STAMP(9);
    if (mlp) {
      __builtin_amdgcn_s_sleep(D_AG);
#pragma unroll
      for (int q = 0; q < 3; ++q) v[q] = ldsc(rsWS, off[q]);
      unsigned spins = 0;
      for (;;) {
        bool all = true;
#pragma unroll
        for (int q = 0; q < 3; ++q) all = all && (off[q] == OOB || tagok4(v[q], T));
        if (__all(all)) break;
        if (++spins > SPIN_CAP) break;
        __builtin_amdgcn_s_sleep(POLL_SLEEP);
        FENCE();
#pragma unroll
        for (int q = 0; q < 3; ++q) { const bool ok = tagok4(v[q], T); const v4u nv = ldsc(rsWS, ok ? OOB : off[q]); v[q] = ok ? v[q] : nv; }
      }
      STAMP(13);
      float xv[12]; float sm = 0.f;
#pragma unroll
      for (int q = 0; q < 3; ++q)
#pragma unroll
        for (int e = 0; e < 4; ++e) { xv[4 * q + e] = off[q] == OOB ? 0.f : __uint_as_float(v[q][e] & 0xffff0000u); sm += xv[4 * q + e]; }
      const float mean = wave_sum(sm) * (1.f / 640.f);
      float q2 = 0.f;
#pragma unroll
      for (int q = 0; q < 3; ++q)
#pragma unroll
        for (int e = 0; e < 4; ++e) { const float dd = off[q] == OOB ? 0.f : xv[4 * q + e] - mean; q2 += dd * dd; }
      const float var = rbf(wave_sum(q2) * (1.f / 640.f));
      const float mb = rbf(mean), rden = 1.f / rbf(sqrtf(rbf(var + 1e-5f)));
#pragma unroll
      for (int q = 0; q < 3; ++q) {
        const float sb4[4] = {bflo(sbv[q].x), bfhi(sbv[q].x), bflo(sbv[q].y), bfhi(sbv[q].y)}, cb4[4] = {bflo(cbv[q].x), bfhi(cbv[q].x), bflo(cbv[q].y), bfhi(cbv[q].y)};
        float hv[4];
#pragma unroll
        for (int e = 0; e < 4; ++e) hv[e] = rbf(rbf(rbf(xv[4 * q + e] - mb) * rden) * cb4[e]) + sb4[e];
        const int idx = 64 * q + lane;
        if (idx < 160) lds_st<v2u>(L, L_H2 + (20 * (idx / 5) + 4 * (idx % 5)) * 2, v2u{cvtpk(hv[0], hv[1]), cvtpk(hv[2], hv[3])});
      }
    }
  }
  if (mlp) {
    BAR();
    STAMP(10);
    {
      unsigned X[5];
#pragma unroll
      for (int d = 0; d < 5; ++d) X[d] = lds_ld<unsigned>(L, L_H2 + 160 * w + 80 * (c >> 3) + 20 * g + d * 4);
      f4 a0 = {0.f, 0.f, 0.f, 0.f}, a1 = {0.f, 0.f, 0.f, 0.f};
#pragma unroll
      for (int p = 0; p < 10; p += 2) { a0 = mfma(diagA(X[p >> 1], S, 0), W1[p], a0); a1 = mfma(diagA(X[p >> 1], S, 1), W1[p + 1], a1); }
      a0 += a1;
      if ((g >> 1) == (c >> 3)) lds_st<f4>(L, L_HP + (2 * w + (c >> 3)) * 256 + (8 * (c & 7) + 4 * (g & 1)) * 4, a0);
    }
    BAR();
    {
      const int kap = (c >> 2) & 1, sig = (c & 3) + 4 * (c >> 3);
      const int n = (8 * w + 2 * g + kap) * 4;
      float hd = lds_ld<float>(L, L_HP + (2 * sig) * 256 + n) + lds_ld<float>(L, L_HP + (2 * sig + 1) * 256 + n);
      hd += dppf<XOR1>(hd); hd += dppf<XOR2>(hd); hd += dppf<XOR8>(hd);
      hd = rbf(hd);
      hd = hd / (1.f + __expf(-hd));
      const float ot = dppf<XOR7>(hd);
      const unsigned X = kap ? cvtpk(ot, hd) : cvtpk(hd, ot);
      const v4u A0 = diagA(X, S, 0), A1 = diagA(X, S, 1);
#pragma unroll
      for (int cb = 0; cb < 5; ++cb) {
        f4 y = {0.f, 0.f, 0.f, 0.f};
        y = mfma(A0, W2[cb], y); y = mfma(A1, W2[5 + cb], y);
        if (lane < 32) lds_st<f4>(L, L_YP + w * 2560 + (128 * cb + 8 * c + 4 * g) * 4, y);
      }
    }
    BAR();
    STAMP(11);
    if (w < 4) publish(WS_OUT2 + wg * 4096);
  }
  if (w == 4) {
    const f4 gg = lds_ld<f4>(L, L_GAB + 80 + (lane & 7) * 16);
    if (!mlp) __builtin_amdgcn_s_sleep(48);
    __builtin_amdgcn_s_sleep(D_RS2);
    const f4 s = rs_gather(WS_OUT2, 27);
    float o[4];
#pragma unroll
    for (int e = 0; e < 4; ++e) o[e] = x1v[e] + rbf(gg[e] * rbf(s[e]));
    if (lane < 5) __builtin_amdgcn_raw_buffer_store_b64(v2u{cvtpk(o[0], o[1]), cvtpk(o[2], o[3])}, rsOUT, b * 1280 + 40 * h + lane * 8, 0, 0);
    STAMP(12);
  }
}

extern "C" void kernel_launch(void* const* d_in, const int* in_sizes, int n_in, void* d_out, int out_size, void* d_ws, size_t ws_size,
                              hipStream_t stream) {
  fused_kernel<<<256, 512, 0, stream>>>(d_in[0], d_in[1], d_in[2], d_in[3], d_in[4], d_in[6], d_in[7], d_in[8], d_in[9], d_out, d_ws);
}
```

```cpp
#include <hip/hip_runtime.h>
#include <hip/hip_bf16.h>
#include <stdint.h>
#include <type_traits>

typedef unsigned v4u __attribute__((ext_vector_type(4)));
typedef unsigned v2u __attribute__((ext_vector_type(2)));
typedef float f4 __attribute__((ext_vector_type(4)));
typedef __bf16 bf8 __attribute__((ext_vector_type(8)));
typedef __bf16 bf2 __attribute__((ext_vector_type(2)));
typedef short s4 __attribute__((ext_vector_type(4)));
#define LAS __attribute__((address_space(3)))
typedef __amdgpu_buffer_rsrc_t rsrc_t;

constexpr int AUX_NT = 2, AUX_SC1 = 16;
constexpr unsigned OOB = 0x80000000u;

constexpr int WS_EPOCH = 0;
constexpr int WS_ALIVE = 4096;
constexpr int WS_AGP = 65536;
constexpr int WS_PM = 262144;
constexpr int WS_AG = 131072;
constexpr int WS_OUT1 = 1 << 20;
constexpr int WS_OUT2 = 2 << 20;
constexpr int WS_STAMP = 4 << 20;
constexpr int WS_BYTES = 8 << 20;

constexpr int L_WL = 0;
constexpr int L_SC = 20480;
constexpr int SC_STRIDE = 336;
constexpr int L_PART = L_SC + 2688;
constexpr int L_ST = L_PART + 4096;
constexpr int L_HB = L_ST + 64;
constexpr int L_SB = L_HB + 1280;
constexpr int L_CB = L_SB + 1280;
constexpr int L_GAB = L_CB + 1280;
constexpr int L_XR = L_GAB + 160;
constexpr int L_DUMMY = L_XR + 64;
constexpr int L_QP = L_DUMMY + 2048;
constexpr int L_OP = L_QP + 4096;
constexpr int L_ML = L_OP + 4096;
constexpr int L_H2 = L_ML + 64;
constexpr int L_HP = L_H2 + 1280;
constexpr int L_YP = L_HP + 4096;
constexpr int L_END = L_YP + 20480;
static_assert(L_HB % 16 == 0 && L_QP % 16 == 0 && L_YP % 16 == 0 && L_OP % 16 == 0 && L_HP % 16 == 0 && L_H2 % 16 == 0, "align");

#define NKB 20
#define RK 6
#define RD1 4
#define RD2 7
#define GK 15
#define D_RS1 12
#define D_AG 12
#define D_RS2 12
#define POLL_SLEEP 1
#define STAMPS 0
constexpr unsigned SPIN_CAP = 1u << 15;

__device__ __forceinline__ float bf2f(unsigned b16) { return __uint_as_float(b16 << 16); }
__device__ __forceinline__ float bflo(unsigned d) { return __uint_as_float(d << 16); }
__device__ __forceinline__ float bfhi(unsigned d) { return __uint_as_float(d & 0xffff0000u); }
__device__ __forceinline__ unsigned cvtpk(float lo, float hi) {
  bf2 r = {(__bf16)lo, (__bf16)hi}; return __builtin_bit_cast(unsigned, r);
}
__device__ __forceinline__ float rbf(float f) { return bflo(cvtpk(f, 0.f)); }
__device__ __forceinline__ void rbf2(float& a, float& b) { const unsigned p = cvtpk(a, b); a = bflo(p); b = bfhi(p); }
__device__ __forceinline__ float frcp(float x) { return __builtin_amdgcn_rcpf(x); }
__device__ __forceinline__ float fsilu(float a) { return a * frcp(1.f + __builtin_amdgcn_exp2f(a * -1.4426950408889634f)); }
template <int CTRL> __device__ __forceinline__ float dppf(float x) {
  return __builtin_bit_cast(float, __builtin_amdgcn_update_dpp(0, __builtin_bit_cast(int, x), CTRL, 0xf, 0xf, true));
}
template <int CTRL> __device__ __forceinline__ unsigned dppu(unsigned x) { return (unsigned)__builtin_amdgcn_update_dpp(0, (int)x, CTRL, 0xf, 0xf, true); }
constexpr int XOR1 = 0xB1, XOR2 = 0x4E, XOR7 = 0x141, XOR8 = 0x128, QB0 = 0x00, QB2 = 0xAA;
__device__ __forceinline__ float x16_sum(float x) { auto s = __builtin_amdgcn_permlane16_swap(__float_as_uint(x), __float_as_uint(x), false, false); return __uint_as_float(s[0]) + __uint_as_float(s[1]); }
__device__ __forceinline__ float x32_sum(float x) { auto s = __builtin_amdgcn_permlane32_swap(__float_as_uint(x), __float_as_uint(x), false, false); return __uint_as_float(s[0]) + __uint_as_float(s[1]); }
__device__ __forceinline__ float x16_max(float x) { auto s = __builtin_amdgcn_permlane16_swap(__float_as_uint(x), __float_as_uint(x), false, false); return fmaxf(__uint_as_float(s[0]), __uint_as_float(s[1])); }
__device__ __forceinline__ float x32_max(float x) { auto s = __builtin_amdgcn_permlane32_swap(__float_as_uint(x), __float_as_uint(x), false, false); return fmaxf(__uint_as_float(s[0]), __uint_as_float(s[1])); }
__device__ __forceinline__ float wave_sum(float x) {
  x += dppf<XOR1>(x); x += dppf<XOR2>(x); x += dppf<XOR7>(x); x += dppf<XOR8>(x); return x32_sum(x16_sum(x));
}
__device__ __forceinline__ float wave_max(float x) {
  x = fmaxf(x, dppf<XOR1>(x)); x = fmaxf(x, dppf<XOR2>(x)); x = fmaxf(x, dppf<XOR7>(x)); x = fmaxf(x, dppf<XOR8>(x)); return x32_max(x16_max(x));
}
__device__ __forceinline__ f4 mfma(v4u a, v4u b, f4 c) {
  return __builtin_amdgcn_mfma_f32_16x16x32_bf16(__builtin_bit_cast(bf8, a), __builtin_bit_cast(bf8, b), c, 0, 0, 0);
}
__device__ __forceinline__ float dot2(unsigned a, unsigned b, float c) {
  return __builtin_amdgcn_fdot2_f32_bf16(__builtin_bit_cast(bf2, a), __builtin_bit_cast(bf2, b), c, false);
}
__device__ __forceinline__ rsrc_t mkrs(const void* p, unsigned bytes) { return __builtin_amdgcn_make_buffer_rsrc((void*)p, 0, (int)bytes, 0x00020000); }
__device__ __forceinline__ v4u ldnt(rsrc_t r, unsigned voff, unsigned soff) { return __builtin_amdgcn_raw_buffer_load_b128(r, (int)voff, (int)soff, AUX_NT); }
#define AUX_K 2
#define AUX_V 2
#define AUX_W 0
#define NOW 0
__device__ __forceinline__ v4u ldw(rsrc_t r, unsigned voff, unsigned soff) { return __builtin_amdgcn_raw_buffer_load_b128(r, (int)(NOW ? OOB : voff), (int)soff, AUX_W); }
__device__ __forceinline__ v4u ldk(rsrc_t r, unsigned voff, unsigned soff) { return __builtin_amdgcn_raw_buffer_load_b128(r, (int)voff, (int)soff, AUX_K); }
__device__ __forceinline__ v4u ldv(rsrc_t r, unsigned voff, unsigned soff) { return __builtin_amdgcn_raw_buffer_load_b128(r, (int)voff, (int)soff, AUX_V); }
__device__ __forceinline__ v4u ldsc(rsrc_t r, unsigned voff) { return __builtin_amdgcn_raw_buffer_load_b128(r, (int)voff, 0, AUX_SC1); }
__device__ __forceinline__ void stsc(rsrc_t r, unsigned voff, v4u v) { __builtin_amdgcn_raw_buffer_store_b128(v, r, (int)voff, 0, AUX_SC1); }
__device__ __forceinline__ void stsc2(rsrc_t r, unsigned voff, v2u v) { __builtin_amdgcn_raw_buffer_store_b64(v, r, (int)voff, 0, AUX_SC1); }
#define WAITVM(n) asm volatile("s_waitcnt vmcnt(%0)" ::"n"(n) : "memory")
#define BAR() asm volatile("s_waitcnt lgkmcnt(0)\n\ts_barrier" ::: "memory")
#define SB() __builtin_amdgcn_sched_barrier(0)
#define FENCE() asm volatile("" ::: "memory")

template <typename T> __device__ __forceinline__ T lds_ld(LAS unsigned char* L, int off) { return *(LAS T*)(L + off); }
template <typename T> __device__ __forceinline__ void lds_st(LAS unsigned char* L, int off, T v) { *(LAS T*)(L + off) = v; }

template <int I, int N, typename F> __device__ __forceinline__ void sfor(F&& f) {
  if constexpr (I < N) { f(std::integral_constant<int, I>{}); sfor<I + 1, N>(f); }
}
#define AINL __attribute__((always_inline))

struct Sel { unsigned s[2][4]; };
__device__ __forceinline__ v4u diagA(unsigned X, const Sel& S, int par) {
  v4u a;
#pragma unroll
  for (int d = 0; d < 4; ++d) a[d] = __builtin_amdgcn_perm(X, X, S.s[par][d]);
  return a;
}
__device__ __forceinline__ v4u tag4(f4 v, unsigned T) {
  v4u r;
#pragma unroll
  for (int e = 0; e < 4; ++e) r[e] = ((__float_as_uint(v[e]) + 0x80u) & 0xffffff00u) | ((T >> (8 * e)) & 0xffu);
  return r;
}
__device__ __forceinline__ bool tagok4(v4u v, unsigned T) {
  const unsigned lo = __builtin_amdgcn_perm(v.y, v.x, 0x0c0c0400u), hi = __builtin_amdgcn_perm(v.w, v.z, 0x04000c0cu);
  return (lo | hi) == T;
}
__device__ __forceinline__ unsigned long long ballot(bool p) { return __builtin_amdgcn_ballot_w64(p); }

#define STAMP(n) do { } while (0)

#define KVW 4
constexpr int NPOST = (32 - NKB) + 32 + 40;
struct It { int kind, idx; };
constexpr It wsel(int n) { return n < 20 ? It{2, n} : (n < 30 ? It{3, n - 20} : It{4, n - 30}); }
constexpr It post_item(int m) {
  constexpr int NK = 32 - NKB;
  if (m < 2 * NK) return (m & 1) ? wsel(m >> 1) : It{0, NKB + (m >> 1)};
  const int r = m - 2 * NK, nw = 40 - NK;
  if (r < 2 * nw) return (r & 1) ? wsel(NK + (r >> 1)) : It{1, r >> 1};
  return It{1, r - nw};
}
constexpr int NKV = (32 - NKB) + 32;
constexpr int post_kvpos(int k) { int n = -1; for (int m = 0; m < NPOST; ++m) { if (post_item(m).kind < 2) { ++n; if (n == k) return m; } } return NPOST; }
constexpr int post_kvno(int m) { int n = 0; for (int i = 0; i < m; ++i) if (post_item(i).kind < 2) ++n; return n; }
constexpr int NPRE = 20 + NKB;
constexpr It pre_item(int m) {
  if (m < 30) return (m % 3 == 0) ? It{0, m / 3} : It{2, (m / 3) * 2 + (m % 3) - 1};
  return It{0, m - 20};
}
constexpr int pre_kvpos(int k) { int n = -1; for (int m = 0; m < NPRE; ++m) { if (pre_item(m).kind == 0) { ++n; if (n == k) return m; } } return NPRE; }

__global__ void __launch_bounds__(512, 2)
fused_kernel(const void* __restrict__ px, const void* __restrict__ pk, const void* __restrict__ pv, const void* __restrict__ pwq,
             const void* __restrict__ pwo, const void* __restrict__ pw1, const void* __restrict__ pw2, const void* __restrict__ pc,
             const void* __restrict__ pwm, void* __restrict__ pout, void* __restrict__ pws) {
  __shared__ __attribute__((aligned(16))) unsigned char smem[L_END];
  LAS unsigned char* L = (LAS unsigned char*)smem;
  const int t = threadIdx.x, lane = t & 63, w = __builtin_amdgcn_readfirstlane(t >> 6);
  const int wg = blockIdx.x, xg = wg & 7, e8 = wg >> 3, b = wg >> 5, h = xg * 4 + (e8 & 3);
  const int g = lane >> 4, c = lane & 15;
  const bool mlp = h < 27;
  STAMP(0);
  const rsrc_t rsWS = mkrs(pws, WS_BYTES);
  const rsrc_t rsK = mkrs((const char*)pk + ((size_t)(b * 32 + h) * 4096 + 3072) * 256, 262144);
  const rsrc_t rsV = mkrs((const char*)pv + ((size_t)(b * 32 + h) * 4096 + 3072) * 256, 262144);
  const rsrc_t rsWQ = mkrs(pwq, 640 * 4096 * 2), rsWO = mkrs(pwo, 4096 * 640 * 2), rsW1 = mkrs(pw1, 640 * 1728 * 2), rsW2 = mkrs(pw2, 1728 * 640 * 2);
  const rsrc_t rsWM = mkrs(pwm, 640 * 3840 * 2), rsC = mkrs(pc, 10240), rsX = mkrs(px, 10240), rsOUT = mkrs(pout, 10240);

  const bool role = wg < 240;
  const int ta = wg % 60, kq = wg / 60;
  const unsigned T = *(const __attribute__((address_space(4))) unsigned*)((const char*)pws + WS_EPOCH) + 1u;
  v4u cv0 = ldnt(rsC, (role && t < 160) ? (t / 20) * 1280 + kq * 320 + (t % 20) * 16 : OOB, 0);
  const unsigned wmoff = (unsigned)(160 * kq + 8 * w + (lane >> 3)) * 7680u + ta * 128 + (lane & 7) * 16;
  v4u wm0 = ldnt(rsWM, role ? wmoff : OOB, 0);
  v4u wm1 = ldnt(rsWM, role ? wmoff : OOB, 64 * 7680);
  v4u wm2 = ldnt(rsWM, (role && w < 4) ? wmoff : OOB, 128 * 7680);
  v4u xa = ldnt(rsX, w == 5 ? b * 1280 + lane * 16 : OOB, 0);
  unsigned xb = __builtin_amdgcn_raw_buffer_load_b32(rsX, w == 5 ? b * 1280 + 1024 + lane * 4 : OOB, 0, AUX_NT);
  v2u xown = __builtin_amdgcn_raw_buffer_load_b64(rsX, (w == 5) ? b * 1280 + 40 * h + (lane % 5) * 8 : OOB, 0, AUX_NT);
  SB();
  v4u WB[20], KB[NKB];
  const unsigned offWQ = (unsigned)(80 * w + 20 * g) * 8192u + h * 256 + c * 16;
  const unsigned offKV = w * 1024 + lane * 16;
  auto pre_issue = [&](auto mc) AINL {
    constexpr It it = pre_item(decltype(mc)::value);
    if constexpr (it.kind == 0) KB[it.idx] = ldk(rsK, offKV, it.idx * 8192); else WB[it.idx] = ldw(rsWQ, offWQ, it.idx * 8192);
  };
  constexpr int NINIT0 = pre_kvpos(1), NINIT = pre_kvpos(KVW);
  sfor<0, NINIT0>(pre_issue);
  SB();
  STAMP(1);
  {
    auto silu8 = [&](v4u v) AINL {
      v4u r;
#pragma unroll
      for (int d = 0; d < 4; ++d) r[d] = cvtpk(fsilu(bflo(v[d])), fsilu(bfhi(v[d])));
      return r;
    };
    if (t < 160) lds_st<v4u>(L, L_SC + (t / 20) * SC_STRIDE + (t % 20) * 16, silu8(cv0));
    lds_st<v4u>(L, L_WL + w * 1024 + lane * 16, wm0);
    lds_st<v4u>(L, L_WL + (w + 8) * 1024 + lane * 16, wm1);
    if (w < 4) lds_st<v4u>(L, L_WL + (w + 16) * 1024 + lane * 16, wm2);
  }
  SB();
  sfor<NINIT0, NINIT>(pre_issue);
  SB();
  BAR();
  STAMP(2);
  {
    f4 acc = {0.f, 0.f, 0.f, 0.f};
    const int tl = w & 3, s0_ = (w >> 2) * 3;
    const int trbase = L_WL + (8 * g + ((lane & 15) >> 2)) * 128 + tl * 32 + (lane & 3) * 8;
#pragma unroll
    for (int n = 0; n < 3; ++n) {
      if (n < 2 || w < 4) {
        const int s = s0_ + n;
        const v4u a = lds_ld<v4u>(L, L_SC + (c & 7) * SC_STRIDE + s * 64 + g * 16);
        const s4 b0 = __builtin_amdgcn_ds_read_tr16_b64_v4i16((LAS s4*)(L + trbase + s * 4096));
        const s4 b1 = __builtin_amdgcn_ds_read_tr16_b64_v4i16((LAS s4*)(L + trbase + s * 4096 + 512));
        const v2u u0 = __builtin_bit_cast(v2u, b0), u1 = __builtin_bit_cast(v2u, b1);
        acc = mfma(a, v4u{u0.x, u0.y, u1.x, u1.y}, acc);
      }
    }
    if (g < 2) {
#pragma unroll
      for (int r = 0; r < 4; ++r) lds_st<float>(L, L_PART + (w >> 2) * 2048 + (4 * g + r) * 256 + (16 * tl + c) * 4, acc[r]);
    }
  }
  BAR();
  if (t < 128 && role) {
    const f4 s = lds_ld<f4>(L, L_PART + t * 16) + lds_ld<f4>(L, L_PART + 2048 + t * 16);
    stsc(rsWS, WS_PM + (((t >> 4) * 4 + kq) * 3840 + ta * 64) * 4 + (t & 15) * 16, tag4(s, T));
  }
  float xmean = 0.f, xrden = 0.f;
  if (w == 5) {
    float xv[10];
#pragma unroll
    for (int d = 0; d < 4; ++d) { xv[2 * d] = bflo(xa[d]); xv[2 * d + 1] = bfhi(xa[d]); }
    xv[8] = bflo(xb); xv[9] = bfhi(xb);
    float s = 0.f;
#pragma unroll
    for (int d = 0; d < 10; ++d) s += xv[d];
    const float mean = wave_sum(s) * (1.f / 640.f);
    float q2 = 0.f;
#pragma unroll
    for (int d = 0; d < 10; ++d) { const float dd = xv[d] - mean; q2 += dd * dd; }
    const float var = rbf(wave_sum(q2) * (1.f / 640.f));
    xmean = rbf(mean); xrden = frcp(rbf(__builtin_amdgcn_sqrtf(rbf(var + 1e-5f))));
    if (lane < 5) lds_st<v2u>(L, L_XR + lane * 8, xown);
  }
  STAMP(3);
  unsigned roff[4]; v4u rv[4];
  {
    const int sec = lane / 5, k5 = lane % 5;
#pragma unroll
    for (int q = 0; q < 4; ++q) roff[q] = (w == 5 && lane < 30) ? WS_PM + ((b * 4 + q) * 3840 + sec * 640 + 20 * h + 4 * k5) * 4 : OOB;
  }
  unsigned goff[3]; int gdst;
  {
    gdst = w == 0 ? L_HB : (w == 1 ? L_SB : (w == 2 ? L_CB : L_DUMMY));
#pragma unroll
    for (int n = 0; n < 3; ++n) {
      const int idx = 64 * n + lane;
      goff[n] = (w < 3 && (w == 0 || mlp) && idx < 160) ? WS_AGP + (b * 32 + idx / 5) * 256 + (5 * w + idx % 5) * 16 : OOB;
      if (w == 4 && wg == 0 && idx < 128) goff[n] = WS_ALIVE + idx * 16;
    }
  }
  v4u gv[3];
  const unsigned long long rmask = ballot(roff[0] != OOB);
  bool rdone = w != 5;
  auto rprocess = [&]() AINL {
    STAMP(14);
    f4 mv;
#pragma unroll
    for (int e = 0; e < 4; ++e) mv[e] = (__uint_as_float(rv[0][e] & 0xffffff00u) + __uint_as_float(rv[1][e] & 0xffffff00u)) +
                                        (__uint_as_float(rv[2][e] & 0xffffff00u) + __uint_as_float(rv[3][e] & 0xffffff00u));
    { float a0 = mv[0], a1 = mv[1], a2 = mv[2], a3 = mv[3]; rbf2(a0, a1); rbf2(a2, a3); mv = f4{a0, a1, a2, a3}; }
    const float xx[4] = {bflo(xown.x), bfhi(xown.x), bflo(xown.y), bfhi(xown.y)};
    v4u msg;
#pragma unroll
    for (int e = 0; e < 4; ++e) {
      const float ca = __uint_as_float(__builtin_amdgcn_ds_bpermute(((lane + 5) & 63) * 4, (int)__float_as_uint(mv[e])));
      const float ln = rbf(rbf(xx[e] - xmean) * xrden);
      const float hv = rbf(ln * rbf(1.f + ca)) + mv[e];
      const float o = lane < 5 ? hv : (lane >= 20 ? 1.f + mv[e] : mv[e]);
      msg[e] = (__float_as_uint(rbf(o)) & 0xffff0000u) | ((T >> (8 * e)) & 0xffu);
    }
    if (lane < 5) stsc(rsWS, WS_AGP + (b * 32 + h) * 256 + lane * 16, msg);
    else if (lane >= 15 && lane < 25) stsc(rsWS, WS_AGP + (b * 32 + h) * 256 + (lane - 10) * 16, msg);
    else if (lane >= 10 && lane < 15) lds_st<f4>(L, L_GAB + (lane - 10) * 16, mv);
    else if (lane >= 25 && lane < 30) lds_st<f4>(L, L_GAB + 80 + (lane - 25) * 16, mv);
  };
  auto rok = [&]() AINL {
    const unsigned long long m = ballot(tagok4(rv[0], T)) & ballot(tagok4(rv[1], T)) & ballot(tagok4(rv[2], T)) & ballot(tagok4(rv[3], T));
    return (m & rmask) == rmask;
  };
  auto rreload = [&]() AINL {
#pragma unroll
    for (int q = 0; q < 4; ++q) { const bool k1 = tagok4(rv[q], T); const v4u nv = ldsc(rsWS, k1 ? OOB : roff[q]); rv[q] = k1 ? rv[q] : nv; }
  };
  constexpr int RPOS = pre_kvpos(RK);
  constexpr int RCHK1 = pre_kvpos(RK + RD1);
  constexpr int RCHK2 = pre_kvpos(RK + RD2);
  constexpr int CLRPOS = pre_kvpos(KVW + 1);
  constexpr int GPOS = pre_kvpos(GK);
  static_assert(RPOS >= NINIT && RCHK2 < GPOS && GPOS < NPRE, "stage order");
  sfor<NINIT, NPRE>([&](auto mc) AINL {
    constexpr int m = decltype(mc)::value;
    constexpr It it = pre_item(m);
    if constexpr (it.kind == 0) {
      constexpr int o = pre_kvpos(it.idx - KVW);
      WAITVM(m - 1 - o + (m > GPOS && o <= GPOS ? 3 : 0) + (m > RPOS && o <= RPOS ? 4 : 0));
    }
    pre_issue(mc);
    if constexpr (m == CLRPOS) {
      const v4u clr = tag4(f4{0.f, 0.f, 0.f, 0.f}, T - 1u);
      if (t < 256) stsc(rsWS, WS_OUT1 + wg * 4096 + t * 16, clr); else stsc(rsWS, WS_OUT2 + wg * 4096 + (t - 256) * 16, clr);
      if (w == 5) {
        if (lane < 8) stsc(rsWS, WS_AG + (b * 32 + h) * 128 + lane * 16, clr);
        else if (lane < 24) stsc(rsWS, WS_AGP + (b * 32 + h) * 256 + (lane - 8) * 16, clr);
        else if (lane == 24) stsc2(rsWS, WS_ALIVE + wg * 8, v2u{T, T});
      }
    }
    if constexpr (m == RPOS) {
#pragma unroll
      for (int q = 0; q < 4; ++q) rv[q] = ldsc(rsWS, roff[q]);
    }
    if constexpr (m == RCHK1) {
      if (!rdone) { if (rok()) { rprocess(); rdone = true; } else rreload(); }
    }
    if constexpr (m == RCHK2) {
      if (!rdone) {
        unsigned spins = 0;
        while (!rok() && ++spins < SPIN_CAP) { __builtin_amdgcn_s_sleep(2); FENCE(); rreload(); }
        rprocess();
      }
    }
    if constexpr (m == GPOS) {
#pragma unroll
      for (int n = 0; n < 3; ++n) gv[n] = ldsc(rsWS, goff[n]);
    }
    SB();
  });
  STAMP(4);
  {
    const bool alive = w == 4;
    unsigned spins = 0;
    for (;;) {
      bool ok = true;
#pragma unroll
      for (int n = 0; n < 3; ++n) ok = ok && (goff[n] == OOB || (alive ? (gv[n].y == T && gv[n].w == T) : tagok4(gv[n], T)));
      if (__all(ok)) break;
      if (++spins > SPIN_CAP) break;
      __builtin_amdgcn_s_sleep(2);
      FENCE();
#pragma unroll
      for (int n = 0; n < 3; ++n) gv[n] = ldsc(rsWS, goff[n]);
    }
#pragma unroll
    for (int n = 0; n < 3; ++n) {
      const int idx = 64 * n + lane;
      if (goff[n] != OOB && !alive) lds_st<v2u>(L, gdst + (20 * (idx / 5) + 4 * (idx % 5)) * 2,
                                                v2u{__builtin_amdgcn_perm(gv[n].y, gv[n].x, 0x07060302u), __builtin_amdgcn_perm(gv[n].w, gv[n].z, 0x07060302u)});
    }
    if (alive && wg == 0 && lane == 0) __builtin_amdgcn_raw_buffer_store_b32(T, rsWS, WS_EPOCH, 0, AUX_SC1);
  }
  BAR();
  STAMP(5);
  Sel S;
  {
    const int e = c & 7;
#pragma unroll
    for (int par = 0; par < 2; ++par)
#pragma unroll
      for (int d = 0; d < 4; ++d) {
        const unsigned lo = (unsigned)(2 * par) | ((unsigned)(2 * par + 1) << 8);
        S.s[par][d] = (d == (e >> 1)) ? ((e & 1) ? (0x0c0cu | (lo << 16)) : (0x0c0c0000u | lo)) : 0x0c0c0c0cu;
      }
  }
  unsigned qr[4];
  {
    unsigned X[10];
#pragma unroll
    for (int d = 0; d < 5; ++d) { const v2u u = lds_ld<v2u>(L, L_HB + 160 * w + 40 * g + d * 8); X[2 * d] = u.x; X[2 * d + 1] = u.y; }
    f4 a0 = {0.f, 0.f, 0.f, 0.f}, a1 = {0.f, 0.f, 0.f, 0.f};
#pragma unroll
    for (int p = 0; p < 20; p += 2) { a0 = mfma(diagA(X[p >> 1], S, 0), WB[p], a0); a1 = mfma(diagA(X[p >> 1], S, 1), WB[p + 1], a1); }
    a0 += a1;
    if (lane < 32) lds_st<f4>(L, L_QP + w * 512 + (8 * c + 4 * g) * 4, a0);
    BAR();
    float q0 = 0.f, q1 = 0.f;
#pragma unroll
    for (int ww = 0; ww < 8; ++ww) { const v2u u = lds_ld<v2u>(L, L_QP + ww * 512 + lane * 8); q0 += __uint_as_float(u.x); q1 += __uint_as_float(u.y); }
    const unsigned qp = cvtpk(q0, q1);
#pragma unroll
    for (int d = 0; d < 4; ++d) qr[d] = (unsigned)__builtin_amdgcn_ds_bpermute((4 * c + d) * 4, (int)qp);
  }
  STAMP(6);
  float xs[32];
  auto kdot = [&](v4u k) __attribute__((always_inline)) { return dot2(k.w, qr[3], dot2(k.z, qr[2], dot2(k.y, qr[1], dot2(k.x, qr[0], 0.f)))); };
  v4u WO[20], W1[10], W2[10], KR[32 - NKB > 0 ? 32 - NKB : 1], VR[KVW + 1];
  const unsigned offWO = (unsigned)(h * 128 + 16 * w + 4 * g) * 1280u + c * 16;
  const unsigned offW1 = mlp ? (unsigned)(80 * w + 40 * (c >> 3) + 10 * g) * 3456u + (64 * h + 8 * (c & 7)) * 2 : OOB;
  const unsigned offW2 = mlp ? (unsigned)(64 * h + 8 * w + 2 * g) * 1280u + c * 16 : OOB;
  auto post_issue = [&](auto mc) AINL {
    constexpr It it = post_item(decltype(mc)::value);
    if constexpr (it.kind == 0) KR[it.idx - NKB] = ldk(rsK, offKV, it.idx * 8192);
    else if constexpr (it.kind == 1) VR[it.idx % (KVW + 1)] = ldv(rsV, offKV, it.idx * 8192);
    else if constexpr (it.kind == 2) WO[it.idx] = ldw(rsWO, offWO, (it.idx / 5) * 1280 + (it.idx % 5) * 256);
    else if constexpr (it.kind == 3) W1[it.idx] = ldw(rsW1, offW1, it.idx * 3456);
    else W2[it.idx] = ldw(rsW2, offW2, (it.idx / 5) * 1280 + (it.idx % 5) * 256);
  };
  auto treduce = [&](auto bc) AINL {
    constexpr int B0 = decltype(bc)::value;
    float x[8];
#pragma unroll
    for (int i = 0; i < 8; ++i) { const float lo = xs[B0 + i] + dppf<XOR8>(xs[B0 + i]), hi = xs[B0 + i + 8] + dppf<XOR8>(xs[B0 + i + 8]); x[i] = (c & 8) ? hi : lo; }
#pragma unroll
    for (int i = 0; i < 4; ++i) { const float lo = x[i] + dppf<XOR7>(x[i]), hi = x[i + 4] + dppf<XOR7>(x[i + 4]); x[i] = (c & 4) ? hi : lo; }
#pragma unroll
    for (int i = 0; i < 2; ++i) { const float lo = x[i] + dppf<XOR2>(x[i]), hi = x[i + 2] + dppf<XOR2>(x[i + 2]); x[i] = (c & 2) ? hi : lo; }
    const float lo = x[0] + dppf<XOR1>(x[0]), hi = x[1] + dppf<XOR1>(x[1]);
    return (c & 1) ? hi : lo;
  };
  constexpr int PINIT = post_kvpos(KVW);
  sfor<0, PINIT>(post_issue);
  SB();
#pragma unroll
  for (int j = 0; j < NKB; ++j) xs[j] = kdot(KB[j]);
  float s0 = treduce(std::integral_constant<int, 0>{});
  SB();
  constexpr float C2 = 0.08838834764831845f * 1.4426950408889634f;
  float s1 = 0.f, mw = 0.f, fw = 0.f;
  unsigned ppk = 0;
  f4 oacc = {0.f, 0.f, 0.f, 0.f};
  auto process = [&](auto kc) AINL {
    constexpr It it = post_item(post_kvpos(decltype(kc)::value));
    if constexpr (it.kind == 0) {
      xs[it.idx] = kdot(KR[it.idx - NKB]);
      if constexpr (it.idx == 31) {
        s1 = treduce(std::integral_constant<int, 16>{});
        mw = wave_max(fmaxf(s0, s1));
        const float p0 = __builtin_amdgcn_exp2f((s0 - mw) * C2), p1 = __builtin_amdgcn_exp2f((s1 - mw) * C2);
        const float lw = wave_sum(p0 + p1);
        ppk = cvtpk(p0, p1);
        if (lane == 0) lds_st<v2u>(L, L_ML + w * 8, v2u{__float_as_uint(mw), __float_as_uint(lw)});
      }
    } else {
      const unsigned X = dppu<0x150 + (it.idx & 15)>(ppk);
      oacc = mfma(diagA(X, S, it.idx >> 4), VR[it.idx % (KVW + 1)], oacc);
      if constexpr (it.idx == 8) {
        BAR();
        float M = -INFINITY;
        v2u ml[8];
#pragma unroll
        for (int ww = 0; ww < 8; ++ww) { ml[ww] = lds_ld<v2u>(L, L_ML + ww * 8); M = fmaxf(M, __uint_as_float(ml[ww].x)); }
        float Ls = 0.f;
#pragma unroll
        for (int ww = 0; ww < 8; ++ww) Ls += __builtin_amdgcn_exp2f((__uint_as_float(ml[ww].x) - M) * C2) * __uint_as_float(ml[ww].y);
        fw = __builtin_amdgcn_exp2f((mw - M) * C2) * frcp(Ls);
      }
    }
  };
  sfor<PINIT, NPOST>([&](auto mc) AINL {
    constexpr int m = decltype(mc)::value;
    constexpr It it = post_item(m);
    if constexpr (it.kind < 2) {
      constexpr int k = post_kvno(m);
      WAITVM(m - 1 - post_kvpos(k - KVW));
      post_issue(mc);
      process(std::integral_constant<int, k - KVW>{});
    } else post_issue(mc);
    SB();
  });
  sfor<NKV - KVW, NKV>([&](auto kc) AINL { process(kc); SB(); });
  STAMP(7);
  if (lane < 32) lds_st<f4>(L, L_OP + w * 512 + (8 * c + 4 * g) * 4, oacc * fw);
  BAR();
  {
    const int kap = (c >> 1) & 3, sig = (c & 1) + 2 * (c >> 3);
    const int ko = (16 * w + 4 * g + kap) * 4;
    float o = lds_ld<float>(L, L_OP + (2 * sig) * 512 + ko) + lds_ld<float>(L, L_OP + (2 * sig + 1) * 512 + ko);
    o += dppf<XOR1>(o); o += dppf<XOR8>(o);
    const unsigned P = cvtpk(dppf<QB0>(o), dppf<QB2>(o)), Q = dppu<XOR7>(P);
    const unsigned X0 = (c & 4) ? Q : P, X1 = (c & 4) ? P : Q;
    const v4u A0 = diagA(X0, S, 0), A1 = diagA(X0, S, 1), A2 = diagA(X1, S, 0), A3 = diagA(X1, S, 1);
#pragma unroll
    for (int cb = 0; cb < 5; ++cb) {
      f4 y = {0.f, 0.f, 0.f, 0.f};
      y = mfma(A0, WO[cb], y); y = mfma(A1, WO[5 + cb], y); y = mfma(A2, WO[10 + cb], y); y = mfma(A3, WO[15 + cb], y);
      if (lane < 32) lds_st<f4>(L, L_YP + w * 2560 + (128 * cb + 8 * c + 4 * g) * 4, y);
    }
  }
  BAR();
  STAMP(8);
  auto publish = [&](int wsoff) __attribute__((always_inline)) {
    const int k = t & 7, j = t >> 3;
    f4 s = {0.f, 0.f, 0.f, 0.f};
    if (k < 5) {
#pragma unroll
      for (int ww = 0; ww < 8; ++ww) s += lds_ld<f4>(L, L_YP + ww * 2560 + (20 * j + 4 * k) * 4);
    }
    stsc(rsWS, wsoff + t * 16, tag4(s, T));
  };
  float x1v[4] = {0.f, 0.f, 0.f, 0.f};
  auto rs_gather = [&](int wsbase, int nprod) __attribute__((always_inline)) -> f4 {
    const int pl = lane >> 3, k = lane & 7;
    unsigned off[4]; v4u v[4];
#pragma unroll
    for (int q = 0; q < 4; ++q) {
      const int u = 8 * q + pl;
      const int wgu = (b << 5) | ((u & 3) << 3) | (u >> 2);
      off[q] = (k < 5 && u < nprod) ? wsbase + wgu * 4096 + h * 128 + k * 16 : OOB;
      v[q] = ldsc(rsWS, off[q]);
    }
    unsigned spins = 0;
    for (;;) {
      bool all = true;
#pragma unroll
      for (int q = 0; q < 4; ++q) all = all && (off[q] == OOB || tagok4(v[q], T));
      if (__all(all)) break;
      if (++spins > SPIN_CAP) break;
      __builtin_amdgcn_s_sleep(POLL_SLEEP);
      FENCE();
#pragma unroll
      for (int q = 0; q < 4; ++q) { const bool ok = tagok4(v[q], T); const v4u nv = ldsc(rsWS, ok ? OOB : off[q]); v[q] = ok ? v[q] : nv; }
    }
    f4 s = {0.f, 0.f, 0.f, 0.f};
#pragma unroll
    for (int q = 0; q < 4; ++q)
#pragma unroll
      for (int e = 0; e < 4; ++e) s[e] += off[q] == OOB ? 0.f : __uint_as_float(v[q][e] & 0xffffff00u);
#pragma unroll
    for (int e = 0; e < 4; ++e) { float a = s[e]; a += dppf<XOR8>(a); s[e] = x32_sum(x16_sum(a)); }
    return s;
  };
  if (w < 4) publish(WS_OUT1 + wg * 4096);
  else if (w == 4) {
    const v2u xr = lds_ld<v2u>(L, L_XR + (lane & 7) * 8); const f4 gg = lds_ld<f4>(L, L_GAB + (lane & 7) * 16);
    unsigned off[3]; v4u v[3]; v2u sbv[3], cbv[3];
#pragma unroll
    for (int q = 0; q < 3; ++q) {
      const int idx = 64 * q + lane, jj = idx / 5, k = idx % 5;
      off[q] = (idx < 160 && mlp) ? WS_AG + (b * 32 + jj) * 128 + k * 16 : OOB;
      const int col = idx < 160 ? 20 * jj + 4 * k : 0;
      sbv[q] = lds_ld<v2u>(L, L_SB + col * 2); cbv[q] = lds_ld<v2u>(L, L_CB + col * 2);
    }
    __builtin_amdgcn_s_sleep(D_RS1);
    const f4 s = rs_gather(WS_OUT1, 32);
    const float xx[4] = {bflo(xr.x), bfhi(xr.x), bflo(xr.y), bfhi(xr.y)};
    v4u m;
#pragma unroll
    for (int e = 0; e < 4; ++e) { x1v[e] = rbf(xx[e] + rbf(gg[e] * rbf(s[e]))); m[e] = __float_as_uint(x1v[e]) | ((T >> (8 * e)) & 0xffu); }
    if (lane < 5) stsc(rsWS, WS_AG + (b * 32 + h) * 128 + lane * 16, m);
    STAMP(9);
    if (mlp) {
      __builtin_amdgcn_s_sleep(D_AG);
#pragma unroll
      for (int q = 0; q < 3; ++q) v[q] = ldsc(rsWS, off[q]);
      unsigned spins = 0;
      for (;;) {
        bool all = true;
#pragma unroll
        for (int q = 0; q < 3; ++q) all = all && (off[q] == OOB || tagok4(v[q], T));
        if (__all(all)) break;
        if (++spins > SPIN_CAP) break;
        __builtin_amdgcn_s_sleep(POLL_SLEEP);
        FENCE();
#pragma unroll
        for (int q = 0; q < 3; ++q) { const bool ok = tagok4(v[q], T); const v4u nv = ldsc(rsWS, ok ? OOB : off[q]); v[q] = ok ? v[q] : nv; }
      }
      STAMP(13);
      float xv[12]; float sm = 0.f;
#pragma unroll
      for (int q = 0; q < 3; ++q)
#pragma unroll
        for (int e = 0; e < 4; ++e) { xv[4 * q + e] = off[q] == OOB ? 0.f : __uint_as_float(v[q][e] & 0xffff0000u); sm += xv[4 * q + e]; }
      const float mean = wave_sum(sm) * (1.f / 640.f);
      float q2 = 0.f;
#pragma unroll
      for (int q = 0; q < 3; ++q)
#pragma unroll
        for (int e = 0; e < 4; ++e) { const float dd = off[q] == OOB ? 0.f : xv[4 * q + e] - mean; q2 += dd * dd; }
      const float var = rbf(wave_sum(q2) * (1.f / 640.f));
      const float mb = rbf(mean), rden = frcp(rbf(__builtin_amdgcn_sqrtf(rbf(var + 1e-5f))));
#pragma unroll
      for (int q = 0; q < 3; ++q) {
        const float sb4[4] = {bflo(sbv[q].x), bfhi(sbv[q].x), bflo(sbv[q].y), bfhi(sbv[q].y)}, cb4[4] = {bflo(cbv[q].x), bfhi(cbv[q].x), bflo(cbv[q].y), bfhi(cbv[q].y)};
        float hv[4];
#pragma unroll
        for (int e = 0; e < 4; ++e) hv[e] = rbf(rbf(rbf(xv[4 * q + e] - mb) * rden) * cb4[e]) + sb4[e];
        const int idx = 64 * q + lane;
        if (idx < 160) lds_st<v2u>(L, L_H2 + (20 * (idx / 5) + 4 * (idx % 5)) * 2, v2u{cvtpk(hv[0], hv[1]), cvtpk(hv[2], hv[3])});
      }
    }
  }
  if (mlp) {
    BAR();
    STAMP(10);
    {
      unsigned X[5];
#pragma unroll
      for (int d = 0; d < 5; ++d) X[d] = lds_ld<unsigned>(L, L_H2 + 160 * w + 80 * (c >> 3) + 20 * g + d * 4);
      f4 a0 = {0.f, 0.f, 0.f, 0.f}, a1 = {0.f, 0.f, 0.f, 0.f};
#pragma unroll
      for (int p = 0; p < 10; p += 2) { a0 = mfma(diagA(X[p >> 1], S, 0), W1[p], a0); a1 = mfma(diagA(X[p >> 1], S, 1), W1[p + 1], a1); }
      a0 += a1;
      if ((g >> 1) == (c >> 3)) lds_st<f4>(L, L_HP + (2 * w + (c >> 3)) * 256 + (8 * (c & 7) + 4 * (g & 1)) * 4, a0);
    }
    BAR();
    {
      const int kap = (c >> 2) & 1, sig = (c & 3) + 4 * (c >> 3);
      const int n = (8 * w + 2 * g + kap) * 4;
      float hd = lds_ld<float>(L, L_HP + (2 * sig) * 256 + n) + lds_ld<float>(L, L_HP + (2 * sig + 1) * 256 + n);
      hd += dppf<XOR1>(hd); hd += dppf<XOR2>(hd); hd += dppf<XOR8>(hd);
      hd = rbf(hd);
      hd = fsilu(hd);
      const float ot = dppf<XOR7>(hd);
      const unsigned X = kap ? cvtpk(ot, hd) : cvtpk(hd, ot);
      const v4u A0 = diagA(X, S, 0), A1 = diagA(X, S, 1);
#pragma unroll
      for (int cb = 0; cb < 5; ++cb) {
        f4 y = {0.f, 0.f, 0.f, 0.f};
        y = mfma(A0, W2[cb], y); y = mfma(A1, W2[5 + cb], y);
        if (lane < 32) lds_st<f4>(L, L_YP + w * 2560 + (128 * cb + 8 * c + 4 * g) * 4, y);
      }
    }
    BAR();
    STAMP(11);
    if (w < 4) publish(WS_OUT2 + wg * 4096);
  }
  if (w == 4) {
    const f4 gg = lds_ld<f4>(L, L_GAB + 80 + (lane & 7) * 16);
    if (!mlp) __builtin_amdgcn_s_sleep(48);
    __builtin_amdgcn_s_sleep(D_RS2);
    const f4 s = rs_gather(WS_OUT2, 27);
    float o[4];
#pragma unroll
    for (int e = 0; e < 4; ++e) o[e] = x1v[e] + rbf(gg[e] * rbf(s[e]));
    if (lane < 5) __builtin_amdgcn_raw_buffer_store_b64(v2u{cvtpk(o[0], o[1]), cvtpk(o[2], o[3])}, rsOUT, b * 1280 + 40 * h + lane * 8, 0, 0);
    STAMP(12);
  }
}

extern "C" void kernel_launch(void* const* d_in, const int* in_sizes, int n_in, void* d_out, int out_size, void* d_ws, size_t ws_size,
                              hipStream_t stream) {
  fused_kernel<<<256, 512, 0, stream>>>(d_in[0], d_in[1], d_in[2], d_in[3], d_in[4], d_in[6], d_in[7], d_in[8], d_in[9], d_out, d_ws);
}
```

```cpp
#include <hip/hip_runtime.h>
#include <hip/hip_bf16.h>
#include <stdint.h>
#include <type_traits>

typedef unsigned v4u __attribute__((ext_vector_type(4)));
typedef unsigned v2u __attribute__((ext_vector_type(2)));
typedef float f4 __attribute__((ext_vector_type(4)));
typedef __bf16 bf8 __attribute__((ext_vector_type(8)));
typedef __bf16 bf2 __attribute__((ext_vector_type(2)));
typedef short s4 __attribute__((ext_vector_type(4)));
#define LAS __attribute__((address_space(3)))
typedef __amdgpu_buffer_rsrc_t rsrc_t;

#define AUX_K 2
#define AUX_V 2
#define AUX_W 0
constexpr int AUX_NT = 2, AUX_SC1 = 16;
constexpr unsigned OOB = 0x80000000u;

constexpr int WS_EPOCH = 0;
constexpr int WS_ALIVE = 4096;
constexpr int WS_AGP = 65536;
constexpr int WS_AG = 131072;
constexpr int WS_PM = 262144;
constexpr int WS_OUT1 = 1 << 20;
constexpr int WS_OUT2 = 2 << 20;
constexpr int WS_QPM = 3 << 20;
constexpr int WS_STAMP = 4 << 20;
constexpr int WS_BYTES = 8 << 20;

#define TW 5
#define VRN 10
constexpr int NR = 16;
constexpr int NLDS = 12;

constexpr int L_WL = 0;
constexpr int L_SC = 20480;
constexpr int SC_STRIDE = 336;
constexpr int L_PART = L_SC + 2688;
constexpr int L_QP8 = 0;
constexpr int L_HB = 32768;
constexpr int L_SB = L_HB + 1280;
constexpr int L_CB = L_SB + 1280;
constexpr int L_GAB = L_CB + 1280;
constexpr int L_XR = L_GAB + 160;
constexpr int L_ML = L_XR + 64;
constexpr int L_FLAG = L_ML + 64;
constexpr int L_QB = L_FLAG + 16;
constexpr int L_H2 = L_QB + 256;
constexpr int L_OP = L_H2 + 1280;
constexpr int L_HP = L_OP + 4096;
constexpr int L_KS = L_HP + 4096;
constexpr int L_YP = L_KS;
constexpr int L_END = L_KS + (8 * NLDS * 1024 > 20480 ? 8 * NLDS * 1024 : 20480);
static_assert(L_PART + 4096 <= 32768 && L_HB % 16 == 0 && L_QB % 16 == 0 && L_OP % 16 == 0 && L_KS % 16 == 0 && L_GAB % 16 == 0 && L_H2 % 16 == 0 && L_END <= 163840, "lds map");

#define RK 3
#define RD 2
#define RN 6
#define HK 7
#define HN 6
#define MK 17
#define QK 18
#define D_RS1 8
#define D_AG 10
#define D_RS2 8
#define POLL_SLEEP 1
#define STAMPS 0
#define NOW 0
constexpr unsigned SPIN_CAP = 1u << 15;

__device__ __forceinline__ float bflo(unsigned d) { return __uint_as_float(d << 16); }
__device__ __forceinline__ float bfhi(unsigned d) { return __uint_as_float(d & 0xffff0000u); }
__device__ __forceinline__ unsigned cvtpk(float lo, float hi) {
  bf2 r = {(__bf16)lo, (__bf16)hi}; return __builtin_bit_cast(unsigned, r);
}
__device__ __forceinline__ float rbf(float f) { return bflo(cvtpk(f, 0.f)); }
__device__ __forceinline__ void rbf2(float& a, float& b) { const unsigned p = cvtpk(a, b); a = bflo(p); b = bfhi(p); }
__device__ __forceinline__ float frcp(float x) { return __builtin_amdgcn_rcpf(x); }
__device__ __forceinline__ float fsilu(float a) { return a * frcp(1.f + __builtin_amdgcn_exp2f(a * -1.4426950408889634f)); }
template <int CTRL> __device__ __forceinline__ float dppf(float x) {
  return __builtin_bit_cast(float, __builtin_amdgcn_update_dpp(0, __builtin_bit_cast(int, x), CTRL, 0xf, 0xf, true));
}
template <int CTRL> __device__ __forceinline__ unsigned dppu(unsigned x) { return (unsigned)__builtin_amdgcn_update_dpp(0, (int)x, CTRL, 0xf, 0xf, true); }
constexpr int XOR1 = 0xB1, XOR2 = 0x4E, XOR7 = 0x141, XOR8 = 0x128, QB0 = 0x00, QB2 = 0xAA;
__device__ __forceinline__ float x16_sum(float x) { auto s = __builtin_amdgcn_permlane16_swap(__float_as_uint(x), __float_as_uint(x), false, false); return __uint_as_float(s[0]) + __uint_as_float(s[1]); }
__device__ __forceinline__ float x32_sum(float x) { auto s = __builtin_amdgcn_permlane32_swap(__float_as_uint(x), __float_as_uint(x), false, false); return __uint_as_float(s[0]) + __uint_as_float(s[1]); }
__device__ __forceinline__ float x16_max(float x) { auto s = __builtin_amdgcn_permlane16_swap(__float_as_uint(x), __float_as_uint(x), false, false); return fmaxf(__uint_as_float(s[0]), __uint_as_float(s[1])); }
__device__ __forceinline__ float x32_max(float x) { auto s = __builtin_amdgcn_permlane32_swap(__float_as_uint(x), __float_as_uint(x), false, false); return fmaxf(__uint_as_float(s[0]), __uint_as_float(s[1])); }
__device__ __forceinline__ float wave_sum(float x) {
  x += dppf<XOR1>(x); x += dppf<XOR2>(x); x += dppf<XOR7>(x); x += dppf<XOR8>(x); return x32_sum(x16_sum(x));
}
__device__ __forceinline__ float wave_max(float x) {
  x = fmaxf(x, dppf<XOR1>(x)); x = fmaxf(x, dppf<XOR2>(x)); x = fmaxf(x, dppf<XOR7>(x)); x = fmaxf(x, dppf<XOR8>(x)); return x32_max(x16_max(x));
}
__device__ __forceinline__ f4 mfma(v4u a, v4u b, f4 c) {
  return __builtin_amdgcn_mfma_f32_16x16x32_bf16(__builtin_bit_cast(bf8, a), __builtin_bit_cast(bf8, b), c, 0, 0, 0);
}
__device__ __forceinline__ float dot2(unsigned a, unsigned b, float c) {
  return __builtin_amdgcn_fdot2_f32_bf16(__builtin_bit_cast(bf2, a), __builtin_bit_cast(bf2, b), c, false);
}
__device__ __forceinline__ rsrc_t mkrs(const void* p, unsigned bytes) { return __builtin_amdgcn_make_buffer_rsrc((void*)p, 0, (int)bytes, 0x00020000); }
template <int AUX> __device__ __forceinline__ v4u ld(rsrc_t r, unsigned voff, unsigned soff) { return __builtin_amdgcn_raw_buffer_load_b128(r, (int)voff, (int)soff, AUX); }
__device__ __forceinline__ v4u ldsc(rsrc_t r, unsigned voff) { return __builtin_amdgcn_raw_buffer_load_b128(r, (int)voff, 0, AUX_SC1); }
__device__ __forceinline__ void stsc(rsrc_t r, unsigned voff, v4u v) { __builtin_amdgcn_raw_buffer_store_b128(v, r, (int)voff, 0, AUX_SC1); }
__device__ __forceinline__ void stsc2(rsrc_t r, unsigned voff, v2u v) { __builtin_amdgcn_raw_buffer_store_b64(v, r, (int)voff, 0, AUX_SC1); }
#define WAITVM(n) asm volatile("s_waitcnt vmcnt(%0)" ::"n"((n) > 63 ? 63 : (n)) : "memory")
#define BAR() asm volatile("s_waitcnt lgkmcnt(0)\n\ts_barrier" ::: "memory")
#define SB() __builtin_amdgcn_sched_barrier(0)
#define FENCE() asm volatile("" ::: "memory")
#define PIN(x) asm volatile("" : "+v"(x))

template <typename T> __device__ __forceinline__ T lds_ld(LAS unsigned char* L, int off) { return *(LAS T*)(L + off); }
template <typename T> __device__ __forceinline__ void lds_st(LAS unsigned char* L, int off, T v) { *(LAS T*)(L + off) = v; }
template <int I, int N, typename F> __device__ __forceinline__ void sfor(F&& f) {
  if constexpr (I < N) { f(std::integral_constant<int, I>{}); sfor<I + 1, N>(f); }
}
template <int I> using IC = std::integral_constant<int, I>;
#define AINL __attribute__((always_inline))

struct Sel { unsigned s[2][4]; };
__device__ __forceinline__ v4u diagA(unsigned X, const Sel& S, int par) {
  v4u a;
#pragma unroll
  for (int d = 0; d < 4; ++d) a[d] = __builtin_amdgcn_perm(X, X, S.s[par][d]);
  return a;
}
__device__ __forceinline__ v4u tag4(f4 v, unsigned T) {
  v4u r;
#pragma unroll
  for (int e = 0; e < 4; ++e) r[e] = ((__float_as_uint(v[e]) + 0x80u) & 0xffffff00u) | ((T >> (8 * e)) & 0xffu);
  return r;
}
__device__ __forceinline__ bool tagok4(v4u v, unsigned T) {
  const unsigned lo = __builtin_amdgcn_perm(v.y, v.x, 0x0c0c0400u), hi = __builtin_amdgcn_perm(v.w, v.z, 0x04000c0cu);
  return (lo | hi) == T;
}
__device__ __forceinline__ unsigned long long ballot(bool p) { return __builtin_amdgcn_ballot_w64(p); }

#define STAMP(n) do { } while (0)

struct It { int kind, idx; };
constexpr int WOA = 10;
constexpr int NA = 3 + 32 + WOA;
constexpr It a_item(int m) {
  if (m < 3) return It{4, m};
  int pos = 3, wn = 0;
  for (int k = 0; k < 32; ++k) {
    if (pos == m) return It{0, k};
    ++pos;
    if (k % 3 == 2 && wn < WOA) { if (pos == m) return It{2, wn}; ++pos; ++wn; }
  }
  return It{-1, 0};
}
constexpr int a_kpos(int k) { for (int m = 0; m < NA; ++m) if (a_item(m).kind == 0 && a_item(m).idx == k) return m; return NA; }
#define WEARLY 2
#define WLAST 27
constexpr int NBV = 32 + 20 - WOA, NB = NBV + 10;
constexpr It b_item(int m) {
  if (m < VRN) return It{1, m};
  if (m < VRN + WEARLY) return It{2, WOA + m - VRN};
  if (m >= NBV) return It{3, m - NBV};
  int pos = VRN + WEARLY, wn = WOA + WEARLY;
  for (int v = VRN; v < 32; ++v) {
    if (pos == m) return It{1, v};
    ++pos;
    int target = WOA + WEARLY + ((20 - WOA - WEARLY) * (v - VRN + 1) + (WLAST - VRN)) / (WLAST - VRN + 1);
    if (target > 20) target = 20;
    while (wn < target) { if (pos == m) return It{2, wn}; ++pos; ++wn; }
  }
  return It{-1, 0};
}
static_assert(b_item(NBV - 1).kind == 1 && b_item(NBV - 1).idx == 31, "phase B list");
constexpr int a_lastpark_f() { int k = NR - 1; for (int m = 0; m + TW < NA; ++m) if (a_item(m).kind == 0 && a_item(m).idx > k) k = a_item(m).idx; return k; }
constexpr int LASTPARK = a_lastpark_f();
static_assert(LASTPARK - NR + 1 <= NLDS, "K parking too small");

__global__ void __launch_bounds__(512, 2)
fused_kernel(const void* __restrict__ px, const void* __restrict__ pk, const void* __restrict__ pv, const void* __restrict__ pwq,
             const void* __restrict__ pwo, const void* __restrict__ pw1, const void* __restrict__ pw2, const void* __restrict__ pc,
             const void* __restrict__ pwm, void* __restrict__ pout, void* __restrict__ pws) {
  __shared__ __attribute__((aligned(16))) unsigned char smem[L_END];
  LAS unsigned char* L = (LAS unsigned char*)smem;
  const int t = threadIdx.x, lane = t & 63, w = __builtin_amdgcn_readfirstlane(t >> 6);
  const int wg = blockIdx.x, xg = wg & 7, e8 = wg >> 3, b = wg >> 5, h = xg * 4 + (e8 & 3);
  const int g = lane >> 4, c = lane & 15;
  const bool mlp = h < 27;
  STAMP(0);
  const rsrc_t rsWS = mkrs(pws, WS_BYTES);
  const rsrc_t rsK = mkrs((const char*)pk + ((size_t)(b * 32 + h) * 4096 + 3072) * 256, 262144);
  const rsrc_t rsV = mkrs((const char*)pv + ((size_t)(b * 32 + h) * 4096 + 3072) * 256, 262144);
  const rsrc_t rsWQ = mkrs(pwq, 640 * 4096 * 2), rsWO = mkrs(pwo, 4096 * 640 * 2), rsW1 = mkrs(pw1, 640 * 1728 * 2), rsW2 = mkrs(pw2, 1728 * 640 * 2);
  const rsrc_t rsWM = mkrs(pwm, 640 * 3840 * 2), rsC = mkrs(pc, 10240), rsX = mkrs(px, 10240), rsOUT = mkrs(pout, 10240);

  const bool role = wg < 240;
  const int ta = wg % 60, kq = wg / 60;
  const unsigned T = *(const __attribute__((address_space(4))) unsigned*)((const char*)pws + WS_EPOCH) + 1u;
  v4u cv0 = ld<AUX_NT>(rsC, (role && t < 160) ? (t / 20) * 1280 + kq * 320 + (t % 20) * 16 : OOB, 0);
  const unsigned wmoff = (unsigned)(160 * kq + 8 * w + (lane >> 3)) * 7680u + ta * 128 + (lane & 7) * 16;
  v4u wm0 = ld<AUX_NT>(rsWM, role ? wmoff : OOB, 0);
  v4u wm1 = ld<AUX_NT>(rsWM, role ? wmoff : OOB, 64 * 7680);
  v4u wm2 = ld<AUX_NT>(rsWM, (role && w < 4) ? wmoff : OOB, 128 * 7680);
  v4u xa = ld<AUX_NT>(rsX, w * 1280 + lane * 16, 0);
  unsigned xb = __builtin_amdgcn_raw_buffer_load_b32(rsX, w * 1280 + 1024 + lane * 4, 0, AUX_NT);
  v2u xh = __builtin_amdgcn_raw_buffer_load_b64(rsX, lane < 20 ? w * 1280 + 160 * b + lane * 8 : OOB, 0, AUX_NT);
  v2u xown = __builtin_amdgcn_raw_buffer_load_b64(rsX, (w == 4 && lane < 5) ? b * 1280 + 40 * h + lane * 8 : OOB, 0, AUX_NT);
  if (t == 0) lds_st<v2u>(L, L_FLAG, v2u{0u, 0u});
  SB();
  asm volatile("s_barrier" ::: "memory");
  v4u WQ[3], WO[20], KB[NR], KL[TW + 1];
  const unsigned offWQ = NOW ? OOB : (unsigned)(80 * b + 4 * w + g) * 8192u + h * 256 + c * 16;
  const unsigned offWO = NOW ? OOB : (unsigned)(h * 128 + 16 * w + 4 * g) * 1280u + c * 16;
  const unsigned offKV = w * 1024 + lane * 16;
  const int ksoff = L_KS + w * (NLDS * 1024) + lane * 16;
  auto ldwo = [&](auto ic) AINL { constexpr int i = decltype(ic)::value; WO[i] = ld<AUX_W>(rsWO, offWO, (i / 5) * 1280 + (i % 5) * 256); };
  auto a_issue = [&](auto mc) AINL {
    constexpr It it = a_item(decltype(mc)::value);
    if constexpr (it.kind == 4) WQ[it.idx] = ld<AUX_NT>(rsWQ, (it.idx < 2 || w < 4) ? offWQ : OOB, it.idx * 32 * 8192);
    else if constexpr (it.kind == 2) ldwo(IC<it.idx>{});
    else if constexpr (it.idx < NR) KB[it.idx] = ld<AUX_K>(rsK, offKV, it.idx * 8192);
    else KL[(it.idx - NR) % (TW + 1)] = ld<AUX_K>(rsK, offKV, it.idx * 8192);
  };
  constexpr int NINIT0 = 2, NINIT = TW;
  sfor<0, NINIT0>(a_issue);
  SB();
  STAMP(1);
  {
    auto silu8 = [&](v4u v) AINL {
      v4u r;
#pragma unroll
      for (int d = 0; d < 4; ++d) r[d] = cvtpk(fsilu(bflo(v[d])), fsilu(bfhi(v[d])));
      return r;
    };
    if (t < 160) lds_st<v4u>(L, L_SC + (t / 20) * SC_STRIDE + (t % 20) * 16, silu8(cv0));
    lds_st<v4u>(L, L_WL + w * 1024 + lane * 16, wm0);
    lds_st<v4u>(L, L_WL + (w + 8) * 1024 + lane * 16, wm1);
    if (w < 4) lds_st<v4u>(L, L_WL + (w + 16) * 1024 + lane * 16, wm2);
  }
  SB();
  sfor<NINIT0, NINIT>(a_issue);
  SB();
  BAR();
  STAMP(2);
  {
    f4 acc = {0.f, 0.f, 0.f, 0.f};
    const int tl = w & 3, s0_ = (w >> 2) * 3;
    const int trbase = L_WL + (8 * g + ((lane & 15) >> 2)) * 128 + tl * 32 + (lane & 3) * 8;
#pragma unroll
    for (int n = 0; n < 3; ++n) {
      if (n < 2 || w < 4) {
        const int s = s0_ + n;
        const v4u a = lds_ld<v4u>(L, L_SC + (c & 7) * SC_STRIDE + s * 64 + g * 16);
        const s4 b0 = __builtin_amdgcn_ds_read_tr16_b64_v4i16((LAS s4*)(L + trbase + s * 4096));
        const s4 b1 = __builtin_amdgcn_ds_read_tr16_b64_v4i16((LAS s4*)(L + trbase + s * 4096 + 512));
        const v2u u0 = __builtin_bit_cast(v2u, b0), u1 = __builtin_bit_cast(v2u, b1);
        acc = mfma(a, v4u{u0.x, u0.y, u1.x, u1.y}, acc);
      }
    }
    if (g < 2) {
#pragma unroll
      for (int r = 0; r < 4; ++r) lds_st<float>(L, L_PART + (w >> 2) * 2048 + (4 * g + r) * 256 + (16 * tl + c) * 4, acc[r]);
    }
  }
  BAR();
  if (t < 128 && role) {
    const f4 s = lds_ld<f4>(L, L_PART + t * 16) + lds_ld<f4>(L, L_PART + 2048 + t * 16);
    stsc(rsWS, WS_PM + (((t >> 4) * 4 + kq) * 3840 + ta * 64) * 4 + (t & 15) * 16, tag4(s, T));
  }
  STAMP(3);
  float xmean, xrden;
  {
    float xv[10];
#pragma unroll
    for (int d = 0; d < 4; ++d) { xv[2 * d] = bflo(xa[d]); xv[2 * d + 1] = bfhi(xa[d]); }
    xv[8] = bflo(xb); xv[9] = bfhi(xb);
    float s = 0.f;
#pragma unroll
    for (int d = 0; d < 10; ++d) s += xv[d];
    const float mean = wave_sum(s) * (1.f / 640.f);
    float q2 = 0.f;
#pragma unroll
    for (int d = 0; d < 10; ++d) { const float dd = xv[d] - mean; q2 += dd * dd; }
    const float var = rbf(wave_sum(q2) * (1.f / 640.f));
    xmean = rbf(mean); xrden = frcp(rbf(__builtin_amdgcn_sqrtf(rbf(var + 1e-5f))));
    if (w == 4 && lane < 5) lds_st<v2u>(L, L_XR + lane * 8, xown);
  }
  float lnv[4];
  {
    float a0 = bflo(xh.x) - xmean, a1 = bfhi(xh.x) - xmean, a2 = bflo(xh.y) - xmean, a3 = bfhi(xh.y) - xmean;
    rbf2(a0, a1); rbf2(a2, a3);
    a0 *= xrden; a1 *= xrden; a2 *= xrden; a3 *= xrden;
    rbf2(a0, a1); rbf2(a2, a3);
    lnv[0] = a0; lnv[1] = a1; lnv[2] = a2; lnv[3] = a3;
  }
  Sel S;
  {
    const int e = c & 7;
#pragma unroll
    for (int par = 0; par < 2; ++par)
#pragma unroll
      for (int d = 0; d < 4; ++d) {
        const unsigned lo = (unsigned)(2 * par) | ((unsigned)(2 * par + 1) << 8);
        S.s[par][d] = (d == (e >> 1)) ? ((e & 1) ? (0x0c0cu | (lo << 16)) : (0x0c0c0000u | lo)) : 0x0c0c0c0cu;
      }
  }
  unsigned po[4]; v4u pvv[4];
  unsigned long long pm0, pm1, pm2, pm3;
  auto pmasks = [&]() AINL { pm0 = ballot(po[0] != OOB); pm1 = ballot(po[1] != OOB); pm2 = ballot(po[2] != OOB); pm3 = ballot(po[3] != OOB); };
#pragma unroll
  for (int q = 0; q < 4; ++q) po[q] = lane < 40 ? WS_PM + ((w * 4 + q) * 3840 + (lane / 20) * 640 + 80 * b + 4 * (lane % 20)) * 4 : OOB;
  pmasks();
  auto pissue = [&]() AINL {
#pragma unroll
    for (int q = 0; q < 4; ++q) pvv[q] = ldsc(rsWS, po[q]);
  };
  auto pok = [&]() AINL {
    return (ballot(tagok4(pvv[0], T)) & pm0) == pm0 && (ballot(tagok4(pvv[1], T)) & pm1) == pm1 && (ballot(tagok4(pvv[2], T)) & pm2) == pm2 && (ballot(tagok4(pvv[3], T)) & pm3) == pm3;
  };
  auto preload = [&]() AINL {
#pragma unroll
    for (int q = 0; q < 4; ++q) { const bool k1 = tagok4(pvv[q], T); const v4u nv = ldsc(rsWS, k1 ? OOB : po[q]); pvv[q] = k1 ? pvv[q] : nv; }
  };
  auto pblock = [&]() AINL {
    unsigned spins = 0;
    while (!pok() && ++spins < SPIN_CAP) { __builtin_amdgcn_s_sleep(2); FENCE(); preload(); }
  };
  auto psum = [&]() AINL {
    f4 mv;
#pragma unroll
    for (int e = 0; e < 4; ++e) mv[e] = (__uint_as_float(pvv[0][e] & 0xffffff00u) + __uint_as_float(pvv[1][e] & 0xffffff00u)) +
                                        (__uint_as_float(pvv[2][e] & 0xffffff00u) + __uint_as_float(pvv[3][e] & 0xffffff00u));
    float a0 = mv[0], a1 = mv[1], a2 = mv[2], a3 = mv[3]; rbf2(a0, a1); rbf2(a2, a3);
    return f4{a0, a1, a2, a3};
  };
  bool pdone = false, hdone = false, mdone = false;
  auto rprocess = [&]() AINL {
    const f4 mv = psum();
    float hv[4];
#pragma unroll
    for (int e = 0; e < 4; ++e) {
      const float ca = __uint_as_float(__builtin_amdgcn_ds_bpermute(((lane + 20) & 63) * 4, (int)__float_as_uint(mv[e])));
      hv[e] = rbf(lnv[e] * rbf(1.f + ca)) + mv[e];
    }
    if (lane < 20) lds_st<v2u>(L, L_HB + w * 160 + lane * 8, v2u{cvtpk(hv[0], hv[1]), cvtpk(hv[2], hv[3])});
    asm volatile("s_waitcnt lgkmcnt(0)" ::: "memory");
    if (lane == 0) __hip_atomic_fetch_add((LAS unsigned*)(L + L_FLAG), 1u, __ATOMIC_RELAXED, __HIP_MEMORY_SCOPE_WORKGROUP);
    STAMP(14);
  };
  auto mprocess = [&]() AINL {
    const f4 mv = psum();
    v4u msg;
#pragma unroll
    for (int e = 0; e < 4; ++e) msg[e] = (__float_as_uint(rbf(lane >= 10 ? 1.f + mv[e] : mv[e])) & 0xffff0000u) | ((T >> (8 * e)) & 0xffu);
    if (lane >= 5 && lane < 15) stsc(rsWS, WS_AGP + (b * 32 + h) * 256 + lane * 16, msg);
    else if (lane < 5) lds_st<f4>(L, L_GAB + lane * 16, mv);
    else if (lane < 20) lds_st<f4>(L, L_GAB + 80 + (lane - 15) * 16, mv);
    STAMP(17);
  };
  auto bfpack = [&](v4u v) AINL { return v2u{__builtin_amdgcn_perm(v.y, v.x, 0x07060302u), __builtin_amdgcn_perm(v.w, v.z, 0x07060302u)}; };
  auto hflag = [&]() AINL { FENCE(); return __builtin_amdgcn_readfirstlane((int)*(volatile LAS unsigned*)(L + L_FLAG)) == 8; };
  auto hstep = [&]() AINL {
    f4 acc[4];
#pragma unroll
    for (int pi = 0; pi < 4; ++pi) acc[pi] = f4{0.f, 0.f, 0.f, 0.f};
#pragma unroll
    for (int n = 0; n < 3; ++n) {
      if (n < 2 || w < 4) {
#pragma unroll
        for (int pi = 0; pi < 4; ++pi) {
          const unsigned X = lds_ld<unsigned short>(L, L_HB + (2 * pi + (c >> 3)) * 160 + (4 * (w + 8 * n) + g) * 2);
          acc[pi] = mfma(diagA(X, S, 0), WQ[n], acc[pi]);
        }
      }
    }
#pragma unroll
    for (int pi = 0; pi < 4; ++pi) lds_st<f4>(L, L_QP8 + w * 4096 + (2 * pi + (g >> 1)) * 512 + (8 * c + 4 * (g & 1)) * 4, acc[pi]);
    asm volatile("s_waitcnt lgkmcnt(0)" ::: "memory");
    unsigned old = 0;
    if (lane == 0) old = __hip_atomic_fetch_add((LAS unsigned*)(L + L_FLAG + 4), 1u, __ATOMIC_RELAXED, __HIP_MEMORY_SCOPE_WORKGROUP);
    if (__builtin_amdgcn_readfirstlane((int)old) == 7) {
      FENCE();
#pragma unroll
      for (int n = 0; n < 4; ++n) {
        const int o = L_QP8 + (64 * n + lane) * 16;
        const f4 s0 = lds_ld<f4>(L, o) + lds_ld<f4>(L, o + 4096), s1 = lds_ld<f4>(L, o + 2 * 4096) + lds_ld<f4>(L, o + 3 * 4096);
        const f4 s2 = lds_ld<f4>(L, o + 4 * 4096) + lds_ld<f4>(L, o + 5 * 4096), s3 = lds_ld<f4>(L, o + 6 * 4096) + lds_ld<f4>(L, o + 7 * 4096);
        stsc(rsWS, WS_QPM + (h * 8 + b) * 4096 + (64 * n + lane) * 16, tag4((s0 + s1) + (s2 + s3), T));
      }
      STAMP(16);
    }
  };
  constexpr int CLRPOS = NINIT + 2;
  static_assert(a_kpos(RK) >= NINIT && RK + RN * RD < MK && HK + (HN - 1) * RD < QK && RK + RN * RD < HK + (HN - 1) * RD + 1 && MK + 9 < 32 && QK + 12 < 32, "stage order");
  sfor<NINIT, NA>([&](auto mc) AINL {
    constexpr int m = decltype(mc)::value;
    constexpr It it = a_item(m);
    constexpr int k = it.kind == 0 ? it.idx : -100;
    WAITVM(TW - 1);
    {
      constexpr It ol = a_item(m - TW);
      if constexpr (ol.kind == 0 && ol.idx >= NR) lds_st<v4u>(L, ksoff + (ol.idx - NR) * 1024, KL[(ol.idx - NR) % (TW + 1)]);
    }
    a_issue(mc);
    if constexpr (m == CLRPOS) {
      const v4u clr = tag4(f4{0.f, 0.f, 0.f, 0.f}, T - 1u);
      if (t < 256) { stsc(rsWS, WS_OUT1 + wg * 4096 + t * 16, clr); stsc(rsWS, WS_QPM + (h * 8 + b) * 4096 + t * 16, clr); }
      else stsc(rsWS, WS_OUT2 + wg * 4096 + (t - 256) * 16, clr);
      if (w == 5) {
        if (lane < 8) stsc(rsWS, WS_AG + (b * 32 + h) * 128 + lane * 16, clr);
        else if (lane < 24) stsc(rsWS, WS_AGP + (b * 32 + h) * 256 + (lane - 8) * 16, clr);
        else if (lane == 24) stsc(rsWS, WS_ALIVE + wg * 16, tag4(f4{0.f, 0.f, 0.f, 0.f}, T));
      }
    }
    if constexpr (k == RK) pissue();
    if constexpr (k > RK && k < RK + RN * RD && (k - RK) % RD == 0) { if (!pdone) { if (pok()) { rprocess(); pdone = true; } else preload(); } }
    if constexpr (k == RK + RN * RD) { if (!pdone) { pblock(); rprocess(); } }
    if constexpr (k >= HK && k < HK + (HN - 1) * RD && (k - HK) % RD == 0) { if (!hdone && hflag()) { hstep(); hdone = true; } }
    if constexpr (k == HK + (HN - 1) * RD) {
      if (!hdone) { unsigned spins = 0; while (!hflag() && ++spins < (SPIN_CAP << 3)) __builtin_amdgcn_s_sleep(1); hstep(); }
    }
    if constexpr (k == MK) {
      if (w == 5) {
#pragma unroll
        for (int q = 0; q < 4; ++q) po[q] = lane < 20 ? WS_PM + ((b * 4 + q) * 3840 + (2 + lane / 5) * 640 + 20 * h + 4 * (lane % 5)) * 4 : OOB;
        pmasks(); pissue();
      }
    }
    if constexpr (k == MK + 3 || k == MK + 6) { if (w == 5 && !mdone) { if (pok()) { mprocess(); mdone = true; } else preload(); } }
    if constexpr (k == MK + 9) { if (w == 5 && !mdone) { pblock(); mprocess(); } }
    if constexpr (k == QK) {
      if (w == 6 || w == 4) {
#pragma unroll
        for (int q = 0; q < 4; ++q)
          po[q] = w == 6 ? WS_QPM + ((h * 8 + 2 * q + (lane >> 5)) * 8 + b) * 512 + (lane & 31) * 16 : (wg == 0 ? WS_ALIVE + (64 * q + lane) * 16 : OOB);
        pmasks(); pissue();
      }
    }
    if constexpr (k == QK + 3 || k == QK + 6 || k == QK + 9 || k == QK + 12) { if ((w == 6 || w == 4) && !pok()) preload(); }
    SB();
  });
  STAMP(4);
  v4u W1[10], W2[10], VR[VRN];
  const unsigned offW1 = (mlp && !NOW) ? (unsigned)(80 * w + 40 * (c >> 3) + 10 * g) * 3456u + (64 * h + 8 * (c & 7)) * 2 : OOB;
  const unsigned offW2 = (mlp && !NOW) ? (unsigned)(64 * h + 8 * w + 2 * g) * 1280u + c * 16 : OOB;
  auto b_issue = [&](auto mc) AINL {
    constexpr It it = b_item(decltype(mc)::value);
    if constexpr (it.kind == 1) VR[it.idx % VRN] = ld<AUX_V>(rsV, offKV, it.idx * 8192);
    else if constexpr (it.kind == 2) ldwo(IC<it.idx>{});
    else W1[it.idx] = ld<AUX_W>(rsW1, offW1, it.idx * 3456);
    SB();
  };
#define VTICK(k) do { WAITVM(TW - 1); b_issue(IC<(k)>{}); } while (0)
  sfor<0, TW>([&](auto kc) AINL { WAITVM(TW - 1); b_issue(kc); });
  if (w == 6 || w == 4) {
    pblock();
    if (w == 6) {
      f4 q4;
#pragma unroll
      for (int e = 0; e < 4; ++e) q4[e] = x32_sum((__uint_as_float(pvv[0][e] & 0xffffff00u) + __uint_as_float(pvv[1][e] & 0xffffff00u)) +
                                                  (__uint_as_float(pvv[2][e] & 0xffffff00u) + __uint_as_float(pvv[3][e] & 0xffffff00u)));
      if (lane < 32) lds_st<v2u>(L, L_QB + lane * 8, v2u{cvtpk(q4[0], q4[1]), cvtpk(q4[2], q4[3])});
    } else {
      if (wg == 0 && lane == 0) __builtin_amdgcn_raw_buffer_store_b32(T, rsWS, WS_EPOCH, 0, AUX_SC1);
    }
  }
  BAR();
  STAMP(5);
  const v4u qr = lds_ld<v4u>(L, L_QB + c * 16);
  STAMP(6);
  auto kdot = [&](v4u k) AINL { return dot2(k.w, qr[3], dot2(k.z, qr[2], dot2(k.y, qr[1], dot2(k.x, qr[0], 0.f)))); };
  auto treduce = [&](float (&xs)[16]) AINL {
    float x[8];
#pragma unroll
    for (int i = 0; i < 8; ++i) { const float lo = xs[i] + dppf<XOR8>(xs[i]), hi = xs[i + 8] + dppf<XOR8>(xs[i + 8]); x[i] = (c & 8) ? hi : lo; }
#pragma unroll
    for (int i = 0; i < 4; ++i) { const float lo = x[i] + dppf<XOR7>(x[i]), hi = x[i + 4] + dppf<XOR7>(x[i + 4]); x[i] = (c & 4) ? hi : lo; }
#pragma unroll
    for (int i = 0; i < 2; ++i) { const float lo = x[i] + dppf<XOR2>(x[i]), hi = x[i + 2] + dppf<XOR2>(x[i + 2]); x[i] = (c & 2) ? hi : lo; }
    const float lo = x[0] + dppf<XOR1>(x[0]), hi = x[1] + dppf<XOR1>(x[1]);
    return (c & 1) ? hi : lo;
  };
  constexpr float C2 = 0.08838834764831845f * 1.4426950408889634f;
  float mw, fw = 0.f;
  unsigned ppk;
  {
    float xs[16];
#pragma unroll
    for (int j = 0; j < 16; ++j) xs[j] = kdot(KB[j]);
    SB();
    VTICK(TW);
    const float s0 = treduce(xs);
    SB();
    VTICK(TW + 1);
#pragma unroll
    for (int j = 0; j <= LASTPARK - NR; ++j) xs[j] = kdot(lds_ld<v4u>(L, ksoff + j * 1024));
#pragma unroll
    for (int j = LASTPARK - NR + 1; j < 16; ++j) xs[j] = kdot(KL[j % (TW + 1)]);
    SB();
    sfor<TW + 2, VRN>([&](auto kc) AINL { WAITVM(TW - 1); b_issue(kc); });
    const float s1 = treduce(xs);
    SB();
    sfor<VRN, VRN + WEARLY>([&](auto kc) AINL { WAITVM(TW - 1); b_issue(kc); });
    mw = wave_max(fmaxf(s0, s1));
    const float p0 = __builtin_amdgcn_exp2f((s0 - mw) * C2), p1 = __builtin_amdgcn_exp2f((s1 - mw) * C2);
    const float lw = wave_sum(p0 + p1);
    ppk = cvtpk(p0, p1);
    if (lane == 0) lds_st<v2u>(L, L_ML + w * 8, v2u{__float_as_uint(mw), __float_as_uint(lw)});
  }
  STAMP(15);
  f4 oacc = {0.f, 0.f, 0.f, 0.f};
  auto pvstep = [&](auto jc) AINL {
    constexpr int j = decltype(jc)::value;
    const unsigned X = dppu<0x150 + (j & 15)>(ppk);
    oacc = mfma(diagA(X, S, j >> 4), VR[j % VRN], oacc);
    if constexpr (j == 4) {
      if (w == 1 || w == 2) {
#pragma unroll
        for (int q = 0; q < 4; ++q) { const int idx = 64 * q + lane; po[q] = (mlp && idx < 160) ? WS_AGP + (b * 32 + idx / 5) * 256 + (5 * w + idx % 5) * 16 : OOB; }
        pmasks(); pissue();
      }
    }
    if constexpr (j == 14) {
      if (w == 1 || w == 2) {
        pblock();
#pragma unroll
        for (int n = 0; n < 3; ++n) {
          const int idx = 64 * n + lane;
          if (po[n] != OOB) lds_st<v2u>(L, (w == 1 ? L_SB : L_CB) + (20 * (idx / 5) + 4 * (idx % 5)) * 2, bfpack(pvv[n]));
        }
      }
    }
    if constexpr (j == 10) {
      BAR();
      float M = -INFINITY;
      v2u ml[8];
#pragma unroll
      for (int ww = 0; ww < 8; ++ww) { ml[ww] = lds_ld<v2u>(L, L_ML + ww * 8); M = fmaxf(M, __uint_as_float(ml[ww].x)); }
      float Ls = 0.f;
#pragma unroll
      for (int ww = 0; ww < 8; ++ww) Ls += __builtin_amdgcn_exp2f((__uint_as_float(ml[ww].x) - M) * C2) * __uint_as_float(ml[ww].y);
      fw = __builtin_amdgcn_exp2f((mw - M) * C2) * frcp(Ls);
      PIN(fw);
    }
  };
  sfor<VRN + WEARLY, NB>([&](auto mc) AINL {
    constexpr int m = decltype(mc)::value;
    constexpr It it = b_item(m);
    if constexpr (it.kind == 1) pvstep(IC<it.idx - VRN>{});
    if constexpr (it.kind != 3) WAITVM(TW - 1);
    else if constexpr (it.idx < VRN) { pvstep(IC<32 - VRN + it.idx>{}); }
    b_issue(mc);
  });
  sfor<(VRN > 10 ? 32 - VRN + 10 : 32), 32>([&](auto jc) AINL { pvstep(jc); SB(); });
  STAMP(7);
  {
    const f4 ov = oacc * fw;
    if (lane < 32) lds_st<f4>(L, L_OP + w * 512 + (8 * c + 4 * g) * 4, ov);
  }
  BAR();
  const int ypoff = L_YP + w * 2560 + (8 * c + 4 * (g & 1)) * 4;
  {
    const int kap = (c >> 1) & 3, sig = (c & 1) + 2 * (c >> 3);
    const int ko = (16 * w + 4 * g + kap) * 4;
    float o = lds_ld<float>(L, L_OP + (2 * sig) * 512 + ko) + lds_ld<float>(L, L_OP + (2 * sig + 1) * 512 + ko);
    o += dppf<XOR1>(o); o += dppf<XOR8>(o);
    const unsigned P = cvtpk(dppf<QB0>(o), dppf<QB2>(o)), Q = dppu<XOR7>(P);
    const unsigned X0 = (c & 4) ? Q : P, X1 = (c & 4) ? P : Q;
    f4 y[5];
    { const v4u A = diagA(X0, S, 0);
#pragma unroll
      for (int cb = 0; cb < 5; ++cb) y[cb] = mfma(A, WO[cb], f4{0.f, 0.f, 0.f, 0.f}); }
    { const v4u A = diagA(X0, S, 1);
#pragma unroll
      for (int cb = 0; cb < 5; ++cb) y[cb] = mfma(A, WO[5 + cb], y[cb]); }
    { const v4u A = diagA(X1, S, 0);
#pragma unroll
      for (int cb = 0; cb < 5; ++cb) y[cb] = mfma(A, WO[10 + cb], y[cb]); }
    { const v4u A = diagA(X1, S, 1);
#pragma unroll
      for (int cb = 0; cb < 5; ++cb) y[cb] = mfma(A, WO[15 + cb], y[cb]); }
    if (lane < 32) {
#pragma unroll
      for (int cb = 0; cb < 5; ++cb) lds_st<f4>(L, ypoff + cb * 512, y[cb]);
    }
  }
  BAR();
  STAMP(8);
  auto publish = [&](int wsoff) AINL {
    const int k = t & 7, j = t >> 3;
    const int o = L_YP + (20 * j + 4 * (k < 5 ? k : 0)) * 4;
    f4 s0 = lds_ld<f4>(L, o) + lds_ld<f4>(L, o + 2560), s1 = lds_ld<f4>(L, o + 2 * 2560) + lds_ld<f4>(L, o + 3 * 2560);
    f4 s2 = lds_ld<f4>(L, o + 4 * 2560) + lds_ld<f4>(L, o + 5 * 2560), s3 = lds_ld<f4>(L, o + 6 * 2560) + lds_ld<f4>(L, o + 7 * 2560);
    stsc(rsWS, wsoff + t * 16, tag4((s0 + s1) + (s2 + s3), T));
  };
  float x1v[4] = {0.f, 0.f, 0.f, 0.f};
  auto rs_gather = [&](int wsbase, int nprod, int delay_done) AINL -> f4 {
    const int pl = lane >> 3, k = lane & 7;
    unsigned off[4]; v4u v[4];
#pragma unroll
    for (int q = 0; q < 4; ++q) {
      const int u = 8 * q + pl;
      const int wgu = (b << 5) | ((u & 3) << 3) | (u >> 2);
      off[q] = (k < 5 && u < nprod) ? wsbase + wgu * 4096 + h * 128 + k * 16 : OOB;
      PIN(off[q]);
    }
    const unsigned long long m0 = ballot(off[0] != OOB), m3 = ballot(off[3] != OOB);
    __builtin_amdgcn_s_sleep(0);
    (void)delay_done;
#pragma unroll
    for (int q = 0; q < 4; ++q) v[q] = ldsc(rsWS, off[q]);
    unsigned spins = 0;
    for (;;) {
      const bool k0 = tagok4(v[0], T), k1 = tagok4(v[1], T), k2 = tagok4(v[2], T), k3 = tagok4(v[3], T);
      if ((ballot(k0) & ballot(k1) & ballot(k2) & m0) == m0 && (ballot(k3) & m3) == m3) break;
      if (++spins > SPIN_CAP) break;
      __builtin_amdgcn_s_sleep(POLL_SLEEP);
      FENCE();
      const v4u n0 = ldsc(rsWS, k0 ? OOB : off[0]), n1 = ldsc(rsWS, k1 ? OOB : off[1]), n2 = ldsc(rsWS, k2 ? OOB : off[2]), n3 = ldsc(rsWS, k3 ? OOB : off[3]);
      v[0] = k0 ? v[0] : n0; v[1] = k1 ? v[1] : n1; v[2] = k2 ? v[2] : n2; v[3] = k3 ? v[3] : n3;
    }
    f4 s;
#pragma unroll
    for (int e = 0; e < 4; ++e) {
      float a = (__uint_as_float(v[0][e] & 0xffffff00u) + __uint_as_float(v[1][e] & 0xffffff00u)) + (__uint_as_float(v[2][e] & 0xffffff00u) + __uint_as_float(v[3][e] & 0xffffff00u));
      a += dppf<XOR8>(a); s[e] = x32_sum(x16_sum(a));
    }
    return s;
  };
  if (w < 4) publish(WS_OUT1 + wg * 4096);
  if (w != 4) {
#pragma unroll
    for (int i = 0; i < 10; ++i) W2[i] = ld<AUX_W>(rsW2, offW2, (i / 5) * 1280 + (i % 5) * 256);
  }
  if (w == 4) {
    const v2u xr = lds_ld<v2u>(L, L_XR + (lane & 7) * 8); const f4 gg = lds_ld<f4>(L, L_GAB + (lane & 7) * 16);
    unsigned off[3]; v4u v[3]; float sbv[12], cbv[12]; int hoff[3];
#pragma unroll
    for (int q = 0; q < 3; ++q) {
      const int idx = 64 * q + lane, jj = idx / 5, k = idx % 5;
      off[q] = (idx < 160 && mlp) ? WS_AG + (b * 32 + jj) * 128 + k * 16 : OOB;
      const int col = idx < 160 ? 20 * jj + 4 * k : 0;
      const v2u s2 = lds_ld<v2u>(L, L_SB + col * 2), c2 = lds_ld<v2u>(L, L_CB + col * 2);
      sbv[4 * q] = bflo(s2.x); sbv[4 * q + 1] = bfhi(s2.x); sbv[4 * q + 2] = bflo(s2.y); sbv[4 * q + 3] = bfhi(s2.y);
      cbv[4 * q] = bflo(c2.x); cbv[4 * q + 1] = bfhi(c2.x); cbv[4 * q + 2] = bflo(c2.y); cbv[4 * q + 3] = bfhi(c2.y);
      hoff[q] = L_H2 + col * 2;
      PIN(off[q]); PIN(hoff[q]);
    }
#pragma unroll
    for (int i = 0; i < 12; ++i) { PIN(sbv[i]); PIN(cbv[i]); }
    float xx[4] = {bflo(xr.x), bfhi(xr.x), bflo(xr.y), bfhi(xr.y)};
    const unsigned long long am0 = ballot(off[0] != OOB), am2 = ballot(off[2] != OOB);
    __builtin_amdgcn_s_sleep(D_RS1);
    const f4 s = rs_gather(WS_OUT1, 32, 0);
    v4u m;
    {
      float a0 = s[0], a1 = s[1], a2 = s[2], a3 = s[3];
      rbf2(a0, a1); rbf2(a2, a3);
      a0 *= gg[0]; a1 *= gg[1]; a2 *= gg[2]; a3 *= gg[3];
      rbf2(a0, a1); rbf2(a2, a3);
      a0 += xx[0]; a1 += xx[1]; a2 += xx[2]; a3 += xx[3];
      const unsigned p01 = cvtpk(a0, a1), p23 = cvtpk(a2, a3);
      m = v4u{(p01 << 16) | (T & 0xffu), (p01 & 0xffff0000u) | ((T >> 8) & 0xffu), (p23 << 16) | ((T >> 16) & 0xffu), (p23 & 0xffff0000u) | (T >> 24)};
      x1v[0] = bflo(p01); x1v[1] = bfhi(p01); x1v[2] = bflo(p23); x1v[3] = bfhi(p23);
    }
    if (lane < 5) stsc(rsWS, WS_AG + (b * 32 + h) * 128 + lane * 16, m);
#pragma unroll
    for (int i = 0; i < 10; ++i) W2[i] = ld<AUX_W>(rsW2, offW2, (i / 5) * 1280 + (i % 5) * 256);
    STAMP(9);
    if (mlp) {
      __builtin_amdgcn_s_sleep(D_AG);
#pragma unroll
      for (int q = 0; q < 3; ++q) v[q] = ldsc(rsWS, off[q]);
      unsigned spins = 0;
      for (;;) {
        const bool k0 = tagok4(v[0], T), k1 = tagok4(v[1], T), k2 = tagok4(v[2], T);
        if ((ballot(k0) & ballot(k1) & am0) == am0 && (ballot(k2) & am2) == am2) break;
        if (++spins > SPIN_CAP) break;
        __builtin_amdgcn_s_sleep(POLL_SLEEP);
        FENCE();
        const v4u n0 = ldsc(rsWS, k0 ? OOB : off[0]), n1 = ldsc(rsWS, k1 ? OOB : off[1]), n2 = ldsc(rsWS, k2 ? OOB : off[2]);
        v[0] = k0 ? v[0] : n0; v[1] = k1 ? v[1] : n1; v[2] = k2 ? v[2] : n2;
      }
      STAMP(13);
      float xv[12]; float sm = 0.f;
#pragma unroll
      for (int q = 0; q < 3; ++q)
#pragma unroll
        for (int e = 0; e < 4; ++e) { xv[4 * q + e] = __uint_as_float(v[q][e] & 0xffff0000u); sm += xv[4 * q + e]; }
      const float mean = wave_sum(sm) * (1.f / 640.f);
      float q2 = 0.f, q3 = 0.f;
#pragma unroll
      for (int i = 0; i < 8; ++i) { const float dd = xv[i] - mean; q2 += dd * dd; }
#pragma unroll
      for (int i = 8; i < 12; ++i) { const float dd = xv[i] - mean; q3 += dd * dd; }
      q2 += lane < 32 ? q3 : 0.f;
      const float var = rbf(wave_sum(q2) * (1.f / 640.f));
      const float mb = rbf(mean), rden = frcp(rbf(__builtin_amdgcn_sqrtf(rbf(var + 1e-5f))));
#pragma unroll
      for (int q = 0; q < 3; ++q) {
        float a[4];
#pragma unroll
        for (int e = 0; e < 4; ++e) a[e] = xv[4 * q + e] - mb;
        rbf2(a[0], a[1]); rbf2(a[2], a[3]);
#pragma unroll
        for (int e = 0; e < 4; ++e) a[e] *= rden;
        rbf2(a[0], a[1]); rbf2(a[2], a[3]);
#pragma unroll
        for (int e = 0; e < 4; ++e) a[e] *= cbv[4 * q + e];
        rbf2(a[0], a[1]); rbf2(a[2], a[3]);
        const v2u o = {cvtpk(a[0] + sbv[4 * q], a[1] + sbv[4 * q + 1]), cvtpk(a[2] + sbv[4 * q + 2], a[3] + sbv[4 * q + 3])};
        if (q < 2 || lane < 32) lds_st<v2u>(L, hoff[q], o);
      }
    }
  }
  if (mlp) {
    BAR();
    STAMP(10);
    {
      unsigned X[5];
#pragma unroll
      for (int d = 0; d < 5; ++d) X[d] = lds_ld<unsigned>(L, L_H2 + 160 * w + 80 * (c >> 3) + 20 * g + d * 4);
      f4 a0 = {0.f, 0.f, 0.f, 0.f}, a1 = {0.f, 0.f, 0.f, 0.f};
#pragma unroll
      for (int p = 0; p < 10; p += 2) { a0 = mfma(diagA(X[p >> 1], S, 0), W1[p], a0); a1 = mfma(diagA(X[p >> 1], S, 1), W1[p + 1], a1); }
      a0 += a1;
      if ((g >> 1) == (c >> 3)) lds_st<f4>(L, L_HP + (2 * w + (c >> 3)) * 256 + (8 * (c & 7) + 4 * (g & 1)) * 4, a0);
    }
    BAR();
    {
      const int kap = (c >> 2) & 1, sig = (c & 3) + 4 * (c >> 3);
      const int n = (8 * w + 2 * g + kap) * 4;
      float hd = lds_ld<float>(L, L_HP + (2 * sig) * 256 + n) + lds_ld<float>(L, L_HP + (2 * sig + 1) * 256 + n);
      hd += dppf<XOR1>(hd); hd += dppf<XOR2>(hd); hd += dppf<XOR8>(hd);
      hd = fsilu(rbf(hd));
      const float ot = dppf<XOR7>(hd);
      const unsigned X = kap ? cvtpk(ot, hd) : cvtpk(hd, ot);
      const v4u A0 = diagA(X, S, 0), A1 = diagA(X, S, 1);
      f4 y[5];
#pragma unroll
      for (int cb = 0; cb < 5; ++cb) y[cb] = mfma(A0, W2[cb], f4{0.f, 0.f, 0.f, 0.f});
#pragma unroll
      for (int cb = 0; cb < 5; ++cb) y[cb] = mfma(A1, W2[5 + cb], y[cb]);
      if (lane < 32) {
#pragma unroll
        for (int cb = 0; cb < 5; ++cb) lds_st<f4>(L, ypoff + cb * 512, y[cb]);
      }
    }
    BAR();
    STAMP(11);
    if (w < 4) publish(WS_OUT2 + wg * 4096);
  }
  if (w == 4) {
    const f4 gg = lds_ld<f4>(L, L_GAB + 80 + (lane & 7) * 16);
    if (!mlp) __builtin_amdgcn_s_sleep(48);
    __builtin_amdgcn_s_sleep(D_RS2);
    const f4 s = rs_gather(WS_OUT2, 27, 0);
    float a0 = s[0], a1 = s[1], a2 = s[2], a3 = s[3];
    rbf2(a0, a1); rbf2(a2, a3);
    a0 *= gg[0]; a1 *= gg[1]; a2 *= gg[2]; a3 *= gg[3];
    rbf2(a0, a1); rbf2(a2, a3);
    if (lane < 5) __builtin_amdgcn_raw_buffer_store_b64(v2u{cvtpk(x1v[0] + a0, x1v[1] + a1), cvtpk(x1v[2] + a2, x1v[3] + a3)}, rsOUT, b * 1280 + 40 * h + lane * 8, 0, 0);
    STAMP(12);
  }
}

extern "C" void kernel_launch(void* const* d_in, const int* in_sizes, int n_in, void* d_out, int out_size, void* d_ws, size_t ws_size,
                              hipStream_t stream) {
  fused_kernel<<<256, 512, 0, stream>>>(d_in[0], d_in[1], d_in[2], d_in[3], d_in[4], d_in[6], d_in[7], d_in[8], d_in[9], d_out, d_ws);
}
```

```cpp
#include <hip/hip_runtime.h>
#include <hip/hip_bf16.h>
#include <stdint.h>
#include <type_traits>

typedef unsigned v4u __attribute__((ext_vector_type(4)));
typedef unsigned v2u __attribute__((ext_vector_type(2)));
typedef float f4 __attribute__((ext_vector_type(4)));
typedef __bf16 bf8 __attribute__((ext_vector_type(8)));
typedef __bf16 bf2 __attribute__((ext_vector_type(2)));
typedef short s4 __attribute__((ext_vector_type(4)));
#define LAS __attribute__((address_space(3)))
typedef __amdgpu_buffer_rsrc_t rsrc_t;

#define AUX_K 2
#define AUX_V 2
#define AUX_W 0
constexpr int AUX_NT = 2, AUX_SC1 = 16;
constexpr unsigned OOB = 0x80000000u;

constexpr int WS_EPOCH = 0;
constexpr int WS_ALIVE = 4096;
constexpr int WS_AGP = 65536;
constexpr int WS_AG = 131072;
constexpr int WS_PM = 262144;
constexpr int WS_OUT1 = 1 << 20;
constexpr int WS_OUT2 = 2 << 20;
constexpr int WS_QPM = 3 << 20;
constexpr int WS_STAMP = 4 << 20;
constexpr int WS_BYTES = 8 << 20;

#define TW 6
#define DA_LO 1
#define DA_HI 1
#define DB_LO 1
#define DB_HI 0
#define VRN 10
constexpr int NR = 16;
#define RK 7
#define RD 4
#define RN 3
#define HK 11
#define HD 2
#define HN 5
#define QK 21
struct It { int kind, idx; };
#define WOA 10
constexpr int NA = 3 + 32 + WOA;
constexpr It a_item(int m) {
  int pos = 0, wn = 0;
  for (int k = 0; k < 32; ++k) {
    if (pos == m) return It{0, k};
    ++pos;
    if (k >= 4 && k < 7) { if (pos == m) return It{4, k - 4}; ++pos; }
    else if (k >= 7 && (k - 7) * WOA / 24 != (k - 6) * WOA / 24 && wn < WOA) { if (pos == m) return It{2, wn}; ++pos; ++wn; }
  }
  return It{-1, 0};
}
static_assert(a_item(NA - 1).kind == 0 && a_item(NA - 1).idx == 31, "phase A list");
constexpr int a_kpos(int k) { for (int m = 0; m < NA; ++m) if (a_item(m).kind == 0 && a_item(m).idx == k) return m; return NA; }
constexpr bool a_pollk(int k) { return k == RK || k == RK + RD || k == RK + 2 * RD || k == QK || k == QK + RD || k == QK + 2 * RD; }
constexpr int a_polls_between(int lo, int hi) { int n = 0; for (int m = lo; m <= hi; ++m) if (m >= 0 && a_item(m).kind == 0 && a_pollk(a_item(m).idx)) ++n; return n; }
#define WEARLY 2
#define WLAST 27
constexpr int NBV = 32 + 20 - WOA, NB = NBV + 10;
constexpr It b_item(int m) {
  if (m < VRN) return It{1, m};
  if (m < VRN + WEARLY) return It{2, WOA + m - VRN};
  if (m >= NBV) return It{3, m - NBV};
  int pos = VRN + WEARLY, wn = WOA + WEARLY;
  for (int v = VRN; v < 32; ++v) {
    if (pos == m) return It{1, v};
    ++pos;
    int target = WOA + WEARLY + ((20 - WOA - WEARLY) * (v - VRN + 1) + (WLAST - VRN)) / (WLAST - VRN + 1);
    if (target > 20) target = 20;
    while (wn < target) { if (pos == m) return It{2, wn}; ++pos; ++wn; }
  }
  return It{-1, 0};
}
static_assert(b_item(NBV - 1).kind == 1 && b_item(NBV - 1).idx == 31, "phase B list");
constexpr int a_lastpark_f() { int k = NR - 1; for (int m = 0; m + TW < NA; ++m) if (a_item(m).kind == 0 && a_item(m).idx > k) k = a_item(m).idx; return k; }
constexpr int LASTPARK = a_lastpark_f();
constexpr int NLDS = LASTPARK - NR + 1;

constexpr int L_WL = 0;
constexpr int L_SC = 20480;
constexpr int SC_STRIDE = 336;
constexpr int L_PART = L_SC + 2688;
constexpr int L_WQL = 0;
constexpr int L_QP8 = 20480;
constexpr int L_HB = 32768;
constexpr int L_SB = L_HB + 1280;
constexpr int L_CB = L_SB + 1280;
constexpr int L_GAB = L_CB + 1280;
constexpr int L_XR = L_GAB + 160;
constexpr int L_ML = L_XR + 64;
constexpr int L_FLAG = L_ML + 64;
constexpr int L_QB = L_FLAG + 32;
constexpr int L_H2 = L_QB + 256;
constexpr int L_OP = L_H2 + 1280;
constexpr int L_HP = L_OP + 4096;
constexpr int L_KS = L_HP + 4096;
constexpr int L_YP = L_KS;
constexpr int L_END = L_KS + (8 * NLDS * 1024 > 20480 ? 8 * NLDS * 1024 : 20480);
static_assert(L_QP8 + 8192 <= 32768 && L_PART + 4096 <= 32768 && L_HB % 16 == 0 && L_QB % 16 == 0 && L_OP % 16 == 0 && L_KS % 16 == 0 && L_GAB % 16 == 0 && L_H2 % 16 == 0 && L_END <= 163840, "lds map");

#define D_RS1 8
#define D_AG 10
#define D_RS2 8
#define POLL_SLEEP 1
#define STAMPS 0
#define NODUTY 0
#define NOPARK 0
#define NOW 0
constexpr unsigned SPIN_CAP = NODUTY ? 4u : (1u << 15);

__device__ __forceinline__ float bflo(unsigned d) { return __uint_as_float(d << 16); }
__device__ __forceinline__ float bfhi(unsigned d) { return __uint_as_float(d & 0xffff0000u); }
__device__ __forceinline__ unsigned cvtpk(float lo, float hi) {
  bf2 r = {(__bf16)lo, (__bf16)hi}; return __builtin_bit_cast(unsigned, r);
}
__device__ __forceinline__ float rbf(float f) { return bflo(cvtpk(f, 0.f)); }
__device__ __forceinline__ void rbf2(float& a, float& b) { const unsigned p = cvtpk(a, b); a = bflo(p); b = bfhi(p); }
__device__ __forceinline__ float frcp(float x) { return __builtin_amdgcn_rcpf(x); }
__device__ __forceinline__ float fsilu(float a) { return a * frcp(1.f + __builtin_amdgcn_exp2f(a * -1.4426950408889634f)); }
template <int CTRL> __device__ __forceinline__ float dppf(float x) {
  return __builtin_bit_cast(float, __builtin_amdgcn_update_dpp(0, __builtin_bit_cast(int, x), CTRL, 0xf, 0xf, true));
}
template <int CTRL> __device__ __forceinline__ unsigned dppu(unsigned x) { return (unsigned)__builtin_amdgcn_update_dpp(0, (int)x, CTRL, 0xf, 0xf, true); }
constexpr int XOR1 = 0xB1, XOR2 = 0x4E, XOR7 = 0x141, XOR8 = 0x128, QB0 = 0x00, QB2 = 0xAA;
__device__ __forceinline__ float x16_sum(float x) { auto s = __builtin_amdgcn_permlane16_swap(__float_as_uint(x), __float_as_uint(x), false, false); return __uint_as_float(s[0]) + __uint_as_float(s[1]); }
__device__ __forceinline__ float x32_sum(float x) { auto s = __builtin_amdgcn_permlane32_swap(__float_as_uint(x), __float_as_uint(x), false, false); return __uint_as_float(s[0]) + __uint_as_float(s[1]); }
__device__ __forceinline__ float x16_max(float x) { auto s = __builtin_amdgcn_permlane16_swap(__float_as_uint(x), __float_as_uint(x), false, false); return fmaxf(__uint_as_float(s[0]), __uint_as_float(s[1])); }
__device__ __forceinline__ float x32_max(float x) { auto s = __builtin_amdgcn_permlane32_swap(__float_as_uint(x), __float_as_uint(x), false, false); return fmaxf(__uint_as_float(s[0]), __uint_as_float(s[1])); }
__device__ __forceinline__ float wave_sum(float x) {
  x += dppf<XOR1>(x); x += dppf<XOR2>(x); x += dppf<XOR7>(x); x += dppf<XOR8>(x); return x32_sum(x16_sum(x));
}
__device__ __forceinline__ float wave_max(float x) {
  x = fmaxf(x, dppf<XOR1>(x)); x = fmaxf(x, dppf<XOR2>(x)); x = fmaxf(x, dppf<XOR7>(x)); x = fmaxf(x, dppf<XOR8>(x)); return x32_max(x16_max(x));
}
__device__ __forceinline__ f4 mfma(v4u a, v4u b, f4 c) {
  return __builtin_amdgcn_mfma_f32_16x16x32_bf16(__builtin_bit_cast(bf8, a), __builtin_bit_cast(bf8, b), c, 0, 0, 0);
}
__device__ __forceinline__ float dot2(unsigned a, unsigned b, float c) {
  return __builtin_amdgcn_fdot2_f32_bf16(__builtin_bit_cast(bf2, a), __builtin_bit_cast(bf2, b), c, false);
}
__device__ __forceinline__ rsrc_t mkrs(const void* p, unsigned bytes) { return __builtin_amdgcn_make_buffer_rsrc((void*)p, 0, (int)bytes, 0x00020000); }
template <int AUX> __device__ __forceinline__ v4u ld(rsrc_t r, unsigned voff, unsigned soff) { return __builtin_amdgcn_raw_buffer_load_b128(r, (int)voff, (int)soff, AUX); }
__device__ __forceinline__ v4u ldsc(rsrc_t r, unsigned voff) { return __builtin_amdgcn_raw_buffer_load_b128(r, (int)voff, 0, AUX_SC1); }
__device__ __forceinline__ void stsc(rsrc_t r, unsigned voff, v4u v) { __builtin_amdgcn_raw_buffer_store_b128(v, r, (int)voff, 0, AUX_SC1); }
__device__ __forceinline__ void stsc2(rsrc_t r, unsigned voff, v2u v) { __builtin_amdgcn_raw_buffer_store_b64(v, r, (int)voff, 0, AUX_SC1); }
#define WAITVM(n) asm volatile("s_waitcnt vmcnt(%0)" ::"n"((n) > 63 ? 63 : (n)) : "memory")
#define WAITW(n, lo, hi) do { if ((lo) == (hi)) WAITVM((n) - (lo)); else if (w < 4) WAITVM((n) - (lo)); else WAITVM((n) - (hi)); } while (0)
#define BAR() asm volatile("s_waitcnt lgkmcnt(0)\n\ts_barrier" ::: "memory")
#define SB() __builtin_amdgcn_sched_barrier(0)
#define FENCE() asm volatile("" ::: "memory")
#define PIN(x) asm volatile("" : "+v"(x))

template <typename T> __device__ __forceinline__ T lds_ld(LAS unsigned char* L, int off) { return *(LAS T*)(L + off); }
template <typename T> __device__ __forceinline__ void lds_st(LAS unsigned char* L, int off, T v) { *(LAS T*)(L + off) = v; }
template <int I, int N, typename F> __device__ __forceinline__ void sfor(F&& f) {
  if constexpr (I < N) { f(std::integral_constant<int, I>{}); sfor<I + 1, N>(f); }
}
template <int I> using IC = std::integral_constant<int, I>;
#define AINL __attribute__((always_inline))

struct Sel { unsigned s[2][4]; };
__device__ __forceinline__ v4u diagA(unsigned X, const Sel& S, int par) {
  v4u a;
#pragma unroll
  for (int d = 0; d < 4; ++d) a[d] = __builtin_amdgcn_perm(X, X, S.s[par][d]);
  return a;
}
__device__ __forceinline__ v4u tag4(f4 v, unsigned T) {
  v4u r;
#pragma unroll
  for (int e = 0; e < 4; ++e) r[e] = ((__float_as_uint(v[e]) + 0x80u) & 0xffffff00u) | ((T >> (8 * e)) & 0xffu);
  return r;
}
__device__ __forceinline__ bool tagok4(v4u v, unsigned T) {
  const unsigned lo = __builtin_amdgcn_perm(v.y, v.x, 0x0c0c0400u), hi = __builtin_amdgcn_perm(v.w, v.z, 0x04000c0cu);
  return (lo | hi) == T;
}
__device__ __forceinline__ unsigned long long ballot(bool p) { return __builtin_amdgcn_ballot_w64(p); }

#define STAMP(n) do { } while (0)

__global__ void __launch_bounds__(512, 2)
fused_kernel(const void* px, const void* pk, const void* pv, const void* pwq,
             const void* pwo, const void* pw1, const void* pw2, const void* pc,
             const void* pwm, void* pout, void* pws) {
  __shared__ __attribute__((aligned(16))) unsigned char smem[L_END];
  LAS unsigned char* L = (LAS unsigned char*)smem;
  const int t = threadIdx.x, lane = t & 63, w = __builtin_amdgcn_readfirstlane(t >> 6);
  const int wg = blockIdx.x, xg = wg & 7, e8 = wg >> 3, b = wg >> 5, h = xg * 4 + (e8 & 3);
  const int g = lane >> 4, c = lane & 15;
  const bool mlp = h < 27;
  STAMP(0);
  const rsrc_t rsWS = mkrs(pws, WS_BYTES);
  const rsrc_t rsK = mkrs((const char*)pk + ((size_t)(b * 32 + h) * 4096 + 3072) * 256, 262144);
  const rsrc_t rsV = mkrs((const char*)pv + ((size_t)(b * 32 + h) * 4096 + 3072) * 256, 262144);
  const rsrc_t rsWQ = mkrs(pwq, 640 * 4096 * 2), rsWO = mkrs(pwo, 4096 * 640 * 2), rsW1 = mkrs(pw1, 640 * 1728 * 2), rsW2 = mkrs(pw2, 1728 * 640 * 2);
  const rsrc_t rsWM = mkrs(pwm, 640 * 3840 * 2), rsC = mkrs(pc, 10240), rsX = mkrs(px, 10240), rsOUT = mkrs(pout, 10240);

  const bool role = wg < 240;
  const int ta = wg % 60, kq = wg / 60;
  const unsigned T = *(const __attribute__((address_space(4))) unsigned*)((const char*)pws + WS_EPOCH) + 1u;
  v4u cv0 = ld<AUX_NT>(rsC, (role && t < 160) ? (t / 20) * 1280 + kq * 320 + (t % 20) * 16 : OOB, 0);
  const unsigned wmoff = (unsigned)(160 * kq + 8 * w + (lane >> 3)) * 7680u + ta * 128 + (lane & 7) * 16;
  v4u wm0 = ld<AUX_NT>(rsWM, role ? wmoff : OOB, 0);
  v4u wm1 = ld<AUX_NT>(rsWM, role ? wmoff : OOB, 64 * 7680);
  v4u wm2 = ld<AUX_NT>(rsWM, (role && w < 4) ? wmoff : OOB, 128 * 7680);
  if (t < 8) lds_st<unsigned>(L, L_FLAG + t * 4, 0u);
  SB();
  asm volatile("s_barrier" ::: "memory");
  v4u WQ[3], WO[20], KB[NR], KL[TW + 1];
  const unsigned offWQ = NOW ? OOB : (unsigned)(80 * b + 20 * g + w) * 8192u + h * 256 + c * 16;
  const unsigned offWO = NOW ? OOB : (unsigned)(h * 128 + 16 * w + 4 * g) * 1280u + c * 16;
  const unsigned offKV = w * 1024 + lane * 16;
  const int ksoff = L_KS + w * (NLDS * 1024) + lane * 16;
  auto ldwo = [&](auto ic) AINL { constexpr int i = decltype(ic)::value; WO[i] = ld<AUX_W>(rsWO, offWO, (i / 5) * 1280 + (i % 5) * 256); };
  auto a_issue = [&](auto mc) AINL {
    constexpr It it = a_item(decltype(mc)::value);
    if constexpr (it.kind == 4) WQ[it.idx] = ld<AUX_NT>(rsWQ, (it.idx < 2 || w < 4) ? offWQ : OOB, it.idx * 8 * 8192);
    else if constexpr (it.kind == 2) ldwo(IC<it.idx>{});
    else if constexpr (it.idx < NR) KB[it.idx] = ld<AUX_K>(rsK, offKV, it.idx * 8192);
    else KL[(it.idx - NR) % (TW + 1)] = ld<AUX_K>(rsK, offKV, it.idx * 8192);
  };
  constexpr int NINIT0 = 2, NINIT1 = 4, NINIT2 = 5, NINIT3 = 7, NINIT = 8;
  static_assert(NINIT >= TW && a_item(NINIT - 1).idx < NR, "no parking during P1");
  sfor<0, NINIT0>(a_issue);
  SB();
  STAMP(1);
  WAITVM(NINIT0);
  sfor<NINIT0, NINIT1>(a_issue);
  SB();
  {
    auto silu8 = [&](v4u v) AINL {
      v4u r;
#pragma unroll
      for (int d = 0; d < 4; ++d) r[d] = cvtpk(fsilu(bflo(v[d])), fsilu(bfhi(v[d])));
      return r;
    };
    if (t < 160) lds_st<v4u>(L, L_SC + (t / 20) * SC_STRIDE + (t % 20) * 16, silu8(cv0));
    lds_st<v4u>(L, L_WL + w * 1024 + lane * 16, wm0);
    lds_st<v4u>(L, L_WL + (w + 8) * 1024 + lane * 16, wm1);
    if (w < 4) lds_st<v4u>(L, L_WL + (w + 16) * 1024 + lane * 16, wm2);
  }
  SB();
  sfor<NINIT1, NINIT2>(a_issue);
  v4u xa = ld<AUX_NT>(rsX, w * 1280 + lane * 16, 0);
  unsigned xb = __builtin_amdgcn_raw_buffer_load_b32(rsX, w * 1280 + 1024 + lane * 4, 0, AUX_NT);
  v2u xh = __builtin_amdgcn_raw_buffer_load_b64(rsX, lane < 20 ? w * 1280 + 160 * b + lane * 8 : OOB, 0, AUX_NT);
  v2u xown = __builtin_amdgcn_raw_buffer_load_b64(rsX, (w == 4 && lane < 5) ? b * 1280 + 40 * h + lane * 8 : OOB, 0, AUX_NT);
  SB();
  BAR();
  STAMP(2);
  sfor<NINIT2, NINIT3>([&](auto mc) AINL { WAITVM(TW - 2 + 4); a_issue(mc); SB(); });
  {
    f4 acc = {0.f, 0.f, 0.f, 0.f};
    const int tl = w & 3, s0_ = (w >> 2) * 3;
    const int trbase = L_WL + (8 * g + ((lane & 15) >> 2)) * 128 + tl * 32 + (lane & 3) * 8;
#pragma unroll
    for (int n = 0; n < 3; ++n) {
      if (n < 2 || w < 4) {
        const int s = s0_ + n;
        const v4u a = lds_ld<v4u>(L, L_SC + (c & 7) * SC_STRIDE + s * 64 + g * 16);
        const s4 b0 = __builtin_amdgcn_ds_read_tr16_b64_v4i16((LAS s4*)(L + trbase + s * 4096));
        const s4 b1 = __builtin_amdgcn_ds_read_tr16_b64_v4i16((LAS s4*)(L + trbase + s * 4096 + 512));
        const v2u u0 = __builtin_bit_cast(v2u, b0), u1 = __builtin_bit_cast(v2u, b1);
        acc = mfma(a, v4u{u0.x, u0.y, u1.x, u1.y}, acc);
      }
    }
    if (g < 2) {
#pragma unroll
      for (int r = 0; r < 4; ++r) lds_st<float>(L, L_PART + (w >> 2) * 2048 + (4 * g + r) * 256 + (16 * tl + c) * 4, acc[r]);
    }
  }
  sfor<NINIT3, NINIT>([&](auto mc) AINL { WAITVM(TW - 2 + 4); a_issue(mc); SB(); });
  BAR();
  {
    const f4 s = lds_ld<f4>(L, L_PART + (t & 127) * 16) + lds_ld<f4>(L, L_PART + 2048 + (t & 127) * 16);
    unsigned o = OOB;
    if (t < 128) { if (role) o = WS_PM + (((t >> 4) * 4 + kq) * 3840 + ta * 64) * 4 + (t & 15) * 16; }
    else if (t == 128) o = WS_ALIVE + wg * 16;
    stsc(rsWS, o, tag4(s, T));
  }
  STAMP(3);
  float xmean, xrden, lnv[4];
  Sel S;
  {
    const int e = c & 7;
#pragma unroll
    for (int par = 0; par < 2; ++par)
#pragma unroll
      for (int d = 0; d < 4; ++d) {
        const unsigned lo = (unsigned)(2 * par) | ((unsigned)(2 * par + 1) << 8);
        S.s[par][d] = (d == (e >> 1)) ? ((e & 1) ? (0x0c0cu | (lo << 16)) : (0x0c0c0000u | lo)) : 0x0c0c0c0cu;
      }
  }
  unsigned po[4]; v4u pvv[4];
  unsigned long long pm0, pm1, pm2, pm3;
  auto pmasks = [&]() AINL { pm0 = ballot(po[0] != OOB); pm1 = ballot(po[1] != OOB); pm2 = ballot(po[2] != OOB); pm3 = ballot(po[3] != OOB); };
#pragma unroll
  for (int q = 0; q < 4; ++q) po[q] = lane < 40 ? WS_PM + ((w * 4 + q) * 3840 + (lane / 20) * 640 + 80 * b + 4 * (lane % 20)) * 4 : OOB;
  pmasks();
  auto pissue = [&]() AINL {
#pragma unroll
    for (int q = 0; q < 4; ++q) pvv[q] = ldsc(rsWS, po[q]);
  };
  auto pok = [&]() AINL {
    return (ballot(tagok4(pvv[0], T)) & pm0) == pm0 && (ballot(tagok4(pvv[1], T)) & pm1) == pm1 && (ballot(tagok4(pvv[2], T)) & pm2) == pm2 && (ballot(tagok4(pvv[3], T)) & pm3) == pm3;
  };
  auto preload = [&]() AINL {
#pragma unroll
    for (int q = 0; q < 4; ++q) { const bool k1 = tagok4(pvv[q], T); const v4u nv = ldsc(rsWS, k1 ? OOB : po[q]); pvv[q] = k1 ? pvv[q] : nv; }
  };
  auto pblock = [&]() AINL {
    unsigned spins = 0;
    while (!pok() && ++spins < SPIN_CAP) { __builtin_amdgcn_s_sleep(2); FENCE(); preload(); }
  };
  auto psum = [&]() AINL {
    f4 mv;
#pragma unroll
    for (int e = 0; e < 4; ++e) mv[e] = (__uint_as_float(pvv[0][e] & 0xffffff00u) + __uint_as_float(pvv[1][e] & 0xffffff00u)) +
                                        (__uint_as_float(pvv[2][e] & 0xffffff00u) + __uint_as_float(pvv[3][e] & 0xffffff00u));
    float a0 = mv[0], a1 = mv[1], a2 = mv[2], a3 = mv[3]; rbf2(a0, a1); rbf2(a2, a3);
    return f4{a0, a1, a2, a3};
  };
  bool pdone = false, hdone = false, mdone = false;
  auto rprocess = [&]() AINL {
    const f4 mv = psum();
    float hv[4];
#pragma unroll
    for (int e = 0; e < 4; ++e) {
      const float ca = __uint_as_float(__builtin_amdgcn_ds_bpermute(((lane + 20) & 63) * 4, (int)__float_as_uint(mv[e])));
      hv[e] = rbf(lnv[e] * rbf(1.f + ca)) + mv[e];
    }
    if (lane < 20) lds_st<v2u>(L, L_HB + w * 160 + lane * 8, v2u{cvtpk(hv[0], hv[1]), cvtpk(hv[2], hv[3])});
    asm volatile("s_waitcnt lgkmcnt(0)" ::: "memory");
    if (lane == 0) __hip_atomic_fetch_add((LAS unsigned*)(L + L_FLAG), 1u, __ATOMIC_RELAXED, __HIP_MEMORY_SCOPE_WORKGROUP);
    STAMP(14);
  };
  auto mprocess = [&]() AINL {
    const f4 mv = psum();
    v4u msg;
#pragma unroll
    for (int e = 0; e < 4; ++e) msg[e] = (__float_as_uint(rbf(lane >= 10 ? 1.f + mv[e] : mv[e])) & 0xffff0000u) | ((T >> (8 * e)) & 0xffu);
    if (lane >= 5 && lane < 15) stsc(rsWS, WS_AGP + (b * 32 + h) * 256 + lane * 16, msg);
    else if (lane < 5) lds_st<f4>(L, L_GAB + lane * 16, mv);
    else if (lane < 20) lds_st<f4>(L, L_GAB + 80 + (lane - 15) * 16, mv);
    STAMP(17);
  };
  auto bfpack = [&](v4u v) AINL { return v2u{__builtin_amdgcn_perm(v.y, v.x, 0x07060302u), __builtin_amdgcn_perm(v.w, v.z, 0x07060302u)}; };
  auto hflag = [&]() AINL { FENCE(); return __builtin_amdgcn_readfirstlane((int)*(volatile LAS unsigned*)(L + L_FLAG)) == 8; };
  auto hstep = [&]() AINL {
    const int pi = w & 3, hf = w >> 2;
    unsigned X[5]; v4u Bp[10];
#pragma unroll
    for (int d = 0; d < 5; ++d) X[d] = lds_ld<unsigned>(L, L_HB + (2 * pi + (c >> 3)) * 160 + 40 * g + 20 * hf + d * 4);
#pragma unroll
    for (int i = 0; i < 10; ++i) Bp[i] = lds_ld<v4u>(L, L_WQL + (10 * hf + i) * 1024 + lane * 16);
    f4 a0 = {0.f, 0.f, 0.f, 0.f}, a1 = {0.f, 0.f, 0.f, 0.f};
#pragma unroll
    for (int i = 0; i < 10; i += 2) { a0 = mfma(diagA(X[i >> 1], S, 0), Bp[i], a0); a1 = mfma(diagA(X[i >> 1], S, 1), Bp[i + 1], a1); }
    a0 += a1;
    lds_st<f4>(L, L_QP8 + w * 1024 + lane * 16, a0);
    asm volatile("s_waitcnt lgkmcnt(0)" ::: "memory");
    unsigned old = 0;
    if (lane == 0) old = __hip_atomic_fetch_add((LAS unsigned*)(L + L_FLAG + 4 + pi * 4), 1u, __ATOMIC_RELAXED, __HIP_MEMORY_SCOPE_WORKGROUP);
    if (__builtin_amdgcn_readfirstlane((int)old) == 1) {
      FENCE();
      const f4 lo = lds_ld<f4>(L, L_QP8 + pi * 1024 + lane * 16), hi = lds_ld<f4>(L, L_QP8 + (pi + 4) * 1024 + lane * 16);
      stsc(rsWS, WS_QPM + (h * 8 + b) * 4096 + (2 * pi + (g >> 1)) * 512 + (2 * c + (g & 1)) * 16, tag4(lo + hi, T));
      STAMP(16);
    }
  };
  static_assert(a_kpos(RK + RD) >= a_kpos(6) + 1 + TW, "wq parked before the first h arrival");
  static_assert(a_kpos(RK) >= NINIT && RK + 3 * RD < QK && RK + 3 * RD <= HK + (HN - 1) * HD && QK + 2 * RD < 32, "stage order");
  auto pnone = [&]() AINL {
#pragma unroll
    for (int q = 0; q < 4; ++q) po[q] = OOB;
  };
  sfor<NINIT, NA>([&](auto mc) AINL {
    constexpr int m = decltype(mc)::value;
    constexpr It it = a_item(m);
    constexpr int k = it.kind == 0 ? it.idx : -100;
    WAITW(TW - 1 + (m - (TW - DA_LO) < NINIT ? 1 : 0) + (NODUTY ? 0 : 4 * a_polls_between(m - (TW - DA_LO), m - 1)), DA_LO, DA_HI);
    {
      constexpr It ol = a_item(m - TW);
      if constexpr (ol.kind == 4) { if (ol.idx < 2 || w < 4) lds_st<v4u>(L, L_WQL + (w + 8 * ol.idx) * 1024 + lane * 16, WQ[ol.idx]); }
      if constexpr (!NOPARK && ol.kind == 0 && ol.idx >= NR) lds_st<v4u>(L, ksoff + (ol.idx - NR) * 1024, KL[(ol.idx - NR) % (TW + 1)]);
    }
    a_issue(mc);
    if constexpr (k == RK) {
  {
    float xv[10];
#pragma unroll
    for (int d = 0; d < 4; ++d) { xv[2 * d] = bflo(xa[d]); xv[2 * d + 1] = bfhi(xa[d]); }
    xv[8] = bflo(xb); xv[9] = bfhi(xb);
    const float s = ((xv[0] + xv[1]) + (xv[2] + xv[3])) + ((xv[4] + xv[5]) + (xv[6] + xv[7])) + (xv[8] + xv[9]);
    const float mean = wave_sum(s) * (1.f / 640.f);
    float q2 = 0.f, q3 = 0.f;
#pragma unroll
    for (int d = 0; d < 5; ++d) { const float d0 = xv[2 * d] - mean, d1 = xv[2 * d + 1] - mean; q2 = fmaf(d0, d0, q2); q3 = fmaf(d1, d1, q3); }
    const float var = rbf(wave_sum(q2 + q3) * (1.f / 640.f));
    xmean = rbf(mean); xrden = frcp(rbf(__builtin_amdgcn_sqrtf(rbf(var + 1e-5f))));
    if (w == 4 && lane < 5) lds_st<v2u>(L, L_XR + lane * 8, xown);
  }
  {
    float a0 = bflo(xh.x) - xmean, a1 = bfhi(xh.x) - xmean, a2 = bflo(xh.y) - xmean, a3 = bfhi(xh.y) - xmean;
    rbf2(a0, a1); rbf2(a2, a3);
    a0 *= xrden; a1 *= xrden; a2 *= xrden; a3 *= xrden;
    rbf2(a0, a1); rbf2(a2, a3);
    lnv[0] = a0; lnv[1] = a1; lnv[2] = a2; lnv[3] = a3;
  }
    }
    if constexpr (k == 8) STAMP(18);
    if constexpr (k == 16) STAMP(19);
    if constexpr (k == 24) STAMP(20);
    if constexpr (!NODUTY) {
    if constexpr (k == RK + RD || k == RK + 2 * RD) { if (!pdone && pok()) { rprocess(); pdone = true; pnone(); } }
    if constexpr (k == RK + 3 * RD) { if (!pdone) { pblock(); rprocess(); } }
    if constexpr (k >= HK && k < HK + (HN - 1) * HD && (k - HK) % HD == 0) { if (!hdone && hflag()) { hstep(); hdone = true; } }
    if constexpr (k == HK + (HN - 1) * HD) {
      if (!hdone) { unsigned spins = 0; while (!hflag() && ++spins < (SPIN_CAP << 3)) __builtin_amdgcn_s_sleep(1); hstep(); }
    }
    if constexpr (k == QK) {
#pragma unroll
      for (int q = 0; q < 4; ++q) {
        unsigned o = OOB;
        if (w == 5) { if (lane < 20) o = WS_PM + ((b * 4 + q) * 3840 + (2 + lane / 5) * 640 + 20 * h + 4 * (lane % 5)) * 4; }
        else if (w == 6) o = WS_QPM + ((h * 8 + 2 * q + (lane >> 5)) * 8 + b) * 512 + (lane & 31) * 16;
        else if (w == 4 && wg == 0) o = WS_ALIVE + (64 * q + lane) * 16;
        po[q] = o;
      }
      pmasks();
    }
    if constexpr (k == QK + RD || k == QK + 2 * RD) { if (w == 5 && !mdone && pok()) { mprocess(); mdone = true; pnone(); } }
    if constexpr (a_pollk(k)) pissue();
    }
    SB();
  });
  STAMP(4);
  v4u W1[10], W2[10], VR[VRN];
  const unsigned offW1 = (mlp && !NOW) ? (unsigned)(80 * w + 40 * (c >> 3) + 10 * g) * 3456u + (64 * h + 8 * (c & 7)) * 2 : OOB;
  const unsigned offW2 = (mlp && !NOW) ? (unsigned)(64 * h + 8 * w + 2 * g) * 1280u + c * 16 : OOB;
  auto b_issue = [&](auto mc) AINL {
    constexpr It it = b_item(decltype(mc)::value);
    if constexpr (it.kind == 1) VR[it.idx % VRN] = ld<AUX_V>(rsV, offKV, it.idx * 8192);
    else if constexpr (it.kind == 2) ldwo(IC<it.idx>{});
    else W1[it.idx] = ld<AUX_W>(rsW1, offW1, it.idx * 3456);
    SB();
  };
#define VTICK(k) do { WAITW(TW - 1, DB_LO, DB_HI); b_issue(IC<(k)>{}); } while (0)
  sfor<0, TW>([&](auto kc) AINL { WAITW(TW - 1 + 4 * a_polls_between(NA + decltype(kc)::value - (TW - DB_LO), NA - 1), DB_LO, DB_HI); b_issue(kc); });
  if (!NODUTY && w == 5 && !mdone) { pblock(); mprocess(); }
  if (!NODUTY && (w == 6 || w == 4)) {
    pblock();
    if (w == 6) {
      f4 q4;
#pragma unroll
      for (int e = 0; e < 4; ++e) q4[e] = x32_sum((__uint_as_float(pvv[0][e] & 0xffffff00u) + __uint_as_float(pvv[1][e] & 0xffffff00u)) +
                                                  (__uint_as_float(pvv[2][e] & 0xffffff00u) + __uint_as_float(pvv[3][e] & 0xffffff00u)));
      if (lane < 32) lds_st<v2u>(L, L_QB + lane * 8, v2u{cvtpk(q4[0], q4[1]), cvtpk(q4[2], q4[3])});
    } else {
      if (wg == 0 && lane == 0) __builtin_amdgcn_raw_buffer_store_b32(T, rsWS, WS_EPOCH, 0, AUX_SC1);
    }
  }
  BAR();
  STAMP(5);
  const v4u qr = lds_ld<v4u>(L, L_QB + c * 16);
  STAMP(6);
  auto kdot = [&](v4u k) AINL { return dot2(k.w, qr[3], dot2(k.z, qr[2], dot2(k.y, qr[1], dot2(k.x, qr[0], 0.f)))); };
  auto treduce = [&](float (&xs)[16]) AINL {
    float x[8];
#pragma unroll
    for (int i = 0; i < 8; ++i) { const float lo = xs[i] + dppf<XOR8>(xs[i]), hi = xs[i + 8] + dppf<XOR8>(xs[i + 8]); x[i] = (c & 8) ? hi : lo; }
#pragma unroll
    for (int i = 0; i < 4; ++i) { const float lo = x[i] + dppf<XOR7>(x[i]), hi = x[i + 4] + dppf<XOR7>(x[i + 4]); x[i] = (c & 4) ? hi : lo; }
#pragma unroll
    for (int i = 0; i < 2; ++i) { const float lo = x[i] + dppf<XOR2>(x[i]), hi = x[i + 2] + dppf<XOR2>(x[i + 2]); x[i] = (c & 2) ? hi : lo; }
    const float lo = x[0] + dppf<XOR1>(x[0]), hi = x[1] + dppf<XOR1>(x[1]);
    return (c & 1) ? hi : lo;
  };
  constexpr float C2 = 0.08838834764831845f * 1.4426950408889634f;
  float mw, fw = 0.f;
  unsigned ppk;
  {
    float xs[16];
#pragma unroll
    for (int j = 0; j < 16; ++j) xs[j] = kdot(KB[j]);
    SB();
    VTICK(TW);
    const float s0 = treduce(xs);
    SB();
    VTICK(TW + 1);
#pragma unroll
    for (int j = 0; j <= LASTPARK - NR; ++j) xs[j] = kdot(lds_ld<v4u>(L, ksoff + j * 1024));
#pragma unroll
    for (int j = LASTPARK - NR + 1; j < 16; ++j) xs[j] = kdot(KL[j % (TW + 1)]);
    SB();
    sfor<TW + 2, VRN>([&](auto kc) AINL { WAITW(TW - 1, DB_LO, DB_HI); b_issue(kc); });
    const float s1 = treduce(xs);
    SB();
    sfor<VRN, VRN + WEARLY>([&](auto kc) AINL { WAITW(TW - 1, DB_LO, DB_HI); b_issue(kc); });
    mw = wave_max(fmaxf(s0, s1));
    const float p0 = __builtin_amdgcn_exp2f((s0 - mw) * C2), p1 = __builtin_amdgcn_exp2f((s1 - mw) * C2);
    const float lw = wave_sum(p0 + p1);
    ppk = cvtpk(p0, p1);
    if (lane == 0) lds_st<v2u>(L, L_ML + w * 8, v2u{__float_as_uint(mw), __float_as_uint(lw)});
  }
  STAMP(15);
  f4 oacc = {0.f, 0.f, 0.f, 0.f};
  auto pvstep = [&](auto jc) AINL {
    constexpr int j = decltype(jc)::value;
    const unsigned X = dppu<0x150 + (j & 15)>(ppk);
    oacc = mfma(diagA(X, S, j >> 4), VR[j % VRN], oacc);
    if constexpr (j == 4) {
#pragma unroll
      for (int q = 0; q < 4; ++q) { const int idx = 64 * q + lane; po[q] = ((w == 1 || w == 2) && mlp && idx < 160) ? WS_AGP + (b * 32 + idx / 5) * 256 + (5 * w + idx % 5) * 16 : OOB; }
      pmasks(); pissue();
    }
    if constexpr (j == 14) {
      if (!NODUTY && (w == 1 || w == 2)) {
        pblock();
#pragma unroll
        for (int n = 0; n < 3; ++n) {
          const int idx = 64 * n + lane;
          if (po[n] != OOB) lds_st<v2u>(L, (w == 1 ? L_SB : L_CB) + (20 * (idx / 5) + 4 * (idx % 5)) * 2, bfpack(pvv[n]));
        }
      }
    }
    if constexpr (j == 10) {
      BAR();
      float M = -INFINITY;
      v2u ml[8];
#pragma unroll
      for (int ww = 0; ww < 8; ++ww) { ml[ww] = lds_ld<v2u>(L, L_ML + ww * 8); M = fmaxf(M, __uint_as_float(ml[ww].x)); }
      float Ls = 0.f;
#pragma unroll
      for (int ww = 0; ww < 8; ++ww) Ls += __builtin_amdgcn_exp2f((__uint_as_float(ml[ww].x) - M) * C2) * __uint_as_float(ml[ww].y);
      fw = __builtin_amdgcn_exp2f((mw - M) * C2) * frcp(Ls);
      PIN(fw);
    }
  };
  sfor<VRN + WEARLY, NB>([&](auto mc) AINL {
    constexpr int m = decltype(mc)::value;
    constexpr It it = b_item(m);
    if constexpr (it.kind == 1) pvstep(IC<it.idx - VRN>{});
    if constexpr (it.kind != 3) WAITW(TW - 1, DB_LO, DB_HI);
    else if constexpr (it.idx < VRN) { pvstep(IC<32 - VRN + it.idx>{}); }
    b_issue(mc);
  });
  sfor<(VRN > 10 ? 32 - VRN + 10 : 32), 32>([&](auto jc) AINL { pvstep(jc); SB(); });
  STAMP(7);
  {
    const f4 ov = oacc * fw;
    if (lane < 32) lds_st<f4>(L, L_OP + w * 512 + (8 * c + 4 * g) * 4, ov);
  }
  BAR();
  const int ypoff = L_YP + w * 2560 + (8 * c + 4 * (g & 1)) * 4;
  {
    const int kap = (c >> 1) & 3, sig = (c & 1) + 2 * (c >> 3);
    const int ko = (16 * w + 4 * g + kap) * 4;
    float o = lds_ld<float>(L, L_OP + (2 * sig) * 512 + ko) + lds_ld<float>(L, L_OP + (2 * sig + 1) * 512 + ko);
    o += dppf<XOR1>(o); o += dppf<XOR8>(o);
    const unsigned P = cvtpk(dppf<QB0>(o), dppf<QB2>(o)), Q = dppu<XOR7>(P);
    const unsigned X0 = (c & 4) ? Q : P, X1 = (c & 4) ? P : Q;
    f4 y[5];
    { const v4u A = diagA(X0, S, 0);
#pragma unroll
      for (int cb = 0; cb < 5; ++cb) y[cb] = mfma(A, WO[cb], f4{0.f, 0.f, 0.f, 0.f}); }
    { const v4u A = diagA(X0, S, 1);
#pragma unroll
      for (int cb = 0; cb < 5; ++cb) y[cb] = mfma(A, WO[5 + cb], y[cb]); }
    { const v4u A = diagA(X1, S, 0);
#pragma unroll
      for (int cb = 0; cb < 5; ++cb) y[cb] = mfma(A, WO[10 + cb], y[cb]); }
    { const v4u A = diagA(X1, S, 1);
#pragma unroll
      for (int cb = 0; cb < 5; ++cb) y[cb] = mfma(A, WO[15 + cb], y[cb]); }
    if (lane < 32) {
#pragma unroll
      for (int cb = 0; cb < 5; ++cb) lds_st<f4>(L, ypoff + cb * 512, y[cb]);
    }
  }
  BAR();
  STAMP(8);
  auto publish = [&](int wsoff) AINL {
    const int k = t & 7, j = t >> 3;
    const int o = L_YP + (20 * j + 4 * (k < 5 ? k : 0)) * 4;
    f4 s0 = lds_ld<f4>(L, o) + lds_ld<f4>(L, o + 2560), s1 = lds_ld<f4>(L, o + 2 * 2560) + lds_ld<f4>(L, o + 3 * 2560);
    f4 s2 = lds_ld<f4>(L, o + 4 * 2560) + lds_ld<f4>(L, o + 5 * 2560), s3 = lds_ld<f4>(L, o + 6 * 2560) + lds_ld<f4>(L, o + 7 * 2560);
    stsc(rsWS, wsoff + t * 16, tag4((s0 + s1) + (s2 + s3), T));
  };
  float x1v[4] = {0.f, 0.f, 0.f, 0.f};
  auto rs_gather = [&](int wsbase, int nprod, int delay_done) AINL -> f4 {
    const int pl = lane >> 3, k = lane & 7;
    unsigned off[4]; v4u v[4];
#pragma unroll
    for (int q = 0; q < 4; ++q) {
      const int u = 8 * q + pl;
      const int wgu = (b << 5) | ((u & 3) << 3) | (u >> 2);
      off[q] = (k < 5 && u < nprod) ? wsbase + wgu * 4096 + h * 128 + k * 16 : OOB;
      PIN(off[q]);
    }
    const unsigned long long m0 = ballot(off[0] != OOB), m3 = ballot(off[3] != OOB);
    __builtin_amdgcn_s_sleep(0);
    (void)delay_done;
#pragma unroll
    for (int q = 0; q < 4; ++q) v[q] = ldsc(rsWS, off[q]);
    unsigned spins = 0;
    for (;;) {
      const bool k0 = tagok4(v[0], T), k1 = tagok4(v[1], T), k2 = tagok4(v[2], T), k3 = tagok4(v[3], T);
      if ((ballot(k0) & ballot(k1) & ballot(k2) & m0) == m0 && (ballot(k3) & m3) == m3) break;
      if (++spins > SPIN_CAP) break;
      __builtin_amdgcn_s_sleep(POLL_SLEEP);
      FENCE();
      const v4u n0 = ldsc(rsWS, k0 ? OOB : off[0]), n1 = ldsc(rsWS, k1 ? OOB : off[1]), n2 = ldsc(rsWS, k2 ? OOB : off[2]), n3 = ldsc(rsWS, k3 ? OOB : off[3]);
      v[0] = k0 ? v[0] : n0; v[1] = k1 ? v[1] : n1; v[2] = k2 ? v[2] : n2; v[3] = k3 ? v[3] : n3;
    }
    f4 s;
#pragma unroll
    for (int e = 0; e < 4; ++e) {
      float a = (__uint_as_float(v[0][e] & 0xffffff00u) + __uint_as_float(v[1][e] & 0xffffff00u)) + (__uint_as_float(v[2][e] & 0xffffff00u) + __uint_as_float(v[3][e] & 0xffffff00u));
      a += dppf<XOR8>(a); s[e] = x32_sum(x16_sum(a));
    }
    return s;
  };
  if (w < 4) publish(WS_OUT1 + wg * 4096);
  if (w != 4) {
#pragma unroll
    for (int i = 0; i < 10; ++i) W2[i] = ld<AUX_W>(rsW2, offW2, (i / 5) * 1280 + (i % 5) * 256);
  }
  if (w == 4) {
    const v2u xr = lds_ld<v2u>(L, L_XR + (lane & 7) * 8); const f4 gg = lds_ld<f4>(L, L_GAB + (lane & 7) * 16);
    unsigned off[3]; v4u v[3]; float sbv[12], cbv[12]; int hoff[3];
#pragma unroll
    for (int q = 0; q < 3; ++q) {
      const int idx = 64 * q + lane, jj = idx / 5, k = idx % 5;
      off[q] = (idx < 160 && mlp) ? WS_AG + (b * 32 + jj) * 128 + k * 16 : OOB;
      const int col = idx < 160 ? 20 * jj + 4 * k : 0;
      const v2u s2 = lds_ld<v2u>(L, L_SB + col * 2), c2 = lds_ld<v2u>(L, L_CB + col * 2);
      sbv[4 * q] = bflo(s2.x); sbv[4 * q + 1] = bfhi(s2.x); sbv[4 * q + 2] = bflo(s2.y); sbv[4 * q + 3] = bfhi(s2.y);
      cbv[4 * q] = bflo(c2.x); cbv[4 * q + 1] = bfhi(c2.x); cbv[4 * q + 2] = bflo(c2.y); cbv[4 * q + 3] = bfhi(c2.y);
      hoff[q] = L_H2 + col * 2;
      PIN(off[q]); PIN(hoff[q]);
    }
#pragma unroll
    for (int i = 0; i < 12; ++i) { PIN(sbv[i]); PIN(cbv[i]); }
    float xx[4] = {bflo(xr.x), bfhi(xr.x), bflo(xr.y), bfhi(xr.y)};
    const unsigned long long am0 = ballot(off[0] != OOB), am2 = ballot(off[2] != OOB);
    __builtin_amdgcn_s_sleep(D_RS1);
    const f4 s = rs_gather(WS_OUT1, 32, 0);
    v4u m;
    {
      float a0 = s[0], a1 = s[1], a2 = s[2], a3 = s[3];
      rbf2(a0, a1); rbf2(a2, a3);
      a0 *= gg[0]; a1 *= gg[1]; a2 *= gg[2]; a3 *= gg[3];
      rbf2(a0, a1); rbf2(a2, a3);
      a0 += xx[0]; a1 += xx[1]; a2 += xx[2]; a3 += xx[3];
      const unsigned p01 = cvtpk(a0, a1), p23 = cvtpk(a2, a3);
      m = v4u{(p01 << 16) | (T & 0xffu), (p01 & 0xffff0000u) | ((T >> 8) & 0xffu), (p23 << 16) | ((T >> 16) & 0xffu), (p23 & 0xffff0000u) | (T >> 24)};
      x1v[0] = bflo(p01); x1v[1] = bfhi(p01); x1v[2] = bflo(p23); x1v[3] = bfhi(p23);
    }
    if (lane < 5) stsc(rsWS, WS_AG + (b * 32 + h) * 128 + lane * 16, m);
#pragma unroll
    for (int i = 0; i < 10; ++i) W2[i] = ld<AUX_W>(rsW2, offW2, (i / 5) * 1280 + (i % 5) * 256);
    STAMP(9);
    if (mlp) {
      __builtin_amdgcn_s_sleep(D_AG);
#pragma unroll
      for (int q = 0; q < 3; ++q) v[q] = ldsc(rsWS, off[q]);
      unsigned spins = 0;
      for (;;) {
        const bool k0 = tagok4(v[0], T), k1 = tagok4(v[1], T), k2 = tagok4(v[2], T);
        if ((ballot(k0) & ballot(k1) & am0) == am0 && (ballot(k2) & am2) == am2) break;
        if (++spins > SPIN_CAP) break;
        __builtin_amdgcn_s_sleep(POLL_SLEEP);
        FENCE();
        const v4u n0 = ldsc(rsWS, k0 ? OOB : off[0]), n1 = ldsc(rsWS, k1 ? OOB : off[1]), n2 = ldsc(rsWS, k2 ? OOB : off[2]);
        v[0] = k0 ? v[0] : n0; v[1] = k1 ? v[1] : n1; v[2] = k2 ? v[2] : n2;
      }
      STAMP(13);
      float xv[12]; float sm = 0.f;
#pragma unroll
      for (int q = 0; q < 3; ++q)
#pragma unroll
        for (int e = 0; e < 4; ++e) { xv[4 * q + e] = __uint_as_float(v[q][e] & 0xffff0000u); sm += xv[4 * q + e]; }
      const float mean = wave_sum(sm) * (1.f / 640.f);
      float q2 = 0.f, q3 = 0.f;
#pragma unroll
      for (int i = 0; i < 8; ++i) { const float dd = xv[i] - mean; q2 += dd * dd; }
#pragma unroll
      for (int i = 8; i < 12; ++i) { const float dd = xv[i] - mean; q3 += dd * dd; }
      q2 += lane < 32 ? q3 : 0.f;
      const float var = rbf(wave_sum(q2) * (1.f / 640.f));
      const float mb = rbf(mean), rden = frcp(rbf(__builtin_amdgcn_sqrtf(rbf(var + 1e-5f))));
#pragma unroll
      for (int q = 0; q < 3; ++q) {
        float a[4];
#pragma unroll
        for (int e = 0; e < 4; ++e) a[e] = xv[4 * q + e] - mb;
        rbf2(a[0], a[1]); rbf2(a[2], a[3]);
#pragma unroll
        for (int e = 0; e < 4; ++e) a[e] *= rden;
        rbf2(a[0], a[1]); rbf2(a[2], a[3]);
#pragma unroll
        for (int e = 0; e < 4; ++e) a[e] *= cbv[4 * q + e];
        rbf2(a[0], a[1]); rbf2(a[2], a[3]);
        const v2u o = {cvtpk(a[0] + sbv[4 * q], a[1] + sbv[4 * q + 1]), cvtpk(a[2] + sbv[4 * q + 2], a[3] + sbv[4 * q + 3])};
        if (q < 2 || lane < 32) lds_st<v2u>(L, hoff[q], o);
      }
    }
  }
  if (mlp) {
    BAR();
    STAMP(10);
    {
      unsigned X[5];
#pragma unroll
      for (int d = 0; d < 5; ++d) X[d] = lds_ld<unsigned>(L, L_H2 + 160 * w + 80 * (c >> 3) + 20 * g + d * 4);
      f4 a0 = {0.f, 0.f, 0.f, 0.f}, a1 = {0.f, 0.f, 0.f, 0.f};
#pragma unroll
      for (int p = 0; p < 10; p += 2) { a0 = mfma(diagA(X[p >> 1], S, 0), W1[p], a0); a1 = mfma(diagA(X[p >> 1], S, 1), W1[p + 1], a1); }
      a0 += a1;
      if ((g >> 1) == (c >> 3)) lds_st<f4>(L, L_HP + (2 * w + (c >> 3)) * 256 + (8 * (c & 7) + 4 * (g & 1)) * 4, a0);
    }
    BAR();
    {
      const int kap = (c >> 2) & 1, sig = (c & 3) + 4 * (c >> 3);
      const int n = (8 * w + 2 * g + kap) * 4;
      float hd = lds_ld<float>(L, L_HP + (2 * sig) * 256 + n) + lds_ld<float>(L, L_HP + (2 * sig + 1) * 256 + n);
      hd += dppf<XOR1>(hd); hd += dppf<XOR2>(hd); hd += dppf<XOR8>(hd);
      hd = fsilu(rbf(hd));
      const float ot = dppf<XOR7>(hd);
      const unsigned X = kap ? cvtpk(ot, hd) : cvtpk(hd, ot);
      const v4u A0 = diagA(X, S, 0), A1 = diagA(X, S, 1);
      f4 y[5];
#pragma unroll
      for (int cb = 0; cb < 5; ++cb) y[cb] = mfma(A0, W2[cb], f4{0.f, 0.f, 0.f, 0.f});
#pragma unroll
      for (int cb = 0; cb < 5; ++cb) y[cb] = mfma(A1, W2[5 + cb], y[cb]);
      if (lane < 32) {
#pragma unroll
        for (int cb = 0; cb < 5; ++cb) lds_st<f4>(L, ypoff + cb * 512, y[cb]);
      }
    }
    BAR();
    STAMP(11);
    if (w < 4) publish(WS_OUT2 + wg * 4096);
  }
  if (w == 4) {
    const f4 gg = lds_ld<f4>(L, L_GAB + 80 + (lane & 7) * 16);
    if (!mlp) __builtin_amdgcn_s_sleep(48);
    __builtin_amdgcn_s_sleep(D_RS2);
    const f4 s = rs_gather(WS_OUT2, 27, 0);
    float a0 = s[0], a1 = s[1], a2 = s[2], a3 = s[3];
    rbf2(a0, a1); rbf2(a2, a3);
    a0 *= gg[0]; a1 *= gg[1]; a2 *= gg[2]; a3 *= gg[3];
    rbf2(a0, a1); rbf2(a2, a3);
    if (lane < 5) __builtin_amdgcn_raw_buffer_store_b64(v2u{cvtpk(x1v[0] + a0, x1v[1] + a1), cvtpk(x1v[2] + a2, x1v[3] + a3)}, rsOUT, b * 1280 + 40 * h + lane * 8, 0, 0);
    STAMP(12);
  }
}

extern "C" void kernel_launch(void* const* d_in, const int* in_sizes, int n_in, void* d_out, int out_size, void* d_ws, size_t ws_size,
                              hipStream_t stream) {
  fused_kernel<<<256, 512, 0, stream>>>(d_in[0], d_in[1], d_in[2], d_in[3], d_in[4], d_in[6], d_in[7], d_in[8], d_in[9], d_out, d_ws);
}
```

```cpp
#include <hip/hip_runtime.h>
#include <hip/hip_bf16.h>
#include <stdint.h>
#include <type_traits>

typedef unsigned v4u __attribute__((ext_vector_type(4)));
typedef unsigned v2u __attribute__((ext_vector_type(2)));
typedef float f4 __attribute__((ext_vector_type(4)));
typedef __bf16 bf8 __attribute__((ext_vector_type(8)));
typedef __bf16 bf2 __attribute__((ext_vector_type(2)));
typedef short s4 __attribute__((ext_vector_type(4)));
#define LAS __attribute__((address_space(3)))
typedef __amdgpu_buffer_rsrc_t rsrc_t;

#define AUX_K 2
#define AUX_V 2
#define AUX_W 0
constexpr int AUX_NT = 2, AUX_SC1 = 16;
constexpr unsigned OOB = 0x80000000u;

constexpr int WS_EPOCH = 0;
constexpr int WS_ALIVE = 4096;
constexpr int WS_XST = 16384;
constexpr int WS_AGP = 65536;
constexpr int WS_AG = 131072;
constexpr int WS_PM = 262144;
constexpr int WS_OUT1 = 1 << 20;
constexpr int WS_OUT2 = 2 << 20;
constexpr int WS_QPM = 3 << 20;
constexpr int WS_STAMP = 4 << 20;
constexpr int WS_BYTES = 8 << 20;

#define TW 6
#define DA_LO 1
#define DA_HI 1
#define DB_LO 1
#define DB_HI 0
#define VRN 14
#define VPRE 9
constexpr int NR = 16;
#define RK 12
#define RL 19
#define HK 19
#define HD 2
#define HN 3
#define QK 25
struct It { int kind, idx; };
#define WOA 10
constexpr int NA = 3 + 32 + WOA + 8;
constexpr It a_item(int m) {
  int pos = 0, wn = 0;
  for (int k = 0; k < 32; ++k) {
    if (pos == m) return It{0, k};
    ++pos;
    if (k >= 4 && k < 7) { if (pos == m) return It{4, k - 4}; ++pos; }
    else if (k >= RK && k < RK + 4) { if (pos == m) return It{5, k - RK}; ++pos; }
    else if (k >= QK && k < QK + 4) { if (pos == m) return It{6, k - QK}; ++pos; }
    else if (k >= 7 && k < 31) {
      int idx = 0; for (int u = 7; u < k; ++u) if (!(u >= RK && u < RK + 4) && !(u >= QK && u < QK + 4)) ++idx;
      if (idx * WOA / 16 != (idx + 1) * WOA / 16 && wn < WOA) { if (pos == m) return It{2, wn}; ++pos; ++wn; }
    }
  }
  return It{-1, 0};
}
static_assert(a_item(NA - 1).kind == 0 && a_item(NA - 1).idx == 31, "phase A list");
constexpr int a_kpos(int k) { for (int m = 0; m < NA; ++m) if (a_item(m).kind == 0 && a_item(m).idx == k) return m; return NA; }
#define WEARLY 2
#define WLAST 27
constexpr int NBV = 32 + 20 - WOA, NB = NBV + 10;
constexpr It b_item(int m) {
  if (m < VRN) return It{1, m};
  if (m < VRN + WEARLY) return It{2, WOA + m - VRN};
  if (m >= NBV) return It{3, m - NBV};
  int pos = VRN + WEARLY, wn = WOA + WEARLY;
  for (int v = VRN; v < 32; ++v) {
    if (pos == m) return It{1, v};
    ++pos;
    int target = WOA + WEARLY + ((20 - WOA - WEARLY) * (v - VRN + 1) + (WLAST - VRN)) / (WLAST - VRN + 1);
    if (target > 20) target = 20;
    while (wn < target) { if (pos == m) return It{2, wn}; ++pos; ++wn; }
  }
  return It{-1, 0};
}
static_assert(b_item(NBV - 1).kind == 1 && b_item(NBV - 1).idx == 31, "phase B list");
constexpr int b_vpos(int k) { for (int m = 0; m < NB; ++m) if (b_item(m).kind == 1 && b_item(m).idx == k) return m; return NB; }
constexpr int b_extra(int m) { int n = 0; const int p4 = b_vpos(4 + VRN), p8 = b_vpos(8 + VRN); if (m >= p4 && m - p4 < TW) n += 4; if (m >= p8 && m - p8 < TW) n += 2; return n; }
constexpr int a_lastpark_f() { int k = NR - 1; for (int m = 0; m + TW < NA; ++m) if (a_item(m).kind == 0 && a_item(m).idx > k) k = a_item(m).idx; return k; }
constexpr int LASTPARK = a_lastpark_f();
constexpr int NLDS = LASTPARK - NR + 1;

constexpr int L_WL = 0;
constexpr int L_SC = 20480;
constexpr int SC_STRIDE = 336;
constexpr int L_PART = L_SC + 2688;
constexpr int L_WQL = 0;
constexpr int L_QP8 = 20480;
constexpr int L_HB = 32768;
constexpr int L_SB = L_HB + 1280;
constexpr int L_CB = L_SB + 1280;
constexpr int L_GAB = L_CB + 1280;
constexpr int L_XR = L_GAB + 160;
constexpr int L_ML = L_XR + 64;
constexpr int L_FLAG = L_ML + 64;
constexpr int L_QB = L_FLAG + 32;
constexpr int L_H2 = L_QB + 256;
constexpr int L_OP = L_H2 + 1280;
constexpr int L_HP = L_OP + 4096;
constexpr int L_KS = L_HP + 4096;
constexpr int L_YP = L_KS;
constexpr int L_END = L_KS + (8 * NLDS * 1024 > 20480 ? 8 * NLDS * 1024 : 20480);
static_assert(L_QP8 + 8192 <= 32768 && L_PART + 4096 <= 32768 && L_HB % 16 == 0 && L_QB % 16 == 0 && L_OP % 16 == 0 && L_KS % 16 == 0 && L_GAB % 16 == 0 && L_H2 % 16 == 0 && L_END <= 163840, "lds map");

#define D_RS1 8
#define D_AG 10
#define D_RS2 8
#define POLL_SLEEP 1
#define STAMPS 0
#define NODUTY 0
#define NOPARK 0
#define NOW 0
constexpr unsigned SPIN_CAP = NODUTY ? 4u : (1u << 15);

__device__ __forceinline__ float bflo(unsigned d) { return __uint_as_float(d << 16); }
__device__ __forceinline__ float bfhi(unsigned d) { return __uint_as_float(d & 0xffff0000u); }
__device__ __forceinline__ unsigned cvtpk(float lo, float hi) {
  bf2 r = {(__bf16)lo, (__bf16)hi}; return __builtin_bit_cast(unsigned, r);
}
__device__ __forceinline__ float rbf(float f) { return bflo(cvtpk(f, 0.f)); }
__device__ __forceinline__ void rbf2(float& a, float& b) { const unsigned p = cvtpk(a, b); a = bflo(p); b = bfhi(p); }
__device__ __forceinline__ float frcp(float x) { return __builtin_amdgcn_rcpf(x); }
__device__ __forceinline__ float fsilu(float a) { return a * frcp(1.f + __builtin_amdgcn_exp2f(a * -1.4426950408889634f)); }
template <int CTRL> __device__ __forceinline__ float dppf(float x) {
  return __builtin_bit_cast(float, __builtin_amdgcn_update_dpp(0, __builtin_bit_cast(int, x), CTRL, 0xf, 0xf, true));
}
template <int CTRL> __device__ __forceinline__ unsigned dppu(unsigned x) { return (unsigned)__builtin_amdgcn_update_dpp(0, (int)x, CTRL, 0xf, 0xf, true); }
constexpr int XOR1 = 0xB1, XOR2 = 0x4E, XOR7 = 0x141, XOR8 = 0x128, QB0 = 0x00, QB2 = 0xAA;
__device__ __forceinline__ float x16_sum(float x) { auto s = __builtin_amdgcn_permlane16_swap(__float_as_uint(x), __float_as_uint(x), false, false); return __uint_as_float(s[0]) + __uint_as_float(s[1]); }
__device__ __forceinline__ float x32_sum(float x) { auto s = __builtin_amdgcn_permlane32_swap(__float_as_uint(x), __float_as_uint(x), false, false); return __uint_as_float(s[0]) + __uint_as_float(s[1]); }
__device__ __forceinline__ float x16_max(float x) { auto s = __builtin_amdgcn_permlane16_swap(__float_as_uint(x), __float_as_uint(x), false, false); return fmaxf(__uint_as_float(s[0]), __uint_as_float(s[1])); }
__device__ __forceinline__ float x32_max(float x) { auto s = __builtin_amdgcn_permlane32_swap(__float_as_uint(x), __float_as_uint(x), false, false); return fmaxf(__uint_as_float(s[0]), __uint_as_float(s[1])); }
__device__ __forceinline__ float wave_sum(float x) {
  x += dppf<XOR1>(x); x += dppf<XOR2>(x); x += dppf<XOR7>(x); x += dppf<XOR8>(x); return x32_sum(x16_sum(x));
}
__device__ __forceinline__ float wave_max(float x) {
  x = fmaxf(x, dppf<XOR1>(x)); x = fmaxf(x, dppf<XOR2>(x)); x = fmaxf(x, dppf<XOR7>(x)); x = fmaxf(x, dppf<XOR8>(x)); return x32_max(x16_max(x));
}
__device__ __forceinline__ f4 mfma(v4u a, v4u b, f4 c) {
  return __builtin_amdgcn_mfma_f32_16x16x32_bf16(__builtin_bit_cast(bf8, a), __builtin_bit_cast(bf8, b), c, 0, 0, 0);
}
__device__ __forceinline__ float dot2(unsigned a, unsigned b, float c) {
  return __builtin_amdgcn_fdot2_f32_bf16(__builtin_bit_cast(bf2, a), __builtin_bit_cast(bf2, b), c, false);
}
__device__ __forceinline__ rsrc_t mkrs(const void* p, unsigned bytes) { return __builtin_amdgcn_make_buffer_rsrc((void*)p, 0, (int)bytes, 0x00020000); }
template <int AUX> __device__ __forceinline__ v4u ld(rsrc_t r, unsigned voff, unsigned soff) { return __builtin_amdgcn_raw_buffer_load_b128(r, (int)voff, (int)soff, AUX); }
__device__ __forceinline__ v4u ldsc(rsrc_t r, unsigned voff) { return __builtin_amdgcn_raw_buffer_load_b128(r, (int)voff, 0, AUX_SC1); }
__device__ __forceinline__ void stsc(rsrc_t r, unsigned voff, v4u v) { __builtin_amdgcn_raw_buffer_store_b128(v, r, (int)voff, 0, AUX_SC1); }
__device__ __forceinline__ void stsc2(rsrc_t r, unsigned voff, v2u v) { __builtin_amdgcn_raw_buffer_store_b64(v, r, (int)voff, 0, AUX_SC1); }
#define WAITVM(n) asm volatile("s_waitcnt vmcnt(%0)" ::"n"((n) > 63 ? 63 : (n)) : "memory")
#define WAITW(n, lo, hi) do { if ((lo) == (hi)) WAITVM((n) - (lo)); else if (w < 4) WAITVM((n) - (lo)); else WAITVM((n) - (hi)); } while (0)
#define BAR() asm volatile("s_waitcnt lgkmcnt(0)\n\ts_barrier" ::: "memory")
#define SB() __builtin_amdgcn_sched_barrier(0)
#define FENCE() asm volatile("" ::: "memory")
#define PIN(x) asm volatile("" : "+v"(x))

template <typename T> __device__ __forceinline__ T lds_ld(LAS unsigned char* L, int off) { return *(LAS T*)(L + off); }
template <typename T> __device__ __forceinline__ void lds_st(LAS unsigned char* L, int off, T v) { *(LAS T*)(L + off) = v; }
template <int I, int N, typename F> __device__ __forceinline__ void sfor(F&& f) {
  if constexpr (I < N) { f(std::integral_constant<int, I>{}); sfor<I + 1, N>(f); }
}
template <int I> using IC = std::integral_constant<int, I>;
#define AINL __attribute__((always_inline))

struct Sel { unsigned s[2][4]; };
__device__ __forceinline__ v4u diagA(unsigned X, const Sel& S, int par) {
  v4u a;
#pragma unroll
  for (int d = 0; d < 4; ++d) a[d] = __builtin_amdgcn_perm(X, X, S.s[par][d]);
  return a;
}
__device__ __forceinline__ v4u tag4(f4 v, unsigned T) {
  v4u r;
#pragma unroll
  for (int e = 0; e < 4; ++e) r[e] = ((__float_as_uint(v[e]) + 0x80u) & 0xffffff00u) | ((T >> (8 * e)) & 0xffu);
  return r;
}
__device__ __forceinline__ bool tagok4(v4u v, unsigned T) {
  const unsigned lo = __builtin_amdgcn_perm(v.y, v.x, 0x0c0c0400u), hi = __builtin_amdgcn_perm(v.w, v.z, 0x04000c0cu);
  return (lo | hi) == T;
}
__device__ __forceinline__ unsigned long long ballot(bool p) { return __builtin_amdgcn_ballot_w64(p); }

#define STAMPK 0
#define STAMP(n) do { } while (0)
#define KSTAMP(k) do { } while (0)

__global__ void __launch_bounds__(512, 2)
fused_kernel(const void* px, const void* pk, const void* pv, const void* pwq,
             const void* pwo, const void* pw1, const void* pw2, const void* pc,
             const void* pwm, void* pout, void* pws) {
  __shared__ __attribute__((aligned(16))) unsigned char smem[L_END];
  LAS unsigned char* L = (LAS unsigned char*)smem;
  const int t = threadIdx.x, lane = t & 63, w = __builtin_amdgcn_readfirstlane(t >> 6);
  const int wg = blockIdx.x, xg = wg & 7, e8 = wg >> 3, b = wg >> 5, h = xg * 4 + (e8 & 3);
  const int g = lane >> 4, c = lane & 15;
  const bool mlp = h < 27;
  STAMP(0);
  const rsrc_t rsWS = mkrs(pws, WS_BYTES);
  const rsrc_t rsK = mkrs((const char*)pk + ((size_t)(b * 32 + h) * 4096 + 3072) * 256, 262144);
  const rsrc_t rsV = mkrs((const char*)pv + ((size_t)(b * 32 + h) * 4096 + 3072) * 256, 262144);
  const rsrc_t rsWQ = mkrs(pwq, 640 * 4096 * 2), rsWO = mkrs(pwo, 4096 * 640 * 2), rsW1 = mkrs(pw1, 640 * 1728 * 2), rsW2 = mkrs(pw2, 1728 * 640 * 2);
  const rsrc_t rsWM = mkrs(pwm, 640 * 3840 * 2), rsC = mkrs(pc, 10240), rsX = mkrs(px, 10240), rsOUT = mkrs(pout, 10240);

  const bool role = wg < 240;
  const int ta = wg % 60, kq = wg / 60;
  const unsigned T = *(const __attribute__((address_space(4))) unsigned*)((const char*)pws + WS_EPOCH) + 1u;
  v4u cv0 = ld<AUX_NT>(rsC, (role && t < 160) ? (t / 20) * 1280 + kq * 320 + (t % 20) * 16 : OOB, 0);
  const unsigned wmoff = (unsigned)(160 * kq + 8 * w + (lane >> 3)) * 7680u + ta * 128 + (lane & 7) * 16;
  v4u wm0 = ld<AUX_NT>(rsWM, role ? wmoff : OOB, 0);
  v4u wm1 = ld<AUX_NT>(rsWM, role ? wmoff : OOB, 64 * 7680);
  v4u wm2 = ld<AUX_NT>(rsWM, (role && w < 4) ? wmoff : OOB, 128 * 7680);
  if (t < 8) lds_st<unsigned>(L, L_FLAG + t * 4, 0u);
  SB();
  asm volatile("s_barrier" ::: "memory");
  unsigned po[4]; v4u pvv[4];
  v4u WQ[3], WO[20], KB[NR], KL[TW + 1];
  const unsigned offWQ = NOW ? OOB : (unsigned)(80 * b + 20 * g + w) * 8192u + h * 256 + c * 16;
  const unsigned offWO = NOW ? OOB : (unsigned)(h * 128 + 16 * w + 4 * g) * 1280u + c * 16;
  const unsigned offKV = w * 1024 + lane * 16;
  const int ksoff = L_KS + w * (NLDS * 1024) + lane * 16;
  auto ldwo = [&](auto ic) AINL { constexpr int i = decltype(ic)::value; WO[i] = ld<AUX_W>(rsWO, offWO, (i / 5) * 1280 + (i % 5) * 256); };
  auto a_issue = [&](auto mc) AINL {
    constexpr It it = a_item(decltype(mc)::value);
    if constexpr (it.kind == 4) WQ[it.idx] = ld<AUX_NT>(rsWQ, (it.idx < 2 || w < 4) ? offWQ : OOB, it.idx * 8 * 8192);
    else if constexpr (it.kind == 2) ldwo(IC<it.idx>{});
    else if constexpr (it.kind == 5 || it.kind == 6) pvv[it.idx] = ldsc(rsWS, po[it.idx]);
    else if constexpr (it.idx < NR) KB[it.idx] = ld<AUX_K>(rsK, offKV, it.idx * 8192);
    else KL[(it.idx - NR) % (TW + 1)] = ld<AUX_K>(rsK, offKV, it.idx * 8192);
  };
  constexpr int NINIT0 = 2, NINIT1 = 4, NINIT2 = 5, NINIT3 = 7, NINIT = 8;
  static_assert(NINIT >= TW && a_item(NINIT - 1).idx < NR, "no parking during P1");
  sfor<0, NINIT0>(a_issue);
  SB();
  STAMP(1);
  WAITVM(NINIT0);
  sfor<NINIT0, NINIT1>(a_issue);
  SB();
  if (wg >= 240 && wg < 248 && w == 0) {
    const v4u xa = ld<AUX_NT>(rsX, (wg - 240) * 1280 + lane * 16, 0);
    const unsigned xb = __builtin_amdgcn_raw_buffer_load_b32(rsX, (wg - 240) * 1280 + 1024 + lane * 4, 0, AUX_NT);
    float xv[10];
#pragma unroll
    for (int d = 0; d < 4; ++d) { xv[2 * d] = bflo(xa[d]); xv[2 * d + 1] = bfhi(xa[d]); }
    xv[8] = bflo(xb); xv[9] = bfhi(xb);
    const float sm = ((xv[0] + xv[1]) + (xv[2] + xv[3])) + ((xv[4] + xv[5]) + (xv[6] + xv[7])) + (xv[8] + xv[9]);
    const float mean = wave_sum(sm) * (1.f / 640.f);
    float q2 = 0.f;
#pragma unroll
    for (int d = 0; d < 10; ++d) { const float dd = xv[d] - mean; q2 = fmaf(dd, dd, q2); }
    const float var = rbf(wave_sum(q2) * (1.f / 640.f));
    const float mb = rbf(mean), den = rbf(__builtin_amdgcn_sqrtf(rbf(var + 1e-5f)));
    if (lane == 0) stsc(rsWS, WS_XST + (wg - 240) * 16, v4u{__float_as_uint(mb) | (T & 0xffu), __float_as_uint(den) | ((T >> 8) & 0xffu), (T >> 16) & 0xffu, T >> 24});
  }
  {
    auto silu8 = [&](v4u v) AINL {
      v4u r;
#pragma unroll
      for (int d = 0; d < 4; ++d) r[d] = cvtpk(fsilu(bflo(v[d])), fsilu(bfhi(v[d])));
      return r;
    };
    if (t < 160) lds_st<v4u>(L, L_SC + (t / 20) * SC_STRIDE + (t % 20) * 16, silu8(cv0));
    lds_st<v4u>(L, L_WL + w * 1024 + lane * 16, wm0);
    lds_st<v4u>(L, L_WL + (w + 8) * 1024 + lane * 16, wm1);
    if (w < 4) lds_st<v4u>(L, L_WL + (w + 16) * 1024 + lane * 16, wm2);
  }
  SB();
  sfor<NINIT1, NINIT2>(a_issue);
  v2u xh = __builtin_amdgcn_raw_buffer_load_b64(rsX, lane < 20 ? w * 1280 + 160 * b + lane * 8 : OOB, 0, AUX_NT);
  v2u xown = __builtin_amdgcn_raw_buffer_load_b64(rsX, (w == 4 && lane < 5) ? b * 1280 + 40 * h + lane * 8 : OOB, 0, AUX_NT);
  SB();
  BAR();
  STAMP(2);
  sfor<NINIT2, NINIT3>([&](auto mc) AINL { WAITVM(TW - 2 + 2); a_issue(mc); SB(); });
  {
    f4 acc = {0.f, 0.f, 0.f, 0.f};
    const int tl = w & 3, s0_ = (w >> 2) * 3;
    const int trbase = L_WL + (8 * g + ((lane & 15) >> 2)) * 128 + tl * 32 + (lane & 3) * 8;
#pragma unroll
    for (int n = 0; n < 3; ++n) {
      if (n < 2 || w < 4) {
        const int s = s0_ + n;
        const v4u a = lds_ld<v4u>(L, L_SC + (c & 7) * SC_STRIDE + s * 64 + g * 16);
        const s4 b0 = __builtin_amdgcn_ds_read_tr16_b64_v4i16((LAS s4*)(L + trbase + s * 4096));
        const s4 b1 = __builtin_amdgcn_ds_read_tr16_b64_v4i16((LAS s4*)(L + trbase + s * 4096 + 512));
        const v2u u0 = __builtin_bit_cast(v2u, b0), u1 = __builtin_bit_cast(v2u, b1);
        acc = mfma(a, v4u{u0.x, u0.y, u1.x, u1.y}, acc);
      }
    }
    if (g < 2) {
#pragma unroll
      for (int r = 0; r < 4; ++r) lds_st<float>(L, L_PART + (w >> 2) * 2048 + (4 * g + r) * 256 + (16 * tl + c) * 4, acc[r]);
    }
  }
  sfor<NINIT3, NINIT>([&](auto mc) AINL { WAITVM(TW - 2 + 2); a_issue(mc); SB(); });
  BAR();
  {
    const f4 s = lds_ld<f4>(L, L_PART + (t & 127) * 16) + lds_ld<f4>(L, L_PART + 2048 + (t & 127) * 16);
    unsigned o = OOB;
    if (t < 128) { if (role) o = WS_PM + (((t >> 4) * 4 + kq) * 3840 + ta * 64) * 4 + (t & 15) * 16; }
    else if (t == 128) o = WS_ALIVE + wg * 16;
    stsc(rsWS, o, tag4(s, T));
  }
  STAMP(3);
  Sel S;
  {
    const int e = c & 7;
#pragma unroll
    for (int par = 0; par < 2; ++par)
#pragma unroll
      for (int d = 0; d < 4; ++d) {
        const unsigned lo = (unsigned)(2 * par) | ((unsigned)(2 * par + 1) << 8);
        S.s[par][d] = (d == (e >> 1)) ? ((e & 1) ? (0x0c0cu | (lo << 16)) : (0x0c0c0000u | lo)) : 0x0c0c0c0cu;
      }
  }
  unsigned long long pm0, pm1, pm2, pm3;
  auto pmasks = [&]() AINL { pm0 = ballot(po[0] != OOB); pm1 = ballot(po[1] != OOB); pm2 = ballot(po[2] != OOB); pm3 = ballot(po[3] != OOB); };
#pragma unroll
  for (int q = 0; q < 4; ++q) po[q] = lane < 40 ? WS_PM + ((w * 4 + q) * 3840 + (lane / 20) * 640 + 80 * b + 4 * (lane % 20)) * 4 : OOB;
  if (lane == 40) po[0] = WS_XST + w * 16;
  pmasks();
  auto pissue = [&]() AINL {
#pragma unroll
    for (int q = 0; q < 4; ++q) pvv[q] = ldsc(rsWS, po[q]);
  };
  auto pok = [&]() AINL {
    return (ballot(tagok4(pvv[0], T)) & pm0) == pm0 && (ballot(tagok4(pvv[1], T)) & pm1) == pm1 && (ballot(tagok4(pvv[2], T)) & pm2) == pm2 && (ballot(tagok4(pvv[3], T)) & pm3) == pm3;
  };
  auto preload = [&]() AINL {
#pragma unroll
    for (int q = 0; q < 4; ++q) { const bool k1 = tagok4(pvv[q], T); const v4u nv = ldsc(rsWS, k1 ? OOB : po[q]); pvv[q] = k1 ? pvv[q] : nv; }
  };
  auto pblock = [&]() AINL {
    unsigned spins = 0;
    while (!pok() && ++spins < SPIN_CAP) { __builtin_amdgcn_s_sleep(2); FENCE(); preload(); }
  };
  auto psum = [&]() AINL {
    f4 mv;
#pragma unroll
    for (int e = 0; e < 4; ++e) mv[e] = (__uint_as_float(pvv[0][e] & 0xffffff00u) + __uint_as_float(pvv[1][e] & 0xffffff00u)) +
                                        (__uint_as_float(pvv[2][e] & 0xffffff00u) + __uint_as_float(pvv[3][e] & 0xffffff00u));
    float a0 = mv[0], a1 = mv[1], a2 = mv[2], a3 = mv[3]; rbf2(a0, a1); rbf2(a2, a3);
    return f4{a0, a1, a2, a3};
  };
  bool hdone = false;
  auto rprocess = [&]() AINL {
    const f4 mv = psum();
    const float xmean = __uint_as_float((unsigned)__builtin_amdgcn_readlane((int)pvv[0].x, 40) & 0xffff0000u);
    const float xrden = frcp(__uint_as_float((unsigned)__builtin_amdgcn_readlane((int)pvv[0].y, 40) & 0xffff0000u));
    float lnv[4] = {bflo(xh.x) - xmean, bfhi(xh.x) - xmean, bflo(xh.y) - xmean, bfhi(xh.y) - xmean};
    rbf2(lnv[0], lnv[1]); rbf2(lnv[2], lnv[3]);
#pragma unroll
    for (int e = 0; e < 4; ++e) lnv[e] *= xrden;
    rbf2(lnv[0], lnv[1]); rbf2(lnv[2], lnv[3]);
    float hv[4];
#pragma unroll
    for (int e = 0; e < 4; ++e) {
      const float ca = __uint_as_float(__builtin_amdgcn_ds_bpermute(((lane + 20) & 63) * 4, (int)__float_as_uint(mv[e])));
      hv[e] = rbf(lnv[e] * rbf(1.f + ca)) + mv[e];
    }
    if (lane < 20) lds_st<v2u>(L, L_HB + w * 160 + lane * 8, v2u{cvtpk(hv[0], hv[1]), cvtpk(hv[2], hv[3])});
    asm volatile("s_waitcnt lgkmcnt(0)" ::: "memory");
    if (lane == 0) __hip_atomic_fetch_add((LAS unsigned*)(L + L_FLAG), 1u, __ATOMIC_RELAXED, __HIP_MEMORY_SCOPE_WORKGROUP);
    STAMP(14);
  };
  auto mprocess = [&]() AINL {
    const f4 mv = psum();
    v4u msg;
#pragma unroll
    for (int e = 0; e < 4; ++e) msg[e] = (__float_as_uint(rbf(lane >= 10 ? 1.f + mv[e] : mv[e])) & 0xffff0000u) | ((T >> (8 * e)) & 0xffu);
    if (lane >= 5 && lane < 15) stsc(rsWS, WS_AGP + (b * 32 + h) * 256 + lane * 16, msg);
    else if (lane < 5) lds_st<f4>(L, L_GAB + lane * 16, mv);
    else if (lane < 20) lds_st<f4>(L, L_GAB + 80 + (lane - 15) * 16, mv);
    STAMP(17);
  };
  auto bfpack = [&](v4u v) AINL { return v2u{__builtin_amdgcn_perm(v.y, v.x, 0x07060302u), __builtin_amdgcn_perm(v.w, v.z, 0x07060302u)}; };
  auto hflag = [&]() AINL { FENCE(); return __builtin_amdgcn_readfirstlane((int)*(volatile LAS unsigned*)(L + L_FLAG)) == 8; };
  auto hstep = [&]() AINL {
    const int pi = w & 3, hf = w >> 2;
    unsigned X[5]; v4u Bp[10];
#pragma unroll
    for (int d = 0; d < 5; ++d) X[d] = lds_ld<unsigned>(L, L_HB + (2 * pi + (c >> 3)) * 160 + 40 * g + 20 * hf + d * 4);
#pragma unroll
    for (int i = 0; i < 10; ++i) Bp[i] = lds_ld<v4u>(L, L_WQL + (10 * hf + i) * 1024 + lane * 16);
    f4 a0 = {0.f, 0.f, 0.f, 0.f}, a1 = {0.f, 0.f, 0.f, 0.f};
#pragma unroll
    for (int i = 0; i < 10; i += 2) { a0 = mfma(diagA(X[i >> 1], S, 0), Bp[i], a0); a1 = mfma(diagA(X[i >> 1], S, 1), Bp[i + 1], a1); }
    a0 += a1;
    lds_st<f4>(L, L_QP8 + w * 1024 + lane * 16, a0);
    asm volatile("s_waitcnt lgkmcnt(0)" ::: "memory");
    unsigned old = 0;
    if (lane == 0) old = __hip_atomic_fetch_add((LAS unsigned*)(L + L_FLAG + 4 + pi * 4), 1u, __ATOMIC_RELAXED, __HIP_MEMORY_SCOPE_WORKGROUP);
    if (__builtin_amdgcn_readfirstlane((int)old) == 1) {
      FENCE();
      const f4 lo = lds_ld<f4>(L, L_QP8 + pi * 1024 + lane * 16), hi = lds_ld<f4>(L, L_QP8 + (pi + 4) * 1024 + lane * 16);
      stsc(rsWS, WS_QPM + (h * 8 + b) * 4096 + (2 * pi + (g >> 1)) * 512 + (2 * c + (g & 1)) * 16, tag4(lo + hi, T));
      STAMP(16);
    }
  };
  static_assert(a_kpos(RL) >= a_kpos(6) + 1 + TW, "wq parked before the first h arrival");
  static_assert(a_kpos(RL) >= a_kpos(RK + 3) + 1 + TW - 1 && RL <= HK && HK + (HN - 1) * HD < QK - 1 && RL < QK - 1 && QK + 3 < 31, "stage order");
  auto pnone = [&]() AINL {
#pragma unroll
    for (int q = 0; q < 4; ++q) po[q] = OOB;
  };
  sfor<NINIT, NA>([&](auto mc) AINL {
    constexpr int m = decltype(mc)::value;
    constexpr It it = a_item(m);
    constexpr int k = it.kind == 0 ? it.idx : -100;
    WAITW(TW - 1 + (m - (TW - DA_LO) < NINIT ? 1 : 0), DA_LO, DA_HI);
    {
      constexpr It ol = a_item(m - TW);
      if constexpr (ol.kind == 4) { if (ol.idx < 2 || w < 4) lds_st<v4u>(L, L_WQL + (w + 8 * ol.idx) * 1024 + lane * 16, WQ[ol.idx]); }
      if constexpr (!NOPARK && ol.kind == 0 && ol.idx >= NR) lds_st<v4u>(L, ksoff + (ol.idx - NR) * 1024, KL[(ol.idx - NR) % (TW + 1)]);
    }
    a_issue(mc);
    if constexpr (k == 9) { if (w == 4 && lane < 5) lds_st<v2u>(L, L_XR + lane * 8, xown); }
    KSTAMP(k);
    if constexpr (k == 8) STAMP(18);
    if constexpr (k == 16) STAMP(19);
    if constexpr (k == 24) STAMP(20);
    if constexpr (!NODUTY) {
    if constexpr (k == RL) { pblock(); rprocess(); }
    if constexpr (k >= HK && k < HK + (HN - 1) * HD && (k - HK) % HD == 0) { if (!hdone && hflag()) { hstep(); hdone = true; } }
    if constexpr (k == HK + (HN - 1) * HD) {
      if (!hdone) { unsigned spins = 0; while (!hflag() && ++spins < (SPIN_CAP << 3)) __builtin_amdgcn_s_sleep(1); hstep(); }
    }
    if constexpr (k == QK - 1) {
#pragma unroll
      for (int q = 0; q < 4; ++q) {
        unsigned o = OOB;
        if (w == 5) { if (lane < 20) o = WS_PM + ((b * 4 + q) * 3840 + (2 + lane / 5) * 640 + 20 * h + 4 * (lane % 5)) * 4; }
        else if (w == 6) o = WS_QPM + ((h * 8 + 2 * q + (lane >> 5)) * 8 + b) * 512 + (lane & 31) * 16;
        else if (w == 4 && wg == 0) o = WS_ALIVE + (64 * q + lane) * 16;
        po[q] = o;
      }
      pmasks();
    }
    }
    SB();
  });
  STAMP(4);
  v4u W1[10], W2[10], VR[VRN];
  const unsigned offW1 = (mlp && !NOW) ? (unsigned)(80 * w + 40 * (c >> 3) + 10 * g) * 3456u + (64 * h + 8 * (c & 7)) * 2 : OOB;
  const unsigned offW2 = (mlp && !NOW) ? (unsigned)(64 * h + 8 * w + 2 * g) * 1280u + c * 16 : OOB;
  auto b_issue = [&](auto mc) AINL {
    constexpr It it = b_item(decltype(mc)::value);
    if constexpr (it.kind == 1) VR[it.idx % VRN] = ld<AUX_V>(rsV, offKV, it.idx * 8192);
    else if constexpr (it.kind == 2) ldwo(IC<it.idx>{});
    else W1[it.idx] = ld<AUX_W>(rsW1, offW1, it.idx * 3456);
    SB();
  };
#define VTICK(k) do { WAITW(TW - 1, DB_LO, DB_HI); b_issue(IC<(k)>{}); } while (0)
  sfor<0, VPRE>([&](auto kc) AINL { WAITW(TW - 1, DB_LO, DB_HI); b_issue(kc); });
  if (!NODUTY && w == 5) { pblock(); mprocess(); }
  if (!NODUTY && (w == 6 || w == 4)) {
    pblock();
    if (w == 6) {
      f4 q4;
#pragma unroll
      for (int e = 0; e < 4; ++e) q4[e] = x32_sum((__uint_as_float(pvv[0][e] & 0xffffff00u) + __uint_as_float(pvv[1][e] & 0xffffff00u)) +
                                                  (__uint_as_float(pvv[2][e] & 0xffffff00u) + __uint_as_float(pvv[3][e] & 0xffffff00u)));
      if (lane < 32) lds_st<v2u>(L, L_QB + lane * 8, v2u{cvtpk(q4[0], q4[1]), cvtpk(q4[2], q4[3])});
    } else {
      if (wg == 0 && lane == 0) __builtin_amdgcn_raw_buffer_store_b32(T, rsWS, WS_EPOCH, 0, AUX_SC1);
    }
  }
  BAR();
  STAMP(5);
  const v4u qr = lds_ld<v4u>(L, L_QB + c * 16);
  STAMP(6);
  auto kdot = [&](v4u k) AINL { return dot2(k.w, qr[3], dot2(k.z, qr[2], dot2(k.y, qr[1], dot2(k.x, qr[0], 0.f)))); };
  auto treduce = [&](float (&xs)[16]) AINL {
    float x[8];
#pragma unroll
    for (int i = 0; i < 8; ++i) { const float lo = xs[i] + dppf<XOR8>(xs[i]), hi = xs[i + 8] + dppf<XOR8>(xs[i + 8]); x[i] = (c & 8) ? hi : lo; }
#pragma unroll
    for (int i = 0; i < 4; ++i) { const float lo = x[i] + dppf<XOR7>(x[i]), hi = x[i + 4] + dppf<XOR7>(x[i + 4]); x[i] = (c & 4) ? hi : lo; }
#pragma unroll
    for (int i = 0; i < 2; ++i) { const float lo = x[i] + dppf<XOR2>(x[i]), hi = x[i + 2] + dppf<XOR2>(x[i + 2]); x[i] = (c & 2) ? hi : lo; }
    const float lo = x[0] + dppf<XOR1>(x[0]), hi = x[1] + dppf<XOR1>(x[1]);
    return (c & 1) ? hi : lo;
  };
  constexpr float C2 = 0.08838834764831845f * 1.4426950408889634f;
  float mw, fw = 0.f;
  unsigned ppk;
  {
    float xs[16];
#pragma unroll
    for (int j = 0; j < 16; ++j) xs[j] = kdot(KB[j]);
    SB();
    VTICK(VPRE);
    const float s0 = treduce(xs);
    SB();
    VTICK(VPRE + 1);
#pragma unroll
    for (int j = 0; j <= LASTPARK - NR; ++j) xs[j] = kdot(lds_ld<v4u>(L, ksoff + j * 1024));
#pragma unroll
    for (int j = LASTPARK - NR + 1; j < 16; ++j) xs[j] = kdot(KL[j % (TW + 1)]);
    SB();
    sfor<VPRE + 2, (VPRE + 4 < VRN + WEARLY ? VPRE + 4 : VRN + WEARLY)>([&](auto kc) AINL { WAITW(TW - 1, DB_LO, DB_HI); b_issue(kc); });
    const float s1 = treduce(xs);
    SB();
    sfor<(VPRE + 4 < VRN + WEARLY ? VPRE + 4 : VRN + WEARLY), VRN + WEARLY>([&](auto kc) AINL { WAITW(TW - 1, DB_LO, DB_HI); b_issue(kc); });
    mw = wave_max(fmaxf(s0, s1));
    const float p0 = __builtin_amdgcn_exp2f((s0 - mw) * C2), p1 = __builtin_amdgcn_exp2f((s1 - mw) * C2);
    const float lw = wave_sum(p0 + p1);
    ppk = cvtpk(p0, p1);
    if (lane == 0) lds_st<v2u>(L, L_ML + w * 8, v2u{__float_as_uint(mw), __float_as_uint(lw)});
  }
  STAMP(15);
  f4 oacc = {0.f, 0.f, 0.f, 0.f};
  unsigned touch0 = 0, touch1 = 0;
  auto pvstep = [&](auto jc) AINL {
    constexpr int j = decltype(jc)::value;
    const unsigned X = dppu<0x150 + (j & 15)>(ppk);
    oacc = mfma(diagA(X, S, j >> 4), VR[j % VRN], oacc);
    if constexpr (j == 4) {
#pragma unroll
      for (int q = 0; q < 4; ++q) { const int idx = 64 * q + lane; po[q] = ((w == 1 || w == 2) && mlp && idx < 160) ? WS_AGP + (b * 32 + idx / 5) * 256 + (5 * w + idx % 5) * 16 : OOB; }
      pmasks(); pissue();
    }
    if constexpr (j == 14) {
      if (!NODUTY && (w == 1 || w == 2)) {
        pblock();
#pragma unroll
        for (int n = 0; n < 3; ++n) {
          const int idx = 64 * n + lane;
          if (po[n] != OOB) lds_st<v2u>(L, (w == 1 ? L_SB : L_CB) + (20 * (idx / 5) + 4 * (idx % 5)) * 2, bfpack(pvv[n]));
        }
      }
    }
    if constexpr (j == 8) {
      unsigned o0 = OOB, o1 = OOB;
      if (w == 4) {
        const int u = lane & 31; const int wgu = (b << 5) | ((u & 3) << 3) | (u >> 2);
        o0 = (lane < 32 ? WS_OUT1 : WS_OUT2) + wgu * 4096 + h * 128;
        o1 = lane < 32 ? WS_AG + (b * 32 + lane) * 128 : OOB;
      } else if (w == 0) { o0 = (lane < 32 ? WS_OUT1 : WS_OUT2) + wg * 4096 + (lane & 31) * 128; }
      touch0 = __builtin_amdgcn_raw_buffer_load_b32(rsWS, (int)o0, 0, AUX_SC1);
      touch1 = __builtin_amdgcn_raw_buffer_load_b32(rsWS, (int)o1, 0, AUX_SC1);
    }
    if constexpr (j == 16) { asm volatile("" :: "v"(touch0), "v"(touch1)); }
    if constexpr (j == 10) {
      BAR();
      float M = -INFINITY;
      v2u ml[8];
#pragma unroll
      for (int ww = 0; ww < 8; ++ww) { ml[ww] = lds_ld<v2u>(L, L_ML + ww * 8); M = fmaxf(M, __uint_as_float(ml[ww].x)); }
      float Ls = 0.f;
#pragma unroll
      for (int ww = 0; ww < 8; ++ww) Ls += __builtin_amdgcn_exp2f((__uint_as_float(ml[ww].x) - M) * C2) * __uint_as_float(ml[ww].y);
      fw = __builtin_amdgcn_exp2f((mw - M) * C2) * frcp(Ls);
      PIN(fw);
    }
  };
  sfor<VRN + WEARLY, NB>([&](auto mc) AINL {
    constexpr int m = decltype(mc)::value;
    constexpr It it = b_item(m);
    if constexpr (it.kind == 1) pvstep(IC<it.idx - VRN>{});
    if constexpr (m == NBV) sfor<32 - VRN, 22>([&](auto jc) AINL { pvstep(jc); SB(); });
    if constexpr (it.kind != 3) WAITW(TW - 1 + b_extra(m), DB_LO, DB_HI);
    else { pvstep(IC<22 + it.idx>{}); }
    b_issue(mc);
  });
  STAMP(7);
  {
    const f4 ov = oacc * fw;
    if (lane < 32) lds_st<f4>(L, L_OP + w * 512 + (8 * c + 4 * g) * 4, ov);
  }
  BAR();
  const int ypoff = L_YP + w * 2560 + (8 * c + 4 * (g & 1)) * 4;
  {
    const int kap = (c >> 1) & 3, sig = (c & 1) + 2 * (c >> 3);
    const int ko = (16 * w + 4 * g + kap) * 4;
    float o = lds_ld<float>(L, L_OP + (2 * sig) * 512 + ko) + lds_ld<float>(L, L_OP + (2 * sig + 1) * 512 + ko);
    o += dppf<XOR1>(o); o += dppf<XOR8>(o);
    const unsigned P = cvtpk(dppf<QB0>(o), dppf<QB2>(o)), Q = dppu<XOR7>(P);
    const unsigned X0 = (c & 4) ? Q : P, X1 = (c & 4) ? P : Q;
    f4 y[5];
    { const v4u A = diagA(X0, S, 0);
#pragma unroll
      for (int cb = 0; cb < 5; ++cb) y[cb] = mfma(A, WO[cb], f4{0.f, 0.f, 0.f, 0.f}); }
    { const v4u A = diagA(X0, S, 1);
#pragma unroll
      for (int cb = 0; cb < 5; ++cb) y[cb] = mfma(A, WO[5 + cb], y[cb]); }
    { const v4u A = diagA(X1, S, 0);
#pragma unroll
      for (int cb = 0; cb < 5; ++cb) y[cb] = mfma(A, WO[10 + cb], y[cb]); }
    { const v4u A = diagA(X1, S, 1);
#pragma unroll
      for (int cb = 0; cb < 5; ++cb) y[cb] = mfma(A, WO[15 + cb], y[cb]); }
    if (lane < 32) {
#pragma unroll
      for (int cb = 0; cb < 5; ++cb) lds_st<f4>(L, ypoff + cb * 512, y[cb]);
    }
  }
  BAR();
  STAMP(8);
  auto publish = [&](int wsoff) AINL {
    const int k = t & 7, j = t >> 3;
    const int o = L_YP + (20 * j + 4 * (k < 5 ? k : 0)) * 4;
    f4 s0 = lds_ld<f4>(L, o) + lds_ld<f4>(L, o + 2560), s1 = lds_ld<f4>(L, o + 2 * 2560) + lds_ld<f4>(L, o + 3 * 2560);
    f4 s2 = lds_ld<f4>(L, o + 4 * 2560) + lds_ld<f4>(L, o + 5 * 2560), s3 = lds_ld<f4>(L, o + 6 * 2560) + lds_ld<f4>(L, o + 7 * 2560);
    stsc(rsWS, wsoff + t * 16, tag4((s0 + s1) + (s2 + s3), T));
  };
  float x1v[4] = {0.f, 0.f, 0.f, 0.f};
  auto rs_gather = [&](int wsbase, int nprod, int delay_done) AINL -> f4 {
    const int pl = lane >> 3, k = lane & 7;
    unsigned off[4]; v4u v[4];
#pragma unroll
    for (int q = 0; q < 4; ++q) {
      const int u = 8 * q + pl;
      const int wgu = (b << 5) | ((u & 3) << 3) | (u >> 2);
      off[q] = (k < 5 && u < nprod) ? wsbase + wgu * 4096 + h * 128 + k * 16 : OOB;
      PIN(off[q]);
    }
    const unsigned long long m0 = ballot(off[0] != OOB), m3 = ballot(off[3] != OOB);
    __builtin_amdgcn_s_sleep(0);
    (void)delay_done;
#pragma unroll
    for (int q = 0; q < 4; ++q) v[q] = ldsc(rsWS, off[q]);
    unsigned spins = 0;
    for (;;) {
      const bool k0 = tagok4(v[0], T), k1 = tagok4(v[1], T), k2 = tagok4(v[2], T), k3 = tagok4(v[3], T);
      if ((ballot(k0) & ballot(k1) & ballot(k2) & m0) == m0 && (ballot(k3) & m3) == m3) break;
      if (++spins > SPIN_CAP) break;
      __builtin_amdgcn_s_sleep(POLL_SLEEP);
      FENCE();
      const v4u n0 = ldsc(rsWS, k0 ? OOB : off[0]), n1 = ldsc(rsWS, k1 ? OOB : off[1]), n2 = ldsc(rsWS, k2 ? OOB : off[2]), n3 = ldsc(rsWS, k3 ? OOB : off[3]);
      v[0] = k0 ? v[0] : n0; v[1] = k1 ? v[1] : n1; v[2] = k2 ? v[2] : n2; v[3] = k3 ? v[3] : n3;
    }
    f4 s;
#pragma unroll
    for (int e = 0; e < 4; ++e) {
      float a = (__uint_as_float(v[0][e] & 0xffffff00u) + __uint_as_float(v[1][e] & 0xffffff00u)) + (__uint_as_float(v[2][e] & 0xffffff00u) + __uint_as_float(v[3][e] & 0xffffff00u));
      a += dppf<XOR8>(a); s[e] = x32_sum(x16_sum(a));
    }
    return s;
  };
  if (w < 4) publish(WS_OUT1 + wg * 4096);
  if (w != 4) {
#pragma unroll
    for (int i = 0; i < 10; ++i) W2[i] = ld<AUX_W>(rsW2, offW2, (i / 5) * 1280 + (i % 5) * 256);
  }
  if (w == 4) {
    const v2u xr = lds_ld<v2u>(L, L_XR + (lane & 7) * 8); const f4 gg = lds_ld<f4>(L, L_GAB + (lane & 7) * 16);
    unsigned off[3]; v4u v[3]; float sbv[12], cbv[12]; int hoff[3];
#pragma unroll
    for (int q = 0; q < 3; ++q) {
      const int idx = 64 * q + lane, jj = idx / 5, k = idx % 5;
      off[q] = (idx < 160 && mlp) ? WS_AG + (b * 32 + jj) * 128 + k * 16 : OOB;
      const int col = idx < 160 ? 20 * jj + 4 * k : 0;
      const v2u s2 = lds_ld<v2u>(L, L_SB + col * 2), c2 = lds_ld<v2u>(L, L_CB + col * 2);
      sbv[4 * q] = bflo(s2.x); sbv[4 * q + 1] = bfhi(s2.x); sbv[4 * q + 2] = bflo(s2.y); sbv[4 * q + 3] = bfhi(s2.y);
      cbv[4 * q] = bflo(c2.x); cbv[4 * q + 1] = bfhi(c2.x); cbv[4 * q + 2] = bflo(c2.y); cbv[4 * q + 3] = bfhi(c2.y);
      hoff[q] = L_H2 + col * 2;
      PIN(off[q]); PIN(hoff[q]);
    }
#pragma unroll
    for (int i = 0; i < 12; ++i) { PIN(sbv[i]); PIN(cbv[i]); }
    float xx[4] = {bflo(xr.x), bfhi(xr.x), bflo(xr.y), bfhi(xr.y)};
    const unsigned long long am0 = ballot(off[0] != OOB), am2 = ballot(off[2] != OOB);
    __builtin_amdgcn_s_sleep(D_RS1);
    const f4 s = rs_gather(WS_OUT1, 32, 0);
    v4u m;
    {
      float a0 = s[0], a1 = s[1], a2 = s[2], a3 = s[3];
      rbf2(a0, a1); rbf2(a2, a3);
      a0 *= gg[0]; a1 *= gg[1]; a2 *= gg[2]; a3 *= gg[3];
      rbf2(a0, a1); rbf2(a2, a3);
      a0 += xx[0]; a1 += xx[1]; a2 += xx[2]; a3 += xx[3];
      const unsigned p01 = cvtpk(a0, a1), p23 = cvtpk(a2, a3);
      m = v4u{(p01 << 16) | (T & 0xffu), (p01 & 0xffff0000u) | ((T >> 8) & 0xffu), (p23 << 16) | ((T >> 16) & 0xffu), (p23 & 0xffff0000u) | (T >> 24)};
      x1v[0] = bflo(p01); x1v[1] = bfhi(p01); x1v[2] = bflo(p23); x1v[3] = bfhi(p23);
    }
    if (lane < 5) stsc(rsWS, WS_AG + (b * 32 + h) * 128 + lane * 16, m);
#pragma unroll
    for (int i = 0; i < 10; ++i) W2[i] = ld<AUX_W>(rsW2, offW2, (i / 5) * 1280 + (i % 5) * 256);
    STAMP(9);
    if (mlp) {
      __builtin_amdgcn_s_sleep(D_AG);
#pragma unroll
      for (int q = 0; q < 3; ++q) v[q] = ldsc(rsWS, off[q]);
      unsigned spins = 0;
      for (;;) {
        const bool k0 = tagok4(v[0], T), k1 = tagok4(v[1], T), k2 = tagok4(v[2], T);
        if ((ballot(k0) & ballot(k1) & am0) == am0 && (ballot(k2) & am2) == am2) break;
        if (++spins > SPIN_CAP) break;
        __builtin_amdgcn_s_sleep(POLL_SLEEP);
        FENCE();
        const v4u n0 = ldsc(rsWS, k0 ? OOB : off[0]), n1 = ldsc(rsWS, k1 ? OOB : off[1]), n2 = ldsc(rsWS, k2 ? OOB : off[2]);
        v[0] = k0 ? v[0] : n0; v[1] = k1 ? v[1] : n1; v[2] = k2 ? v[2] : n2;
      }
      STAMP(13);
      float xv[12]; float sm = 0.f;
#pragma unroll
      for (int q = 0; q < 3; ++q)
#pragma unroll
        for (int e = 0; e < 4; ++e) { xv[4 * q + e] = __uint_as_float(v[q][e] & 0xffff0000u); sm += xv[4 * q + e]; }
      const float mean = wave_sum(sm) * (1.f / 640.f);
      float q2 = 0.f, q3 = 0.f;
#pragma unroll
      for (int i = 0; i < 8; ++i) { const float dd = xv[i] - mean; q2 += dd * dd; }
#pragma unroll
      for (int i = 8; i < 12; ++i) { const float dd = xv[i] - mean; q3 += dd * dd; }
      q2 += lane < 32 ? q3 : 0.f;
      const float var = rbf(wave_sum(q2) * (1.f / 640.f));
      const float mb = rbf(mean), rden = frcp(rbf(__builtin_amdgcn_sqrtf(rbf(var + 1e-5f))));
#pragma unroll
      for (int q = 0; q < 3; ++q) {
        float a[4];
#pragma unroll
        for (int e = 0; e < 4; ++e) a[e] = xv[4 * q + e] - mb;
        rbf2(a[0], a[1]); rbf2(a[2], a[3]);
#pragma unroll
        for (int e = 0; e < 4; ++e) a[e] *= rden;
        rbf2(a[0], a[1]); rbf2(a[2], a[3]);
#pragma unroll
        for (int e = 0; e < 4; ++e) a[e] *= cbv[4 * q + e];
        rbf2(a[0], a[1]); rbf2(a[2], a[3]);
        const v2u o = {cvtpk(a[0] + sbv[4 * q], a[1] + sbv[4 * q + 1]), cvtpk(a[2] + sbv[4 * q + 2], a[3] + sbv[4 * q + 3])};
        if (q < 2 || lane < 32) lds_st<v2u>(L, hoff[q], o);
      }
    }
  }
  if (mlp) {
    BAR();
    STAMP(10);
    {
      unsigned X[5];
#pragma unroll
      for (int d = 0; d < 5; ++d) X[d] = lds_ld<unsigned>(L, L_H2 + 160 * w + 80 * (c >> 3) + 20 * g + d * 4);
      f4 a0 = {0.f, 0.f, 0.f, 0.f}, a1 = {0.f, 0.f, 0.f, 0.f};
#pragma unroll
      for (int p = 0; p < 10; p += 2) { a0 = mfma(diagA(X[p >> 1], S, 0), W1[p], a0); a1 = mfma(diagA(X[p >> 1], S, 1), W1[p + 1], a1); }
      a0 += a1;
      if ((g >> 1) == (c >> 3)) lds_st<f4>(L, L_HP + (2 * w + (c >> 3)) * 256 + (8 * (c & 7) + 4 * (g & 1)) * 4, a0);
    }
    BAR();
    {
      const int kap = (c >> 2) & 1, sig = (c & 3) + 4 * (c >> 3);
      const int n = (8 * w + 2 * g + kap) * 4;
      float hd = lds_ld<float>(L, L_HP + (2 * sig) * 256 + n) + lds_ld<float>(L, L_HP + (2 * sig + 1) * 256 + n);
      hd += dppf<XOR1>(hd); hd += dppf<XOR2>(hd); hd += dppf<XOR8>(hd);
      hd = fsilu(rbf(hd));
      const float ot = dppf<XOR7>(hd);
      const unsigned X = kap ? cvtpk(ot, hd) : cvtpk(hd, ot);
      const v4u A0 = diagA(X, S, 0), A1 = diagA(X, S, 1);
      f4 y[5];
#pragma unroll
      for (int cb = 0; cb < 5; ++cb) y[cb] = mfma(A0, W2[cb], f4{0.f, 0.f, 0.f, 0.f});
#pragma unroll
      for (int cb = 0; cb < 5; ++cb) y[cb] = mfma(A1, W2[5 + cb], y[cb]);
      if (lane < 32) {
#pragma unroll
        for (int cb = 0; cb < 5; ++cb) lds_st<f4>(L, ypoff + cb * 512, y[cb]);
      }
    }
    BAR();
    STAMP(11);
    if (w < 4) publish(WS_OUT2 + wg * 4096);
  }
  if (w == 4) {
    const f4 gg = lds_ld<f4>(L, L_GAB + 80 + (lane & 7) * 16);
    if (!mlp) __builtin_amdgcn_s_sleep(48);
    __builtin_amdgcn_s_sleep(D_RS2);
    const f4 s = rs_gather(WS_OUT2, 27, 0);
    float a0 = s[0], a1 = s[1], a2 = s[2], a3 = s[3];
    rbf2(a0, a1); rbf2(a2, a3);
    a0 *= gg[0]; a1 *= gg[1]; a2 *= gg[2]; a3 *= gg[3];
    rbf2(a0, a1); rbf2(a2, a3);
    if (lane < 5) __builtin_amdgcn_raw_buffer_store_b64(v2u{cvtpk(x1v[0] + a0, x1v[1] + a1), cvtpk(x1v[2] + a2, x1v[3] + a3)}, rsOUT, b * 1280 + 40 * h + lane * 8, 0, 0);
    STAMP(12);
  }
}

extern "C" void kernel_launch(void* const* d_in, const int* in_sizes, int n_in, void* d_out, int out_size, void* d_ws, size_t ws_size,
                              hipStream_t stream) {
  fused_kernel<<<256, 512, 0, stream>>>(d_in[0], d_in[1], d_in[2], d_in[3], d_in[4], d_in[6], d_in[7], d_in[8], d_in[9], d_out, d_ws);
}
```

```cpp
#include <hip/hip_runtime.h>
#include <hip/hip_bf16.h>
#include <stdint.h>
#include <type_traits>

typedef unsigned v4u __attribute__((ext_vector_type(4)));
typedef unsigned v2u __attribute__((ext_vector_type(2)));
typedef float f4 __attribute__((ext_vector_type(4)));
typedef __bf16 bf8 __attribute__((ext_vector_type(8)));
typedef __bf16 bf2 __attribute__((ext_vector_type(2)));
typedef short s4 __attribute__((ext_vector_type(4)));
#define LAS __attribute__((address_space(3)))
typedef __amdgpu_buffer_rsrc_t rsrc_t;

#define AUX_K 2
#define AUX_V 2
#define AUX_W 0
constexpr int AUX_NT = 2, AUX_SC1 = 16;
constexpr unsigned OOB = 0x80000000u;

constexpr int WS_EPOCH = 0;
constexpr int WS_ALIVE = 4096;
constexpr int WS_XST = 16384;
constexpr int WS_AGP = 65536;
constexpr int WS_AG = 131072;
constexpr int WS_PM = 262144;
constexpr int WS_OUT1 = 1 << 20;
constexpr int WS_OUT2 = 2 << 20;
constexpr int WS_QPM = 3 << 20;
constexpr int WS_STAMP = 4 << 20;
constexpr int WS_BYTES = 8 << 20;

#define TW 6
#define DA_LO 1
#define DA_HI 1
#define DB_LO 1
#define DB_HI 0
#define VRN 14
#define VPRE 9
constexpr int NR = 16;
#define RK 12
#define RL 19
#define HK 19
#define HD 2
#define HN 3
#define QK 25
struct It { int kind, idx; };
#define WOA 10
constexpr int NA = 3 + 32 + WOA + 8;
constexpr It a_item(int m) {
  int pos = 0, wn = 0;
  for (int k = 0; k < 32; ++k) {
    if (pos == m) return It{0, k};
    ++pos;
    if (k >= 4 && k < 7) { if (pos == m) return It{4, k - 4}; ++pos; }
    else if (k >= RK && k < RK + 4) { if (pos == m) return It{5, k - RK}; ++pos; }
    else if (k >= QK && k < QK + 4) { if (pos == m) return It{6, k - QK}; ++pos; }
    else if (k >= 7 && k < 31) {
      int idx = 0; for (int u = 7; u < k; ++u) if (!(u >= RK && u < RK + 4) && !(u >= QK && u < QK + 4)) ++idx;
      if (idx * WOA / 16 != (idx + 1) * WOA / 16 && wn < WOA) { if (pos == m) return It{2, wn}; ++pos; ++wn; }
    }
  }
  return It{-1, 0};
}
static_assert(a_item(NA - 1).kind == 0 && a_item(NA - 1).idx == 31, "phase A list");
constexpr int a_kpos(int k) { for (int m = 0; m < NA; ++m) if (a_item(m).kind == 0 && a_item(m).idx == k) return m; return NA; }
#define WEARLY 2
#define WLAST 27
constexpr int NBV = 32 + 20 - WOA, NB = NBV + 10;
constexpr It b_item(int m) {
  if (m < VRN) return It{1, m};
  if (m < VRN + WEARLY) return It{2, WOA + m - VRN};
  if (m >= NBV) return It{3, m - NBV};
  int pos = VRN + WEARLY, wn = WOA + WEARLY;
  for (int v = VRN; v < 32; ++v) {
    if (pos == m) return It{1, v};
    ++pos;
    int target = WOA + WEARLY + ((20 - WOA - WEARLY) * (v - VRN + 1) + (WLAST - VRN)) / (WLAST - VRN + 1);
    if (target > 20) target = 20;
    while (wn < target) { if (pos == m) return It{2, wn}; ++pos; ++wn; }
  }
  return It{-1, 0};
}
static_assert(b_item(NBV - 1).kind == 1 && b_item(NBV - 1).idx == 31, "phase B list");
constexpr int b_vpos(int k) { for (int m = 0; m < NB; ++m) if (b_item(m).kind == 1 && b_item(m).idx == k) return m; return NB; }
constexpr int b_extra(int m) { int n = 0; const int p4 = b_vpos(4 + VRN), p8 = b_vpos(8 + VRN); if (m >= p4 && m - p4 < TW) n += 4; if (m >= p8 && m - p8 < TW) n += 2; return n; }
constexpr int a_lastpark_f() { int k = NR - 1; for (int m = 0; m + TW < NA; ++m) if (a_item(m).kind == 0 && a_item(m).idx > k) k = a_item(m).idx; return k; }
constexpr int LASTPARK = a_lastpark_f();
constexpr int NLDS = LASTPARK - NR + 1;

constexpr int L_WL = 0;
constexpr int L_SC = 20480;
constexpr int SC_STRIDE = 336;
constexpr int L_PART = L_SC + 2688;
constexpr int L_WQL = 0;
constexpr int L_QP8 = 20480;
constexpr int L_HB = 32768;
constexpr int L_SB = L_HB + 1280;
constexpr int L_CB = L_SB + 1280;
constexpr int L_GAB = L_CB + 1280;
constexpr int L_XR = L_GAB + 160;
constexpr int L_ML = L_XR + 64;
constexpr int L_FLAG = L_ML + 64;
constexpr int L_QB = L_FLAG + 32;
constexpr int L_H2 = L_QB + 256;
constexpr int L_OP = L_H2 + 1280;
constexpr int L_HP = L_OP + 4096;
constexpr int L_KS = L_HP + 4096;
constexpr int L_YP = L_KS;
constexpr int L_END = L_KS + (8 * NLDS * 1024 > 20480 ? 8 * NLDS * 1024 : 20480);
static_assert(L_QP8 + 8192 <= 32768 && L_PART + 4096 <= 32768 && L_HB % 16 == 0 && L_QB % 16 == 0 && L_OP % 16 == 0 && L_KS % 16 == 0 && L_GAB % 16 == 0 && L_H2 % 16 == 0 && L_END <= 163840, "lds map");

#define D_RS1 8
#define D_AG 10
#define D_RS2 8
#define POLL_SLEEP 1
#define STAMPS 0
#define NODUTY 0
#define NOPARK 0
#define NOW 0
constexpr unsigned SPIN_CAP = NODUTY ? 4u : (1u << 21);

__device__ __forceinline__ float bflo(unsigned d) { return __uint_as_float(d << 16); }
__device__ __forceinline__ float bfhi(unsigned d) { return __uint_as_float(d & 0xffff0000u); }
__device__ __forceinline__ unsigned cvtpk(float lo, float hi) {
  bf2 r = {(__bf16)lo, (__bf16)hi}; return __builtin_bit_cast(unsigned, r);
}
__device__ __forceinline__ float rbf(float f) { return bflo(cvtpk(f, 0.f)); }
__device__ __forceinline__ void rbf2(float& a, float& b) { const unsigned p = cvtpk(a, b); a = bflo(p); b = bfhi(p); }
__device__ __forceinline__ float frcp(float x) { return __builtin_amdgcn_rcpf(x); }
__device__ __forceinline__ float fsilu(float a) { return a * frcp(1.f + __builtin_amdgcn_exp2f(a * -1.4426950408889634f)); }
template <int CTRL> __device__ __forceinline__ float dppf(float x) {
  return __builtin_bit_cast(float, __builtin_amdgcn_update_dpp(0, __builtin_bit_cast(int, x), CTRL, 0xf, 0xf, true));
}
template <int CTRL> __device__ __forceinline__ unsigned dppu(unsigned x) { return (unsigned)__builtin_amdgcn_update_dpp(0, (int)x, CTRL, 0xf, 0xf, true); }
constexpr int XOR1 = 0xB1, XOR2 = 0x4E, XOR7 = 0x141, XOR8 = 0x128, QB0 = 0x00, QB2 = 0xAA;
__device__ __forceinline__ float x16_sum(float x) { auto s = __builtin_amdgcn_permlane16_swap(__float_as_uint(x), __float_as_uint(x), false, false); return __uint_as_float(s[0]) + __uint_as_float(s[1]); }
__device__ __forceinline__ float x32_sum(float x) { auto s = __builtin_amdgcn_permlane32_swap(__float_as_uint(x), __float_as_uint(x), false, false); return __uint_as_float(s[0]) + __uint_as_float(s[1]); }
__device__ __forceinline__ float x16_max(float x) { auto s = __builtin_amdgcn_permlane16_swap(__float_as_uint(x), __float_as_uint(x), false, false); return fmaxf(__uint_as_float(s[0]), __uint_as_float(s[1])); }
__device__ __forceinline__ float x32_max(float x) { auto s = __builtin_amdgcn_permlane32_swap(__float_as_uint(x), __float_as_uint(x), false, false); return fmaxf(__uint_as_float(s[0]), __uint_as_float(s[1])); }
__device__ __forceinline__ float wave_sum(float x) {
  x += dppf<XOR1>(x); x += dppf<XOR2>(x); x += dppf<XOR7>(x); x += dppf<XOR8>(x); return x32_sum(x16_sum(x));
}
__device__ __forceinline__ float wave_max(float x) {
  x = fmaxf(x, dppf<XOR1>(x)); x = fmaxf(x, dppf<XOR2>(x)); x = fmaxf(x, dppf<XOR7>(x)); x = fmaxf(x, dppf<XOR8>(x)); return x32_max(x16_max(x));
}
__device__ __forceinline__ f4 mfma(v4u a, v4u b, f4 c) {
  return __builtin_amdgcn_mfma_f32_16x16x32_bf16(__builtin_bit_cast(bf8, a), __builtin_bit_cast(bf8, b), c, 0, 0, 0);
}
__device__ __forceinline__ float dot2(unsigned a, unsigned b, float c) {
  return __builtin_amdgcn_fdot2_f32_bf16(__builtin_bit_cast(bf2, a), __builtin_bit_cast(bf2, b), c, false);
}
__device__ __forceinline__ rsrc_t mkrs(const void* p, unsigned bytes) { return __builtin_amdgcn_make_buffer_rsrc((void*)p, 0, (int)bytes, 0x00020000); }
template <int AUX> __device__ __forceinline__ v4u ld(rsrc_t r, unsigned voff, unsigned soff) { return __builtin_amdgcn_raw_buffer_load_b128(r, (int)voff, (int)soff, AUX); }
__device__ __forceinline__ v4u ldsc(rsrc_t r, unsigned voff) { return __builtin_amdgcn_raw_buffer_load_b128(r, (int)voff, 0, AUX_SC1); }
__device__ __forceinline__ void stsc(rsrc_t r, unsigned voff, v4u v) { __builtin_amdgcn_raw_buffer_store_b128(v, r, (int)voff, 0, AUX_SC1); }
__device__ __forceinline__ void stsc2(rsrc_t r, unsigned voff, v2u v) { __builtin_amdgcn_raw_buffer_store_b64(v, r, (int)voff, 0, AUX_SC1); }
#define WAITVM(n) asm volatile("s_waitcnt vmcnt(%0)" ::"n"((n) > 63 ? 63 : (n)) : "memory")
#define WAITW(n, lo, hi) do { if ((lo) == (hi)) WAITVM((n) - (lo)); else if (w < 4) WAITVM((n) - (lo)); else WAITVM((n) - (hi)); } while (0)
#define BAR() asm volatile("s_waitcnt lgkmcnt(0)\n\ts_barrier" ::: "memory")
#define SB() __builtin_amdgcn_sched_barrier(0)
#define FENCE() asm volatile("" ::: "memory")
#define PIN(x) asm volatile("" : "+v"(x))

template <typename T> __device__ __forceinline__ T lds_ld(LAS unsigned char* L, int off) { return *(LAS T*)(L + off); }
template <typename T> __device__ __forceinline__ void lds_st(LAS unsigned char* L, int off, T v) { *(LAS T*)(L + off) = v; }
template <int I, int N, typename F> __device__ __forceinline__ void sfor(F&& f) {
  if constexpr (I < N) { f(std::integral_constant<int, I>{}); sfor<I + 1, N>(f); }
}
template <int I> using IC = std::integral_constant<int, I>;
#define AINL __attribute__((always_inline))

struct Sel { unsigned s[2][4]; };
__device__ __forceinline__ v4u diagA(unsigned X, const Sel& S, int par) {
  v4u a;
#pragma unroll
  for (int d = 0; d < 4; ++d) a[d] = __builtin_amdgcn_perm(X, X, S.s[par][d]);
  return a;
}
__device__ __forceinline__ v4u tag4(f4 v, unsigned T) {
  v4u r;
#pragma unroll
  for (int e = 0; e < 4; ++e) r[e] = ((__float_as_uint(v[e]) + 0x80u) & 0xffffff00u) | ((T >> (8 * e)) & 0xffu);
  return r;
}
__device__ __forceinline__ bool tagok4(v4u v, unsigned T) {
  const unsigned lo = __builtin_amdgcn_perm(v.y, v.x, 0x0c0c0400u), hi = __builtin_amdgcn_perm(v.w, v.z, 0x04000c0cu);
  return (lo | hi) == T;
}
__device__ __forceinline__ unsigned long long ballot(bool p) { return __builtin_amdgcn_ballot_w64(p); }

#define STAMPK 0
#define STAMP(n) do { } while (0)
#define KSTAMP(k) do { } while (0)

__global__ void __launch_bounds__(512, 2)
fused_kernel(const void* px, const void* pk, const void* pv, const void* pwq,
             const void* pwo, const void* pw1, const void* pw2, const void* pc,
             const void* pwm, void* pout, void* pws) {
  __shared__ __attribute__((aligned(16))) unsigned char smem[L_END];
  LAS unsigned char* L = (LAS unsigned char*)smem;
  const int t = threadIdx.x, lane = t & 63, w = __builtin_amdgcn_readfirstlane(t >> 6);
  const int wg = blockIdx.x, xg = wg & 7, e8 = wg >> 3, b = wg >> 5, h = xg * 4 + (e8 & 3);
  const int g = lane >> 4, c = lane & 15;
  const bool mlp = h < 27;
  STAMP(0);
  const rsrc_t rsWS = mkrs(pws, WS_BYTES);
  const rsrc_t rsK = mkrs((const char*)pk + ((size_t)(b * 32 + h) * 4096 + 3072) * 256, 262144);
  const rsrc_t rsV = mkrs((const char*)pv + ((size_t)(b * 32 + h) * 4096 + 3072) * 256, 262144);
  const rsrc_t rsWQ = mkrs(pwq, 640 * 4096 * 2), rsWO = mkrs(pwo, 4096 * 640 * 2), rsW1 = mkrs(pw1, 640 * 1728 * 2), rsW2 = mkrs(pw2, 1728 * 640 * 2);
  const rsrc_t rsWM = mkrs(pwm, 640 * 3840 * 2), rsC = mkrs(pc, 10240), rsX = mkrs(px, 10240), rsOUT = mkrs(pout, 10240);

  const bool role = wg < 240;
  const int ta = wg % 60, kq = wg / 60;
  unsigned Eraw = 0;
  if (w == 0) Eraw = __builtin_amdgcn_raw_buffer_load_b32(rsWS, WS_EPOCH, 0, AUX_SC1);
  v4u cv0 = ld<AUX_NT>(rsC, (role && t < 160) ? (t / 20) * 1280 + kq * 320 + (t % 20) * 16 : OOB, 0);
  const unsigned wmoff = (unsigned)(160 * kq + 8 * w + (lane >> 3)) * 7680u + ta * 128 + (lane & 7) * 16;
  v4u wm0 = ld<AUX_NT>(rsWM, role ? wmoff : OOB, 0);
  v4u wm1 = ld<AUX_NT>(rsWM, role ? wmoff : OOB, 64 * 7680);
  v4u wm2 = ld<AUX_NT>(rsWM, (role && w < 4) ? wmoff : OOB, 128 * 7680);
  if (t < 8) lds_st<unsigned>(L, L_FLAG + t * 4, 0u);
  SB();
  asm volatile("s_barrier" ::: "memory");
  unsigned po[4]; v4u pvv[4];
  v4u WQ[3], WO[20], KB[NR], KL[TW + 1];
  const unsigned offWQ = NOW ? OOB : (unsigned)(80 * b + 20 * g + w) * 8192u + h * 256 + c * 16;
  const unsigned offWO = NOW ? OOB : (unsigned)(h * 128 + 16 * w + 4 * g) * 1280u + c * 16;
  const unsigned offKV = w * 1024 + lane * 16;
  const int ksoff = L_KS + w * (NLDS * 1024) + lane * 16;
  auto ldwo = [&](auto ic) AINL { constexpr int i = decltype(ic)::value; WO[i] = ld<AUX_W>(rsWO, offWO, (i / 5) * 1280 + (i % 5) * 256); };
  auto a_issue = [&](auto mc) AINL {
    constexpr It it = a_item(decltype(mc)::value);
    if constexpr (it.kind == 4) WQ[it.idx] = ld<AUX_NT>(rsWQ, (it.idx < 2 || w < 4) ? offWQ : OOB, it.idx * 8 * 8192);
    else if constexpr (it.kind == 2) ldwo(IC<it.idx>{});
    else if constexpr (it.kind == 5 || it.kind == 6) pvv[it.idx] = ldsc(rsWS, po[it.idx]);
    else if constexpr (it.idx < NR) KB[it.idx] = ld<AUX_K>(rsK, offKV, it.idx * 8192);
    else KL[(it.idx - NR) % (TW + 1)] = ld<AUX_K>(rsK, offKV, it.idx * 8192);
  };
  constexpr int NINIT0 = 2, NINIT1 = 4, NINIT2 = 5, NINIT3 = 7, NINIT = 8;
  static_assert(NINIT >= TW && a_item(NINIT - 1).idx < NR, "no parking during P1");
  sfor<0, NINIT0>(a_issue);
  SB();
  STAMP(1);
  WAITVM(NINIT0);
  sfor<NINIT0, NINIT1>(a_issue);
  SB();
  if (w == 0 && lane == 0) lds_st<unsigned>(L, L_ML, Eraw + 1u);
  if (wg >= 240 && wg < 248 && w == 0) {
    const unsigned T = (unsigned)__builtin_amdgcn_readfirstlane((int)Eraw) + 1u;
    const v4u xa = ld<AUX_NT>(rsX, (wg - 240) * 1280 + lane * 16, 0);
    const unsigned xb = __builtin_amdgcn_raw_buffer_load_b32(rsX, (wg - 240) * 1280 + 1024 + lane * 4, 0, AUX_NT);
    float xv[10];
#pragma unroll
    for (int d = 0; d < 4; ++d) { xv[2 * d] = bflo(xa[d]); xv[2 * d + 1] = bfhi(xa[d]); }
    xv[8] = bflo(xb); xv[9] = bfhi(xb);
    const float sm = ((xv[0] + xv[1]) + (xv[2] + xv[3])) + ((xv[4] + xv[5]) + (xv[6] + xv[7])) + (xv[8] + xv[9]);
    const float mean = wave_sum(sm) * (1.f / 640.f);
    float q2 = 0.f;
#pragma unroll
    for (int d = 0; d < 10; ++d) { const float dd = xv[d] - mean; q2 = fmaf(dd, dd, q2); }
    const float var = rbf(wave_sum(q2) * (1.f / 640.f));
    const float mb = rbf(mean), den = rbf(__builtin_amdgcn_sqrtf(rbf(var + 1e-5f)));
    if (lane == 0) stsc(rsWS, WS_XST + (wg - 240) * 16, v4u{__float_as_uint(mb) | (T & 0xffu), __float_as_uint(den) | ((T >> 8) & 0xffu), (T >> 16) & 0xffu, T >> 24});
  }
  {
    auto silu8 = [&](v4u v) AINL {
      v4u r;
#pragma unroll
      for (int d = 0; d < 4; ++d) r[d] = cvtpk(fsilu(bflo(v[d])), fsilu(bfhi(v[d])));
      return r;
    };
    if (t < 160) lds_st<v4u>(L, L_SC + (t / 20) * SC_STRIDE + (t % 20) * 16, silu8(cv0));
    lds_st<v4u>(L, L_WL + w * 1024 + lane * 16, wm0);
    lds_st<v4u>(L, L_WL + (w + 8) * 1024 + lane * 16, wm1);
    if (w < 4) lds_st<v4u>(L, L_WL + (w + 16) * 1024 + lane * 16, wm2);
  }
  SB();
  sfor<NINIT1, NINIT2>(a_issue);
  v2u xh = __builtin_amdgcn_raw_buffer_load_b64(rsX, lane < 20 ? w * 1280 + 160 * b + lane * 8 : OOB, 0, AUX_NT);
  v2u xown = __builtin_amdgcn_raw_buffer_load_b64(rsX, (w == 4 && lane < 5) ? b * 1280 + 40 * h + lane * 8 : OOB, 0, AUX_NT);
  SB();
  BAR();
  const unsigned T = (unsigned)__builtin_amdgcn_readfirstlane((int)lds_ld<unsigned>(L, L_ML));
  STAMP(2);
  sfor<NINIT2, NINIT3>([&](auto mc) AINL { WAITVM(TW - 2 + 2); a_issue(mc); SB(); });
  {
    f4 acc = {0.f, 0.f, 0.f, 0.f};
    const int tl = w & 3, s0_ = (w >> 2) * 3;
    const int trbase = L_WL + (8 * g + ((lane & 15) >> 2)) * 128 + tl * 32 + (lane & 3) * 8;
#pragma unroll
    for (int n = 0; n < 3; ++n) {
      if (n < 2 || w < 4) {
        const int s = s0_ + n;
        const v4u a = lds_ld<v4u>(L, L_SC + (c & 7) * SC_STRIDE + s * 64 + g * 16);
        const s4 b0 = __builtin_amdgcn_ds_read_tr16_b64_v4i16((LAS s4*)(L + trbase + s * 4096));
        const s4 b1 = __builtin_amdgcn_ds_read_tr16_b64_v4i16((LAS s4*)(L + trbase + s * 4096 + 512));
        const v2u u0 = __builtin_bit_cast(v2u, b0), u1 = __builtin_bit_cast(v2u, b1);
        acc = mfma(a, v4u{u0.x, u0.y, u1.x, u1.y}, acc);
      }
    }
    if (g < 2) {
#pragma unroll
      for (int r = 0; r < 4; ++r) lds_st<float>(L, L_PART + (w >> 2) * 2048 + (4 * g + r) * 256 + (16 * tl + c) * 4, acc[r]);
    }
  }
  sfor<NINIT3, NINIT>([&](auto mc) AINL { WAITVM(TW - 2 + 2); a_issue(mc); SB(); });
  BAR();
  {
    const f4 s = lds_ld<f4>(L, L_PART + (t & 127) * 16) + lds_ld<f4>(L, L_PART + 2048 + (t & 127) * 16);
    unsigned o = OOB;
    if (t < 128) { if (role) o = WS_PM + (((t >> 4) * 4 + kq) * 3840 + ta * 64) * 4 + (t & 15) * 16; }
    else if (t == 128) o = WS_ALIVE + wg * 16;
    stsc(rsWS, o, tag4(s, T));
  }
  STAMP(3);
  Sel S;
  {
    const int e = c & 7;
#pragma unroll
    for (int par = 0; par < 2; ++par)
#pragma unroll
      for (int d = 0; d < 4; ++d) {
        const unsigned lo = (unsigned)(2 * par) | ((unsigned)(2 * par + 1) << 8);
        S.s[par][d] = (d == (e >> 1)) ? ((e & 1) ? (0x0c0cu | (lo << 16)) : (0x0c0c0000u | lo)) : 0x0c0c0c0cu;
      }
  }
  unsigned long long pm0, pm1, pm2, pm3;
  auto pmasks = [&]() AINL { pm0 = ballot(po[0] != OOB); pm1 = ballot(po[1] != OOB); pm2 = ballot(po[2] != OOB); pm3 = ballot(po[3] != OOB); };
#pragma unroll
  for (int q = 0; q < 4; ++q) po[q] = lane < 40 ? WS_PM + ((w * 4 + q) * 3840 + (lane / 20) * 640 + 80 * b + 4 * (lane % 20)) * 4 : OOB;
  if (lane == 40) po[0] = WS_XST + w * 16;
  pmasks();
  auto pissue = [&]() AINL {
#pragma unroll
    for (int q = 0; q < 4; ++q) pvv[q] = ldsc(rsWS, po[q]);
  };
  auto pok = [&]() AINL {
    return (ballot(tagok4(pvv[0], T)) & pm0) == pm0 && (ballot(tagok4(pvv[1], T)) & pm1) == pm1 && (ballot(tagok4(pvv[2], T)) & pm2) == pm2 && (ballot(tagok4(pvv[3], T)) & pm3) == pm3;
  };
  auto preload = [&]() AINL {
#pragma unroll
    for (int q = 0; q < 4; ++q) { const bool k1 = tagok4(pvv[q], T); const v4u nv = ldsc(rsWS, k1 ? OOB : po[q]); pvv[q] = k1 ? pvv[q] : nv; }
  };
  auto pblock = [&]() AINL {
    unsigned spins = 0;
    while (!pok() && ++spins < SPIN_CAP) { __builtin_amdgcn_s_sleep(2); FENCE(); preload(); }
  };
  auto psum = [&]() AINL {
    f4 mv;
#pragma unroll
    for (int e = 0; e < 4; ++e) mv[e] = (__uint_as_float(pvv[0][e] & 0xffffff00u) + __uint_as_float(pvv[1][e] & 0xffffff00u)) +
                                        (__uint_as_float(pvv[2][e] & 0xffffff00u) + __uint_as_float(pvv[3][e] & 0xffffff00u));
    float a0 = mv[0], a1 = mv[1], a2 = mv[2], a3 = mv[3]; rbf2(a0, a1); rbf2(a2, a3);
    return f4{a0, a1, a2, a3};
  };
  bool hdone = false;
  auto rprocess = [&]() AINL {
    const f4 mv = psum();
    const float xmean = __uint_as_float((unsigned)__builtin_amdgcn_readlane((int)pvv[0].x, 40) & 0xffff0000u);
    const float xrden = frcp(__uint_as_float((unsigned)__builtin_amdgcn_readlane((int)pvv[0].y, 40) & 0xffff0000u));
    float lnv[4] = {bflo(xh.x) - xmean, bfhi(xh.x) - xmean, bflo(xh.y) - xmean, bfhi(xh.y) - xmean};
    rbf2(lnv[0], lnv[1]); rbf2(lnv[2], lnv[3]);
#pragma unroll
    for (int e = 0; e < 4; ++e) lnv[e] *= xrden;
    rbf2(lnv[0], lnv[1]); rbf2(lnv[2], lnv[3]);
    float hv[4];
#pragma unroll
    for (int e = 0; e < 4; ++e) {
      const float ca = __uint_as_float(__builtin_amdgcn_ds_bpermute(((lane + 20) & 63) * 4, (int)__float_as_uint(mv[e])));
      hv[e] = rbf(lnv[e] * rbf(1.f + ca)) + mv[e];
    }
    if (lane < 20) lds_st<v2u>(L, L_HB + w * 160 + lane * 8, v2u{cvtpk(hv[0], hv[1]), cvtpk(hv[2], hv[3])});
    asm volatile("s_waitcnt lgkmcnt(0)" ::: "memory");
    if (lane == 0) __hip_atomic_fetch_add((LAS unsigned*)(L + L_FLAG), 1u, __ATOMIC_RELAXED, __HIP_MEMORY_SCOPE_WORKGROUP);
    STAMP(14);
  };
  auto mprocess = [&]() AINL {
    const f4 mv = psum();
    v4u msg;
#pragma unroll
    for (int e = 0; e < 4; ++e) msg[e] = (__float_as_uint(rbf(lane >= 10 ? 1.f + mv[e] : mv[e])) & 0xffff0000u) | ((T >> (8 * e)) & 0xffu);
    if (lane >= 5 && lane < 15) stsc(rsWS, WS_AGP + (b * 32 + h) * 256 + lane * 16, msg);
    else if (lane < 5) lds_st<f4>(L, L_GAB + lane * 16, mv);
    else if (lane < 20) lds_st<f4>(L, L_GAB + 80 + (lane - 15) * 16, mv);
    STAMP(17);
  };
  auto bfpack = [&](v4u v) AINL { return v2u{__builtin_amdgcn_perm(v.y, v.x, 0x07060302u), __builtin_amdgcn_perm(v.w, v.z, 0x07060302u)}; };
  auto hflag = [&]() AINL { FENCE(); return __builtin_amdgcn_readfirstlane((int)*(volatile LAS unsigned*)(L + L_FLAG)) == 8; };
  auto hstep = [&]() AINL {
    const int pi = w & 3, hf = w >> 2;
    unsigned X[5]; v4u Bp[10];
#pragma unroll
    for (int d = 0; d < 5; ++d) X[d] = lds_ld<unsigned>(L, L_HB + (2 * pi + (c >> 3)) * 160 + 40 * g + 20 * hf + d * 4);
#pragma unroll
    for (int i = 0; i < 10; ++i) Bp[i] = lds_ld<v4u>(L, L_WQL + (10 * hf + i) * 1024 + lane * 16);
    f4 a0 = {0.f, 0.f, 0.f, 0.f}, a1 = {0.f, 0.f, 0.f, 0.f};
#pragma unroll
    for (int i = 0; i < 10; i += 2) { a0 = mfma(diagA(X[i >> 1], S, 0), Bp[i], a0); a1 = mfma(diagA(X[i >> 1], S, 1), Bp[i + 1], a1); }
    a0 += a1;
    lds_st<f4>(L, L_QP8 + w * 1024 + lane * 16, a0);
    asm volatile("s_waitcnt lgkmcnt(0)" ::: "memory");
    unsigned old = 0;
    if (lane == 0) old = __hip_atomic_fetch_add((LAS unsigned*)(L + L_FLAG + 4 + pi * 4), 1u, __ATOMIC_RELAXED, __HIP_MEMORY_SCOPE_WORKGROUP);
    if (__builtin_amdgcn_readfirstlane((int)old) == 1) {
      FENCE();
      const f4 lo = lds_ld<f4>(L, L_QP8 + pi * 1024 + lane * 16), hi = lds_ld<f4>(L, L_QP8 + (pi + 4) * 1024 + lane * 16);
      stsc(rsWS, WS_QPM + (h * 8 + b) * 4096 + (2 * pi + (g >> 1)) * 512 + (2 * c + (g & 1)) * 16, tag4(lo + hi, T));
      STAMP(16);
    }
  };
  static_assert(a_kpos(RL) >= a_kpos(6) + 1 + TW, "wq parked before the first h arrival");
  static_assert(a_kpos(RL) >= a_kpos(RK + 3) + 1 + TW - 1 && RL <= HK && HK + (HN - 1) * HD < QK - 1 && RL < QK - 1 && QK + 3 < 31, "stage order");
  auto pnone = [&]() AINL {
#pragma unroll
    for (int q = 0; q < 4; ++q) po[q] = OOB;
  };
  sfor<NINIT, NA>([&](auto mc) AINL {
    constexpr int m = decltype(mc)::value;
    constexpr It it = a_item(m);
    constexpr int k = it.kind == 0 ? it.idx : -100;
    WAITW(TW - 1 + (m - (TW - DA_LO) < NINIT ? 1 : 0), DA_LO, DA_HI);
    {
      constexpr It ol = a_item(m - TW);
      if constexpr (ol.kind == 4) { if (ol.idx < 2 || w < 4) lds_st<v4u>(L, L_WQL + (w + 8 * ol.idx) * 1024 + lane * 16, WQ[ol.idx]); }
      if constexpr (!NOPARK && ol.kind == 0 && ol.idx >= NR) lds_st<v4u>(L, ksoff + (ol.idx - NR) * 1024, KL[(ol.idx - NR) % (TW + 1)]);
    }
    a_issue(mc);
    if constexpr (k == 9) { if (w == 4 && lane < 5) lds_st<v2u>(L, L_XR + lane * 8, xown); }
    KSTAMP(k);
    if constexpr (k == 8) STAMP(18);
    if constexpr (k == 16) STAMP(19);
    if constexpr (k == 24) STAMP(20);
    if constexpr (!NODUTY) {
    if constexpr (k == RL) { pblock(); rprocess(); }
    if constexpr (k >= HK && k < HK + (HN - 1) * HD && (k - HK) % HD == 0) { if (!hdone && hflag()) { hstep(); hdone = true; } }
    if constexpr (k == HK + (HN - 1) * HD) {
      if (!hdone) { unsigned spins = 0; while (!hflag() && ++spins < (SPIN_CAP << 3)) __builtin_amdgcn_s_sleep(1); hstep(); }
    }
    if constexpr (k == QK - 1) {
#pragma unroll
      for (int q = 0; q < 4; ++q) {
        unsigned o = OOB;
        if (w == 5) { if (lane < 20) o = WS_PM + ((b * 4 + q) * 3840 + (2 + lane / 5) * 640 + 20 * h + 4 * (lane % 5)) * 4; }
        else if (w == 6) o = WS_QPM + ((h * 8 + 2 * q + (lane >> 5)) * 8 + b) * 512 + (lane & 31) * 16;
        else if (w == 4 && wg == 0) o = WS_ALIVE + (64 * q + lane) * 16;
        po[q] = o;
      }
      pmasks();
    }
    }
    SB();
  });
  STAMP(4);
  v4u W1[10], W2[10], VR[VRN];
  const unsigned offW1 = (mlp && !NOW) ? (unsigned)(80 * w + 40 * (c >> 3) + 10 * g) * 3456u + (64 * h + 8 * (c & 7)) * 2 : OOB;
  const unsigned offW2 = (mlp && !NOW) ? (unsigned)(64 * h + 8 * w + 2 * g) * 1280u + c * 16 : OOB;
  auto b_issue = [&](auto mc) AINL {
    constexpr It it = b_item(decltype(mc)::value);
    if constexpr (it.kind == 1) VR[it.idx % VRN] = ld<AUX_V>(rsV, offKV, it.idx * 8192);
    else if constexpr (it.kind == 2) ldwo(IC<it.idx>{});
    else W1[it.idx] = ld<AUX_W>(rsW1, offW1, it.idx * 3456);
    SB();
  };
#define VTICK(k) do { WAITW(TW - 1, DB_LO, DB_HI); b_issue(IC<(k)>{}); } while (0)
  sfor<0, VPRE>([&](auto kc) AINL { WAITW(TW - 1, DB_LO, DB_HI); b_issue(kc); });
  if (!NODUTY && w == 5) { pblock(); mprocess(); }
  if (!NODUTY && (w == 6 || w == 4)) {
    pblock();
    if (w == 6) {
      f4 q4;
#pragma unroll
      for (int e = 0; e < 4; ++e) q4[e] = x32_sum((__uint_as_float(pvv[0][e] & 0xffffff00u) + __uint_as_float(pvv[1][e] & 0xffffff00u)) +
                                                  (__uint_as_float(pvv[2][e] & 0xffffff00u) + __uint_as_float(pvv[3][e] & 0xffffff00u)));
      if (lane < 32) lds_st<v2u>(L, L_QB + lane * 8, v2u{cvtpk(q4[0], q4[1]), cvtpk(q4[2], q4[3])});
    } else {
      if (wg == 0 && lane == 0) __builtin_amdgcn_raw_buffer_store_b32(T, rsWS, WS_EPOCH, 0, AUX_SC1);
    }
  }
  BAR();
  STAMP(5);
  const v4u qr = lds_ld<v4u>(L, L_QB + c * 16);
  STAMP(6);
  auto kdot = [&](v4u k) AINL { return dot2(k.w, qr[3], dot2(k.z, qr[2], dot2(k.y, qr[1], dot2(k.x, qr[0], 0.f)))); };
  auto treduce = [&](float (&xs)[16]) AINL {
    float x[8];
#pragma unroll
    for (int i = 0; i < 8; ++i) { const float lo = xs[i] + dppf<XOR8>(xs[i]), hi = xs[i + 8] + dppf<XOR8>(xs[i + 8]); x[i] = (c & 8) ? hi : lo; }
#pragma unroll
    for (int i = 0; i < 4; ++i) { const float lo = x[i] + dppf<XOR7>(x[i]), hi = x[i + 4] + dppf<XOR7>(x[i + 4]); x[i] = (c & 4) ? hi : lo; }
#pragma unroll
    for (int i = 0; i < 2; ++i) { const float lo = x[i] + dppf<XOR2>(x[i]), hi = x[i + 2] + dppf<XOR2>(x[i + 2]); x[i] = (c & 2) ? hi : lo; }
    const float lo = x[0] + dppf<XOR1>(x[0]), hi = x[1] + dppf<XOR1>(x[1]);
    return (c & 1) ? hi : lo;
  };
  constexpr float C2 = 0.08838834764831845f * 1.4426950408889634f;
  float mw, fw = 0.f;
  unsigned ppk;
  {
    float xs[16];
#pragma unroll
    for (int j = 0; j < 16; ++j) xs[j] = kdot(KB[j]);
    SB();
    VTICK(VPRE);
    const float s0 = treduce(xs);
    SB();
    VTICK(VPRE + 1);
#pragma unroll
    for (int j = 0; j <= LASTPARK - NR; ++j) xs[j] = kdot(lds_ld<v4u>(L, ksoff + j * 1024));
#pragma unroll
    for (int j = LASTPARK - NR + 1; j < 16; ++j) xs[j] = kdot(KL[j % (TW + 1)]);
    SB();
    sfor<VPRE + 2, (VPRE + 4 < VRN + WEARLY ? VPRE + 4 : VRN + WEARLY)>([&](auto kc) AINL { WAITW(TW - 1, DB_LO, DB_HI); b_issue(kc); });
    const float s1 = treduce(xs);
    SB();
    sfor<(VPRE + 4 < VRN + WEARLY ? VPRE + 4 : VRN + WEARLY), VRN + WEARLY>([&](auto kc) AINL { WAITW(TW - 1, DB_LO, DB_HI); b_issue(kc); });
    mw = wave_max(fmaxf(s0, s1));
    const float p0 = __builtin_amdgcn_exp2f((s0 - mw) * C2), p1 = __builtin_amdgcn_exp2f((s1 - mw) * C2);
    const float lw = wave_sum(p0 + p1);
    ppk = cvtpk(p0, p1);
    if (lane == 0) lds_st<v2u>(L, L_ML + w * 8, v2u{__float_as_uint(mw), __float_as_uint(lw)});
  }
  STAMP(15);
  f4 oacc = {0.f, 0.f, 0.f, 0.f};
  unsigned touch0 = 0, touch1 = 0;
  auto pvstep = [&](auto jc) AINL {
    constexpr int j = decltype(jc)::value;
    const unsigned X = dppu<0x150 + (j & 15)>(ppk);
    oacc = mfma(diagA(X, S, j >> 4), VR[j % VRN], oacc);
    if constexpr (j == 4) {
#pragma unroll
      for (int q = 0; q < 4; ++q) { const int idx = 64 * q + lane; po[q] = ((w == 1 || w == 2) && mlp && idx < 160) ? WS_AGP + (b * 32 + idx / 5) * 256 + (5 * w + idx % 5) * 16 : OOB; }
      pmasks(); pissue();
    }
    if constexpr (j == 14) {
      if (!NODUTY && (w == 1 || w == 2)) {
        pblock();
#pragma unroll
        for (int n = 0; n < 3; ++n) {
          const int idx = 64 * n + lane;
          if (po[n] != OOB) lds_st<v2u>(L, (w == 1 ? L_SB : L_CB) + (20 * (idx / 5) + 4 * (idx % 5)) * 2, bfpack(pvv[n]));
        }
      }
    }
    if constexpr (j == 8) {
      unsigned o0 = OOB, o1 = OOB;
      if (w == 4) {
        const int u = lane & 31; const int wgu = (b << 5) | ((u & 3) << 3) | (u >> 2);
        o0 = (lane < 32 ? WS_OUT1 : WS_OUT2) + wgu * 4096 + h * 128;
        o1 = lane < 32 ? WS_AG + (b * 32 + lane) * 128 : OOB;
      } else if (w == 0) { o0 = (lane < 32 ? WS_OUT1 : WS_OUT2) + wg * 4096 + (lane & 31) * 128; }
      touch0 = __builtin_amdgcn_raw_buffer_load_b32(rsWS, (int)o0, 0, AUX_SC1);
      touch1 = __builtin_amdgcn_raw_buffer_load_b32(rsWS, (int)o1, 0, AUX_SC1);
    }
    if constexpr (j == 16) { asm volatile("" :: "v"(touch0), "v"(touch1)); }
    if constexpr (j == 10) {
      BAR();
      float M = -INFINITY;
      v2u ml[8];
#pragma unroll
      for (int ww = 0; ww < 8; ++ww) { ml[ww] = lds_ld<v2u>(L, L_ML + ww * 8); M = fmaxf(M, __uint_as_float(ml[ww].x)); }
      float Ls = 0.f;
#pragma unroll
      for (int ww = 0; ww < 8; ++ww) Ls += __builtin_amdgcn_exp2f((__uint_as_float(ml[ww].x) - M) * C2) * __uint_as_float(ml[ww].y);
      fw = __builtin_amdgcn_exp2f((mw - M) * C2) * frcp(Ls);
      PIN(fw);
    }
  };
  sfor<VRN + WEARLY, NB>([&](auto mc) AINL {
    constexpr int m = decltype(mc)::value;
    constexpr It it = b_item(m);
    if constexpr (it.kind == 1) pvstep(IC<it.idx - VRN>{});
    if constexpr (m == NBV) sfor<32 - VRN, 22>([&](auto jc) AINL { pvstep(jc); SB(); });
    if constexpr (it.kind != 3) WAITW(TW - 1 + b_extra(m), DB_LO, DB_HI);
    else { pvstep(IC<22 + it.idx>{}); }
    b_issue(mc);
  });
  STAMP(7);
  {
    const f4 ov = oacc * fw;
    if (lane < 32) lds_st<f4>(L, L_OP + w * 512 + (8 * c + 4 * g) * 4, ov);
  }
  BAR();
  const int ypoff = L_YP + w * 2560 + (8 * c + 4 * (g & 1)) * 4;
  {
    const int kap = (c >> 1) & 3, sig = (c & 1) + 2 * (c >> 3);
    const int ko = (16 * w + 4 * g + kap) * 4;
    float o = lds_ld<float>(L, L_OP + (2 * sig) * 512 + ko) + lds_ld<float>(L, L_OP + (2 * sig + 1) * 512 + ko);
    o += dppf<XOR1>(o); o += dppf<XOR8>(o);
    const unsigned P = cvtpk(dppf<QB0>(o), dppf<QB2>(o)), Q = dppu<XOR7>(P);
    const unsigned X0 = (c & 4) ? Q : P, X1 = (c & 4) ? P : Q;
    f4 y[5];
    { const v4u A = diagA(X0, S, 0);
#pragma unroll
      for (int cb = 0; cb < 5; ++cb) y[cb] = mfma(A, WO[cb], f4{0.f, 0.f, 0.f, 0.f}); }
    { const v4u A = diagA(X0, S, 1);
#pragma unroll
      for (int cb = 0; cb < 5; ++cb) y[cb] = mfma(A, WO[5 + cb], y[cb]); }
    { const v4u A = diagA(X1, S, 0);
#pragma unroll
      for (int cb = 0; cb < 5; ++cb) y[cb] = mfma(A, WO[10 + cb], y[cb]); }
    { const v4u A = diagA(X1, S, 1);
#pragma unroll
      for (int cb = 0; cb < 5; ++cb) y[cb] = mfma(A, WO[15 + cb], y[cb]); }
    if (lane < 32) {
#pragma unroll
      for (int cb = 0; cb < 5; ++cb) lds_st<f4>(L, ypoff + cb * 512, y[cb]);
    }
  }
  BAR();
  STAMP(8);
  auto publish = [&](int wsoff) AINL {
    const int k = t & 7, j = t >> 3;
    const int o = L_YP + (20 * j + 4 * (k < 5 ? k : 0)) * 4;
    f4 s0 = lds_ld<f4>(L, o) + lds_ld<f4>(L, o + 2560), s1 = lds_ld<f4>(L, o + 2 * 2560) + lds_ld<f4>(L, o + 3 * 2560);
    f4 s2 = lds_ld<f4>(L, o + 4 * 2560) + lds_ld<f4>(L, o + 5 * 2560), s3 = lds_ld<f4>(L, o + 6 * 2560) + lds_ld<f4>(L, o + 7 * 2560);
    stsc(rsWS, wsoff + t * 16, tag4((s0 + s1) + (s2 + s3), T));
  };
  float x1v[4] = {0.f, 0.f, 0.f, 0.f};
  auto rs_gather = [&](int wsbase, int nprod, int delay_done) AINL -> f4 {
    const int pl = lane >> 3, k = lane & 7;
    unsigned off[4]; v4u v[4];
#pragma unroll
    for (int q = 0; q < 4; ++q) {
      const int u = 8 * q + pl;
      const int wgu = (b << 5) | ((u & 3) << 3) | (u >> 2);
      off[q] = (k < 5 && u < nprod) ? wsbase + wgu * 4096 + h * 128 + k * 16 : OOB;
      PIN(off[q]);
    }
    const unsigned long long m0 = ballot(off[0] != OOB), m3 = ballot(off[3] != OOB);
    __builtin_amdgcn_s_sleep(0);
    (void)delay_done;
#pragma unroll
    for (int q = 0; q < 4; ++q) v[q] = ldsc(rsWS, off[q]);
    unsigned spins = 0;
    for (;;) {
      const bool k0 = tagok4(v[0], T), k1 = tagok4(v[1], T), k2 = tagok4(v[2], T), k3 = tagok4(v[3], T);
      if ((ballot(k0) & ballot(k1) & ballot(k2) & m0) == m0 && (ballot(k3) & m3) == m3) break;
      if (++spins > SPIN_CAP) break;
      __builtin_amdgcn_s_sleep(POLL_SLEEP);
      FENCE();
      const v4u n0 = ldsc(rsWS, k0 ? OOB : off[0]), n1 = ldsc(rsWS, k1 ? OOB : off[1]), n2 = ldsc(rsWS, k2 ? OOB : off[2]), n3 = ldsc(rsWS, k3 ? OOB : off[3]);
      v[0] = k0 ? v[0] : n0; v[1] = k1 ? v[1] : n1; v[2] = k2 ? v[2] : n2; v[3] = k3 ? v[3] : n3;
    }
    f4 s;
#pragma unroll
    for (int e = 0; e < 4; ++e) {
      float a = (__uint_as_float(v[0][e] & 0xffffff00u) + __uint_as_float(v[1][e] & 0xffffff00u)) + (__uint_as_float(v[2][e] & 0xffffff00u) + __uint_as_float(v[3][e] & 0xffffff00u));
      a += dppf<XOR8>(a); s[e] = x32_sum(x16_sum(a));
    }
    return s;
  };
  if (w < 4) publish(WS_OUT1 + wg * 4096);
  if (w != 4) {
#pragma unroll
    for (int i = 0; i < 10; ++i) W2[i] = ld<AUX_W>(rsW2, offW2, (i / 5) * 1280 + (i % 5) * 256);
  }
  if (w == 4) {
    const v2u xr = lds_ld<v2u>(L, L_XR + (lane & 7) * 8); const f4 gg = lds_ld<f4>(L, L_GAB + (lane & 7) * 16);
    unsigned off[3]; v4u v[3]; float sbv[12], cbv[12]; int hoff[3];
#pragma unroll
    for (int q = 0; q < 3; ++q) {
      const int idx = 64 * q + lane, jj = idx / 5, k = idx % 5;
      off[q] = (idx < 160 && mlp) ? WS_AG + (b * 32 + jj) * 128 + k * 16 : OOB;
      const int col = idx < 160 ? 20 * jj + 4 * k : 0;
      const v2u s2 = lds_ld<v2u>(L, L_SB + col * 2), c2 = lds_ld<v2u>(L, L_CB + col * 2);
      sbv[4 * q] = bflo(s2.x); sbv[4 * q + 1] = bfhi(s2.x); sbv[4 * q + 2] = bflo(s2.y); sbv[4 * q + 3] = bfhi(s2.y);
      cbv[4 * q] = bflo(c2.x); cbv[4 * q + 1] = bfhi(c2.x); cbv[4 * q + 2] = bflo(c2.y); cbv[4 * q + 3] = bfhi(c2.y);
      hoff[q] = L_H2 + col * 2;
      PIN(off[q]); PIN(hoff[q]);
    }
#pragma unroll
    for (int i = 0; i < 12; ++i) { PIN(sbv[i]); PIN(cbv[i]); }
    float xx[4] = {bflo(xr.x), bfhi(xr.x), bflo(xr.y), bfhi(xr.y)};
    const unsigned long long am0 = ballot(off[0] != OOB), am2 = ballot(off[2] != OOB);
    __builtin_amdgcn_s_sleep(D_RS1);
    const f4 s = rs_gather(WS_OUT1, 32, 0);
    v4u m;
    {
      float a0 = s[0], a1 = s[1], a2 = s[2], a3 = s[3];
      rbf2(a0, a1); rbf2(a2, a3);
      a0 *= gg[0]; a1 *= gg[1]; a2 *= gg[2]; a3 *= gg[3];
      rbf2(a0, a1); rbf2(a2, a3);
      a0 += xx[0]; a1 += xx[1]; a2 += xx[2]; a3 += xx[3];
      const unsigned p01 = cvtpk(a0, a1), p23 = cvtpk(a2, a3);
      m = v4u{(p01 << 16) | (T & 0xffu), (p01 & 0xffff0000u) | ((T >> 8) & 0xffu), (p23 << 16) | ((T >> 16) & 0xffu), (p23 & 0xffff0000u) | (T >> 24)};
      x1v[0] = bflo(p01); x1v[1] = bfhi(p01); x1v[2] = bflo(p23); x1v[3] = bfhi(p23);
    }
    if (lane < 5) stsc(rsWS, WS_AG + (b * 32 + h) * 128 + lane * 16, m);
#pragma unroll
    for (int i = 0; i < 10; ++i) W2[i] = ld<AUX_W>(rsW2, offW2, (i / 5) * 1280 + (i % 5) * 256);
    STAMP(9);
    if (mlp) {
      __builtin_amdgcn_s_sleep(D_AG);
#pragma unroll
      for (int q = 0; q < 3; ++q) v[q] = ldsc(rsWS, off[q]);
      unsigned spins = 0;
      for (;;) {
        const bool k0 = tagok4(v[0], T), k1 = tagok4(v[1], T), k2 = tagok4(v[2], T);
        if ((ballot(k0) & ballot(k1) & am0) == am0 && (ballot(k2) & am2) == am2) break;
        if (++spins > SPIN_CAP) break;
        __builtin_amdgcn_s_sleep(POLL_SLEEP);
        FENCE();
        const v4u n0 = ldsc(rsWS, k0 ? OOB : off[0]), n1 = ldsc(rsWS, k1 ? OOB : off[1]), n2 = ldsc(rsWS, k2 ? OOB : off[2]);
        v[0] = k0 ? v[0] : n0; v[1] = k1 ? v[1] : n1; v[2] = k2 ? v[2] : n2;
      }
      STAMP(13);
      float xv[12]; float sm = 0.f;
#pragma unroll
      for (int q = 0; q < 3; ++q)
#pragma unroll
        for (int e = 0; e < 4; ++e) { xv[4 * q + e] = __uint_as_float(v[q][e] & 0xffff0000u); sm += xv[4 * q + e]; }
      const float mean = wave_sum(sm) * (1.f / 640.f);
      float q2 = 0.f, q3 = 0.f;
#pragma unroll
      for (int i = 0; i < 8; ++i) { const float dd = xv[i] - mean; q2 += dd * dd; }
#pragma unroll
      for (int i = 8; i < 12; ++i) { const float dd = xv[i] - mean; q3 += dd * dd; }
      q2 += lane < 32 ? q3 : 0.f;
      const float var = rbf(wave_sum(q2) * (1.f / 640.f));
      const float mb = rbf(mean), rden = frcp(rbf(__builtin_amdgcn_sqrtf(rbf(var + 1e-5f))));
#pragma unroll
      for (int q = 0; q < 3; ++q) {
        float a[4];
#pragma unroll
        for (int e = 0; e < 4; ++e) a[e] = xv[4 * q + e] - mb;
        rbf2(a[0], a[1]); rbf2(a[2], a[3]);
#pragma unroll
        for (int e = 0; e < 4; ++e) a[e] *= rden;
        rbf2(a[0], a[1]); rbf2(a[2], a[3]);
#pragma unroll
        for (int e = 0; e < 4; ++e) a[e] *= cbv[4 * q + e];
        rbf2(a[0], a[1]); rbf2(a[2], a[3]);
        const v2u o = {cvtpk(a[0] + sbv[4 * q], a[1] + sbv[4 * q + 1]), cvtpk(a[2] + sbv[4 * q + 2], a[3] + sbv[4 * q + 3])};
        if (q < 2 || lane < 32) lds_st<v2u>(L, hoff[q], o);
      }
    }
  }
  if (mlp) {
    BAR();
    STAMP(10);
    {
      unsigned X[5];
#pragma unroll
      for (int d = 0; d < 5; ++d) X[d] = lds_ld<unsigned>(L, L_H2 + 160 * w + 80 * (c >> 3) + 20 * g + d * 4);
      f4 a0 = {0.f, 0.f, 0.f, 0.f}, a1 = {0.f, 0.f, 0.f, 0.f};
#pragma unroll
      for (int p = 0; p < 10; p += 2) { a0 = mfma(diagA(X[p >> 1], S, 0), W1[p], a0); a1 = mfma(diagA(X[p >> 1], S, 1), W1[p + 1], a1); }
      a0 += a1;
      if ((g >> 1) == (c >> 3)) lds_st<f4>(L, L_HP + (2 * w + (c >> 3)) * 256 + (8 * (c & 7) + 4 * (g & 1)) * 4, a0);
    }
    BAR();
    {
      const int kap = (c >> 2) & 1, sig = (c & 3) + 4 * (c >> 3);
      const int n = (8 * w + 2 * g + kap) * 4;
      float hd = lds_ld<float>(L, L_HP + (2 * sig) * 256 + n) + lds_ld<float>(L, L_HP + (2 * sig + 1) * 256 + n);
      hd += dppf<XOR1>(hd); hd += dppf<XOR2>(hd); hd += dppf<XOR8>(hd);
      hd = fsilu(rbf(hd));
      const float ot = dppf<XOR7>(hd);
      const unsigned X = kap ? cvtpk(ot, hd) : cvtpk(hd, ot);
      const v4u A0 = diagA(X, S, 0), A1 = diagA(X, S, 1);
      f4 y[5];
#pragma unroll
      for (int cb = 0; cb < 5; ++cb) y[cb] = mfma(A0, W2[cb], f4{0.f, 0.f, 0.f, 0.f});
#pragma unroll
      for (int cb = 0; cb < 5; ++cb) y[cb] = mfma(A1, W2[5 + cb], y[cb]);
      if (lane < 32) {
#pragma unroll
        for (int cb = 0; cb < 5; ++cb) lds_st<f4>(L, ypoff + cb * 512, y[cb]);
      }
    }
    BAR();
    STAMP(11);
    if (w < 4) publish(WS_OUT2 + wg * 4096);
  }
  if (w == 4) {
    const f4 gg = lds_ld<f4>(L, L_GAB + 80 + (lane & 7) * 16);
    if (!mlp) __builtin_amdgcn_s_sleep(48);
    __builtin_amdgcn_s_sleep(D_RS2);
    const f4 s = rs_gather(WS_OUT2, 27, 0);
    float a0 = s[0], a1 = s[1], a2 = s[2], a3 = s[3];
    rbf2(a0, a1); rbf2(a2, a3);
    a0 *= gg[0]; a1 *= gg[1]; a2 *= gg[2]; a3 *= gg[3];
    rbf2(a0, a1); rbf2(a2, a3);
    if (lane < 5) __builtin_amdgcn_raw_buffer_store_b64(v2u{cvtpk(x1v[0] + a0, x1v[1] + a1), cvtpk(x1v[2] + a2, x1v[3] + a3)}, rsOUT, b * 1280 + 40 * h + lane * 8, 0, 0);
    STAMP(12);
  }
}

extern "C" void kernel_launch(void* const* d_in, const int* in_sizes, int n_in, void* d_out, int out_size, void* d_ws, size_t ws_size,
                              hipStream_t stream) {
  fused_kernel<<<256, 512, 0, stream>>>(d_in[0], d_in[1], d_in[2], d_in[3], d_in[4], d_in[6], d_in[7], d_in[8], d_in[9], d_out, d_ws);
}
```

```cpp
#include <hip/hip_runtime.h>
#include <hip/hip_bf16.h>
#include <stdint.h>
#include <type_traits>

typedef unsigned v4u __attribute__((ext_vector_type(4)));
typedef unsigned v2u __attribute__((ext_vector_type(2)));
typedef float f4 __attribute__((ext_vector_type(4)));
typedef __bf16 bf8 __attribute__((ext_vector_type(8)));
typedef __bf16 bf2 __attribute__((ext_vector_type(2)));
typedef short s4 __attribute__((ext_vector_type(4)));
#define LAS __attribute__((address_space(3)))
typedef __amdgpu_buffer_rsrc_t rsrc_t;

#define AUX_K 2
#define AUX_V 2
#define AUX_W 0
constexpr int AUX_NT = 2, AUX_SC1 = 16;
constexpr unsigned OOB = 0x80000000u;

constexpr int WS_EPOCH = 0;
constexpr int WS_ALIVE = 4096;
constexpr int WS_XST = 16384;
constexpr int WS_AGP = 65536;
constexpr int WS_AG = 131072;
constexpr int WS_PM = 262144;
constexpr int WS_OUT1 = 1 << 20;
constexpr int WS_OUT2 = 2 << 20;
constexpr int WS_QPM = 3 << 20;
constexpr int WS_STAMP = 4 << 20;
constexpr int WS_BYTES = 8 << 20;

#define TW 6
#define DA_LO 1
#define DA_HI 1
#define DB_LO 1
#define DB_HI 0
#define VRN 14
#define VPRE 9
constexpr int NR = 16;
#define RK 12
#define RL 19
#define HK 19
#define HD 2
#define HN 3
#define QK 25
struct It { int kind, idx; };
#define WOA 10
constexpr int NA = 3 + 32 + WOA + 8;
constexpr It a_item(int m) {
  int pos = 0, wn = 0;
  for (int k = 0; k < 32; ++k) {
    if (pos == m) return It{0, k};
    ++pos;
    if (k >= 4 && k < 7) { if (pos == m) return It{4, k - 4}; ++pos; }
    else if (k >= RK && k < RK + 4) { if (pos == m) return It{5, k - RK}; ++pos; }
    else if (k >= QK && k < QK + 4) { if (pos == m) return It{6, k - QK}; ++pos; }
    else if (k >= 7 && k < 31) {
      int idx = 0; for (int u = 7; u < k; ++u) if (!(u >= RK && u < RK + 4) && !(u >= QK && u < QK + 4)) ++idx;
      if (idx * WOA / 16 != (idx + 1) * WOA / 16 && wn < WOA) { if (pos == m) return It{2, wn}; ++pos; ++wn; }
    }
  }
  return It{-1, 0};
}
static_assert(a_item(NA - 1).kind == 0 && a_item(NA - 1).idx == 31, "phase A list");
constexpr int a_kpos(int k) { for (int m = 0; m < NA; ++m) if (a_item(m).kind == 0 && a_item(m).idx == k) return m; return NA; }
#define WEARLY 2
#define WLAST 27
constexpr int NBV = 32 + 20 - WOA, NB = NBV + 10;
constexpr It b_item(int m) {
  if (m < VRN) return It{1, m};
  if (m < VRN + WEARLY) return It{2, WOA + m - VRN};
  if (m >= NBV) return It{3, m - NBV};
  int pos = VRN + WEARLY, wn = WOA + WEARLY;
  for (int v = VRN; v < 32; ++v) {
    if (pos == m) return It{1, v};
    ++pos;
    int target = WOA + WEARLY + ((20 - WOA - WEARLY) * (v - VRN + 1) + (WLAST - VRN)) / (WLAST - VRN + 1);
    if (target > 20) target = 20;
    while (wn < target) { if (pos == m) return It{2, wn}; ++pos; ++wn; }
  }
  return It{-1, 0};
}
static_assert(b_item(NBV - 1).kind == 1 && b_item(NBV - 1).idx == 31, "phase B list");
constexpr int b_vpos(int k) { for (int m = 0; m < NB; ++m) if (b_item(m).kind == 1 && b_item(m).idx == k) return m; return NB; }
constexpr int b_extra(int m) { int n = 0; const int p4 = b_vpos(4 + VRN), p8 = b_vpos(8 + VRN); if (m >= p4 && m - p4 < TW) n += 4; if (m >= p8 && m - p8 < TW) n += 2; return n; }
constexpr int a_lastpark_f() { int k = NR - 1; for (int m = 0; m + TW < NA; ++m) if (a_item(m).kind == 0 && a_item(m).idx > k) k = a_item(m).idx; return k; }
constexpr int LASTPARK = a_lastpark_f();
constexpr int NLDS = LASTPARK - NR + 1;

constexpr int L_WL = 0;
constexpr int L_SC = 20480;
constexpr int SC_STRIDE = 336;
constexpr int L_PART = L_SC + 2688;
constexpr int L_WQL = 0;
constexpr int L_QP8 = 20480;
constexpr int L_HB = 32768;
constexpr int L_SB = L_HB + 1280;
constexpr int L_CB = L_SB + 1280;
constexpr int L_GAB = L_CB + 1280;
constexpr int L_XR = L_GAB + 160;
constexpr int L_ML = L_XR + 64;
constexpr int L_FLAG = L_ML + 64;
constexpr int L_QB = L_FLAG + 32;
constexpr int L_H2 = L_QB + 256;
constexpr int L_OP = L_H2 + 1280;
constexpr int L_HP = L_OP + 4096;
constexpr int L_KS = L_HP + 4096;
constexpr int L_YP = L_KS;
constexpr int L_END = L_KS + (8 * NLDS * 1024 > 20480 ? 8 * NLDS * 1024 : 20480);
static_assert(L_QP8 + 8192 <= 32768 && L_PART + 4096 <= 32768 && L_HB % 16 == 0 && L_QB % 16 == 0 && L_OP % 16 == 0 && L_KS % 16 == 0 && L_GAB % 16 == 0 && L_H2 % 16 == 0 && L_END <= 163840, "lds map");

#define D_RS1 8
#define D_AG 10
#define D_RS2 8
#define POLL_SLEEP 1
#define STAMPS 0
#define NODUTY 0
#define NOPARK 0
#define PGT 1
#define NOW 0
constexpr unsigned SPIN_CAP = NODUTY ? 4u : (1u << 21);

__device__ __forceinline__ float bflo(unsigned d) { return __uint_as_float(d << 16); }
__device__ __forceinline__ float bfhi(unsigned d) { return __uint_as_float(d & 0xffff0000u); }
__device__ __forceinline__ unsigned cvtpk(float lo, float hi) {
  bf2 r = {(__bf16)lo, (__bf16)hi}; return __builtin_bit_cast(unsigned, r);
}
__device__ __forceinline__ float rbf(float f) { return bflo(cvtpk(f, 0.f)); }
__device__ __forceinline__ void rbf2(float& a, float& b) { const unsigned p = cvtpk(a, b); a = bflo(p); b = bfhi(p); }
__device__ __forceinline__ float frcp(float x) { return __builtin_amdgcn_rcpf(x); }
__device__ __forceinline__ float fsilu(float a) { return a * frcp(1.f + __builtin_amdgcn_exp2f(a * -1.4426950408889634f)); }
template <int CTRL> __device__ __forceinline__ float dppf(float x) {
  return __builtin_bit_cast(float, __builtin_amdgcn_update_dpp(0, __builtin_bit_cast(int, x), CTRL, 0xf, 0xf, true));
}
template <int CTRL> __device__ __forceinline__ unsigned dppu(unsigned x) { return (unsigned)__builtin_amdgcn_update_dpp(0, (int)x, CTRL, 0xf, 0xf, true); }
constexpr int XOR1 = 0xB1, XOR2 = 0x4E, XOR7 = 0x141, XOR8 = 0x128, QB0 = 0x00, QB2 = 0xAA;
__device__ __forceinline__ float x16_sum(float x) { auto s = __builtin_amdgcn_permlane16_swap(__float_as_uint(x), __float_as_uint(x), false, false); return __uint_as_float(s[0]) + __uint_as_float(s[1]); }
__device__ __forceinline__ float x32_sum(float x) { auto s = __builtin_amdgcn_permlane32_swap(__float_as_uint(x), __float_as_uint(x), false, false); return __uint_as_float(s[0]) + __uint_as_float(s[1]); }
__device__ __forceinline__ float x16_max(float x) { auto s = __builtin_amdgcn_permlane16_swap(__float_as_uint(x), __float_as_uint(x), false, false); return fmaxf(__uint_as_float(s[0]), __uint_as_float(s[1])); }
__device__ __forceinline__ float x32_max(float x) { auto s = __builtin_amdgcn_permlane32_swap(__float_as_uint(x), __float_as_uint(x), false, false); return fmaxf(__uint_as_float(s[0]), __uint_as_float(s[1])); }
__device__ __forceinline__ float wave_sum(float x) {
  x += dppf<XOR1>(x); x += dppf<XOR2>(x); x += dppf<XOR7>(x); x += dppf<XOR8>(x); return x32_sum(x16_sum(x));
}
__device__ __forceinline__ float wave_max(float x) {
  x = fmaxf(x, dppf<XOR1>(x)); x = fmaxf(x, dppf<XOR2>(x)); x = fmaxf(x, dppf<XOR7>(x)); x = fmaxf(x, dppf<XOR8>(x)); return x32_max(x16_max(x));
}
__device__ __forceinline__ f4 mfma(v4u a, v4u b, f4 c) {
  return __builtin_amdgcn_mfma_f32_16x16x32_bf16(__builtin_bit_cast(bf8, a), __builtin_bit_cast(bf8, b), c, 0, 0, 0);
}
__device__ __forceinline__ float dot2(unsigned a, unsigned b, float c) {
  return __builtin_amdgcn_fdot2_f32_bf16(__builtin_bit_cast(bf2, a), __builtin_bit_cast(bf2, b), c, false);
}
__device__ __forceinline__ rsrc_t mkrs(const void* p, unsigned bytes) { return __builtin_amdgcn_make_buffer_rsrc((void*)p, 0, (int)bytes, 0x00020000); }
template <int AUX> __device__ __forceinline__ v4u ld(rsrc_t r, unsigned voff, unsigned soff) { return __builtin_amdgcn_raw_buffer_load_b128(r, (int)voff, (int)soff, AUX); }
__device__ __forceinline__ v4u ldsc(rsrc_t r, unsigned voff) { return __builtin_amdgcn_raw_buffer_load_b128(r, (int)voff, 0, AUX_SC1); }
__device__ __forceinline__ void stsc(rsrc_t r, unsigned voff, v4u v) { __builtin_amdgcn_raw_buffer_store_b128(v, r, (int)voff, 0, AUX_SC1); }
__device__ __forceinline__ void stsc2(rsrc_t r, unsigned voff, v2u v) { __builtin_amdgcn_raw_buffer_store_b64(v, r, (int)voff, 0, AUX_SC1); }
#define WAITVM(n) asm volatile("s_waitcnt vmcnt(%0)" ::"n"((n) > 63 ? 63 : (n)) : "memory")
#define WAITW(n, lo, hi) do { if ((lo) == (hi)) WAITVM((n) - (lo)); else if (w < 4) WAITVM((n) - (lo)); else WAITVM((n) - (hi)); } while (0)
#define BAR() asm volatile("s_waitcnt lgkmcnt(0)\n\ts_barrier" ::: "memory")
#define SB() __builtin_amdgcn_sched_barrier(0)
#define FENCE() asm volatile("" ::: "memory")
#define PIN(x) asm volatile("" : "+v"(x))

template <typename T> __device__ __forceinline__ T lds_ld(LAS unsigned char* L, int off) { return *(LAS T*)(L + off); }
template <typename T> __device__ __forceinline__ void lds_st(LAS unsigned char* L, int off, T v) { *(LAS T*)(L + off) = v; }
template <int I, int N, typename F> __device__ __forceinline__ void sfor(F&& f) {
  if constexpr (I < N) { f(std::integral_constant<int, I>{}); sfor<I + 1, N>(f); }
}
template <int I> using IC = std::integral_constant<int, I>;
#define AINL __attribute__((always_inline))

struct Sel { unsigned s[2][4]; };
__device__ __forceinline__ v4u diagA(unsigned X, const Sel& S, int par) {
  v4u a;
#pragma unroll
  for (int d = 0; d < 4; ++d) a[d] = __builtin_amdgcn_perm(X, X, S.s[par][d]);
  return a;
}
__device__ __forceinline__ v4u tag4(f4 v, unsigned T) {
  v4u r;
#pragma unroll
  for (int e = 0; e < 4; ++e) r[e] = ((__float_as_uint(v[e]) + 0x80u) & 0xffffff00u) | ((T >> (8 * e)) & 0xffu);
  return r;
}
__device__ __forceinline__ bool tagok4(v4u v, unsigned T) {
  const unsigned lo = __builtin_amdgcn_perm(v.y, v.x, 0x0c0c0400u), hi = __builtin_amdgcn_perm(v.w, v.z, 0x04000c0cu);
  return (lo | hi) == T;
}
__device__ __forceinline__ unsigned long long ballot(bool p) { return __builtin_amdgcn_ballot_w64(p); }

#define STAMPK 0
#define STAMP(n) do { } while (0)
#define KSTAMP(k) do { } while (0)

__global__ void __launch_bounds__(512, 2)
fused_kernel(const void* px, const void* pk, const void* pv, const void* pwq,
             const void* pwo, const void* pw1, const void* pw2, const void* pc,
             const void* pwm, void* pout, void* pws) {
  __shared__ __attribute__((aligned(16))) unsigned char smem[L_END];
  LAS unsigned char* L = (LAS unsigned char*)smem;
  const int t = threadIdx.x, lane = t & 63, w = __builtin_amdgcn_readfirstlane(t >> 6);
  const int wg = blockIdx.x, xg = wg & 7, e8 = wg >> 3, b = wg >> 5, h = xg * 4 + (e8 & 3);
  const int g = lane >> 4, c = lane & 15;
  const bool mlp = h < 27;
  STAMP(0);
  const rsrc_t rsWS = mkrs(pws, WS_BYTES);
  const rsrc_t rsK = mkrs((const char*)pk + ((size_t)(b * 32 + h) * 4096 + 3072) * 256, 262144);
  const rsrc_t rsV = mkrs((const char*)pv + ((size_t)(b * 32 + h) * 4096 + 3072) * 256, 262144);
  const rsrc_t rsWQ = mkrs(pwq, 640 * 4096 * 2), rsWO = mkrs(pwo, 4096 * 640 * 2), rsW1 = mkrs(pw1, 640 * 1728 * 2), rsW2 = mkrs(pw2, 1728 * 640 * 2);
  const rsrc_t rsWM = mkrs(pwm, 640 * 3840 * 2), rsC = mkrs(pc, 10240), rsX = mkrs(px, 10240), rsOUT = mkrs(pout, 10240);

  const bool role = wg < 240;
  const int ta = wg % 60, kq = wg / 60;
  unsigned Eraw = 0;
  if (w == 0) Eraw = __builtin_amdgcn_raw_buffer_load_b32(rsWS, WS_EPOCH, 0, AUX_SC1);
  v4u cv0 = ld<AUX_NT>(rsC, (role && t < 160) ? (t / 20) * 1280 + kq * 320 + (t % 20) * 16 : OOB, 0);
  const unsigned wmoff = (unsigned)(160 * kq + 8 * w + (lane >> 3)) * 7680u + ta * 128 + (lane & 7) * 16;
  v4u wm0 = ld<AUX_NT>(rsWM, role ? wmoff : OOB, 0);
  v4u wm1 = ld<AUX_NT>(rsWM, role ? wmoff : OOB, 64 * 7680);
  v4u wm2 = ld<AUX_NT>(rsWM, (role && w < 4) ? wmoff : OOB, 128 * 7680);
  unsigned pg0 = 0, pg1 = 0, pg2 = 0, pg3 = 0, pg4 = 0;
  if (PGT && w == 7) {
    pg0 = __builtin_amdgcn_raw_buffer_load_b32(rsV, lane < 2 ? lane * 262140 : OOB, 0, AUX_NT);
    pg1 = __builtin_amdgcn_raw_buffer_load_b32(rsWO, lane < 2 ? h * 163840 + lane * 163836 : OOB, 0, AUX_W);
    pg2 = __builtin_amdgcn_raw_buffer_load_b32(rsW1, lane < 8 ? lane * 80 * 3456 + h * 128 : OOB, 0, AUX_W);
    pg3 = __builtin_amdgcn_raw_buffer_load_b32(rsW2, lane < 2 ? h * 81920 + lane * 81916 : OOB, 0, AUX_W);
    pg4 = __builtin_amdgcn_raw_buffer_load_b32(rsWS, lane < 16 ? lane * 262144 + 1024 : OOB, 0, AUX_SC1);
  }
  if (t < 8) lds_st<unsigned>(L, L_FLAG + t * 4, 0u);
  SB();
  asm volatile("s_barrier" ::: "memory");
  unsigned po[4]; v4u pvv[4];
  v4u WQ[3], WO[20], KB[NR], KL[TW + 1];
  const unsigned offWQ = NOW ? OOB : (unsigned)(80 * b + 20 * g + w) * 8192u + h * 256 + c * 16;
  const unsigned offWO = NOW ? OOB : (unsigned)(h * 128 + 16 * w + 4 * g) * 1280u + c * 16;
  const unsigned offKV = w * 1024 + lane * 16;
  const int ksoff = L_KS + w * (NLDS * 1024) + lane * 16;
  auto ldwo = [&](auto ic) AINL { constexpr int i = decltype(ic)::value; WO[i] = ld<AUX_W>(rsWO, offWO, (i / 5) * 1280 + (i % 5) * 256); };
  auto a_issue = [&](auto mc) AINL {
    constexpr It it = a_item(decltype(mc)::value);
    if constexpr (it.kind == 4) WQ[it.idx] = ld<AUX_NT>(rsWQ, (it.idx < 2 || w < 4) ? offWQ : OOB, it.idx * 8 * 8192);
    else if constexpr (it.kind == 2) ldwo(IC<it.idx>{});
    else if constexpr (it.kind == 5 || it.kind == 6) pvv[it.idx] = ldsc(rsWS, po[it.idx]);
    else if constexpr (it.idx < NR) KB[it.idx] = ld<AUX_K>(rsK, offKV, it.idx * 8192);
    else KL[(it.idx - NR) % (TW + 1)] = ld<AUX_K>(rsK, offKV, it.idx * 8192);
  };
  constexpr int NINIT0 = 2, NINIT1 = 4, NINIT2 = 5, NINIT3 = 7, NINIT = 8;
  static_assert(NINIT >= TW && a_item(NINIT - 1).idx < NR, "no parking during P1");
  sfor<0, NINIT0>(a_issue);
  SB();
  STAMP(1);
  WAITVM(NINIT0);
  sfor<NINIT0, NINIT1>(a_issue);
  SB();
  if (w == 0 && lane == 0) lds_st<unsigned>(L, L_ML, Eraw + 1u);
  if (wg >= 240 && wg < 248 && w == 0) {
    const unsigned T = (unsigned)__builtin_amdgcn_readfirstlane((int)Eraw) + 1u;
    const v4u xa = ld<AUX_NT>(rsX, (wg - 240) * 1280 + lane * 16, 0);
    const unsigned xb = __builtin_amdgcn_raw_buffer_load_b32(rsX, (wg - 240) * 1280 + 1024 + lane * 4, 0, AUX_NT);
    float xv[10];
#pragma unroll
    for (int d = 0; d < 4; ++d) { xv[2 * d] = bflo(xa[d]); xv[2 * d + 1] = bfhi(xa[d]); }
    xv[8] = bflo(xb); xv[9] = bfhi(xb);
    const float sm = ((xv[0] + xv[1]) + (xv[2] + xv[3])) + ((xv[4] + xv[5]) + (xv[6] + xv[7])) + (xv[8] + xv[9]);
    const float mean = wave_sum(sm) * (1.f / 640.f);
    float q2 = 0.f;
#pragma unroll
    for (int d = 0; d < 10; ++d) { const float dd = xv[d] - mean; q2 = fmaf(dd, dd, q2); }
    const float var = rbf(wave_sum(q2) * (1.f / 640.f));
    const float mb = rbf(mean), den = rbf(__builtin_amdgcn_sqrtf(rbf(var + 1e-5f)));
    if (lane == 0) stsc(rsWS, WS_XST + (wg - 240) * 16, v4u{__float_as_uint(mb) | (T & 0xffu), __float_as_uint(den) | ((T >> 8) & 0xffu), (T >> 16) & 0xffu, T >> 24});
  }
  {
    auto silu8 = [&](v4u v) AINL {
      v4u r;
#pragma unroll
      for (int d = 0; d < 4; ++d) r[d] = cvtpk(fsilu(bflo(v[d])), fsilu(bfhi(v[d])));
      return r;
    };
    if (t < 160) lds_st<v4u>(L, L_SC + (t / 20) * SC_STRIDE + (t % 20) * 16, silu8(cv0));
    lds_st<v4u>(L, L_WL + w * 1024 + lane * 16, wm0);
    lds_st<v4u>(L, L_WL + (w + 8) * 1024 + lane * 16, wm1);
    if (w < 4) lds_st<v4u>(L, L_WL + (w + 16) * 1024 + lane * 16, wm2);
  }
  SB();
  sfor<NINIT1, NINIT2>(a_issue);
  v2u xh = __builtin_amdgcn_raw_buffer_load_b64(rsX, lane < 20 ? w * 1280 + 160 * b + lane * 8 : OOB, 0, AUX_NT);
  v2u xown = __builtin_amdgcn_raw_buffer_load_b64(rsX, (w == 4 && lane < 5) ? b * 1280 + 40 * h + lane * 8 : OOB, 0, AUX_NT);
  SB();
  BAR();
  const unsigned T = (unsigned)__builtin_amdgcn_readfirstlane((int)lds_ld<unsigned>(L, L_ML));
  asm volatile("" :: "v"(pg0), "v"(pg1), "v"(pg2), "v"(pg3), "v"(pg4));
  STAMP(2);
  sfor<NINIT2, NINIT3>([&](auto mc) AINL { WAITVM(TW - 2 + 2); a_issue(mc); SB(); });
  {
    f4 acc = {0.f, 0.f, 0.f, 0.f};
    const int tl = w & 3, s0_ = (w >> 2) * 3;
    const int trbase = L_WL + (8 * g + ((lane & 15) >> 2)) * 128 + tl * 32 + (lane & 3) * 8;
#pragma unroll
    for (int n = 0; n < 3; ++n) {
      if (n < 2 || w < 4) {
        const int s = s0_ + n;
        const v4u a = lds_ld<v4u>(L, L_SC + (c & 7) * SC_STRIDE + s * 64 + g * 16);
        const s4 b0 = __builtin_amdgcn_ds_read_tr16_b64_v4i16((LAS s4*)(L + trbase + s * 4096));
        const s4 b1 = __builtin_amdgcn_ds_read_tr16_b64_v4i16((LAS s4*)(L + trbase + s * 4096 + 512));
        const v2u u0 = __builtin_bit_cast(v2u, b0), u1 = __builtin_bit_cast(v2u, b1);
        acc = mfma(a, v4u{u0.x, u0.y, u1.x, u1.y}, acc);
      }
    }
    if (g < 2) {
#pragma unroll
      for (int r = 0; r < 4; ++r) lds_st<float>(L, L_PART + (w >> 2) * 2048 + (4 * g + r) * 256 + (16 * tl + c) * 4, acc[r]);
    }
  }
  sfor<NINIT3, NINIT>([&](auto mc) AINL { WAITVM(TW - 2 + 2); a_issue(mc); SB(); });
  BAR();
  {
    const f4 s = lds_ld<f4>(L, L_PART + (t & 127) * 16) + lds_ld<f4>(L, L_PART + 2048 + (t & 127) * 16);
    unsigned o = OOB;
    if (t < 128) { if (role) o = WS_PM + (((t >> 4) * 4 + kq) * 3840 + ta * 64) * 4 + (t & 15) * 16; }
    else if (t == 128) o = WS_ALIVE + wg * 16;
    stsc(rsWS, o, tag4(s, T));
  }
  STAMP(3);
  Sel S;
  {
    const int e = c & 7;
#pragma unroll
    for (int par = 0; par < 2; ++par)
#pragma unroll
      for (int d = 0; d < 4; ++d) {
        const unsigned lo = (unsigned)(2 * par) | ((unsigned)(2 * par + 1) << 8);
        S.s[par][d] = (d == (e >> 1)) ? ((e & 1) ? (0x0c0cu | (lo << 16)) : (0x0c0c0000u | lo)) : 0x0c0c0c0cu;
      }
  }
  unsigned long long pm0, pm1, pm2, pm3;
  auto pmasks = [&]() AINL { pm0 = ballot(po[0] != OOB); pm1 = ballot(po[1] != OOB); pm2 = ballot(po[2] != OOB); pm3 = ballot(po[3] != OOB); };
#pragma unroll
  for (int q = 0; q < 4; ++q) po[q] = lane < 40 ? WS_PM + ((w * 4 + q) * 3840 + (lane / 20) * 640 + 80 * b + 4 * (lane % 20)) * 4 : OOB;
  if (lane == 40) po[0] = WS_XST + w * 16;
  pmasks();
  auto pissue = [&]() AINL {
#pragma unroll
    for (int q = 0; q < 4; ++q) pvv[q] = ldsc(rsWS, po[q]);
  };
  auto pok = [&]() AINL {
    return (ballot(tagok4(pvv[0], T)) & pm0) == pm0 && (ballot(tagok4(pvv[1], T)) & pm1) == pm1 && (ballot(tagok4(pvv[2], T)) & pm2) == pm2 && (ballot(tagok4(pvv[3], T)) & pm3) == pm3;
  };
  auto preload = [&]() AINL {
#pragma unroll
    for (int q = 0; q < 4; ++q) { const bool k1 = tagok4(pvv[q], T); const v4u nv = ldsc(rsWS, k1 ? OOB : po[q]); pvv[q] = k1 ? pvv[q] : nv; }
  };
  auto pblock = [&]() AINL {
    unsigned spins = 0;
    while (!pok() && ++spins < SPIN_CAP) { __builtin_amdgcn_s_sleep(2); FENCE(); preload(); }
  };
  auto psum = [&]() AINL {
    f4 mv;
#pragma unroll
    for (int e = 0; e < 4; ++e) mv[e] = (__uint_as_float(pvv[0][e] & 0xffffff00u) + __uint_as_float(pvv[1][e] & 0xffffff00u)) +
                                        (__uint_as_float(pvv[2][e] & 0xffffff00u) + __uint_as_float(pvv[3][e] & 0xffffff00u));
    float a0 = mv[0], a1 = mv[1], a2 = mv[2], a3 = mv[3]; rbf2(a0, a1); rbf2(a2, a3);
    return f4{a0, a1, a2, a3};
  };
  bool hdone = false;
  auto rprocess = [&]() AINL {
    const f4 mv = psum();
    const float xmean = __uint_as_float((unsigned)__builtin_amdgcn_readlane((int)pvv[0].x, 40) & 0xffff0000u);
    const float xrden = frcp(__uint_as_float((unsigned)__builtin_amdgcn_readlane((int)pvv[0].y, 40) & 0xffff0000u));
    float lnv[4] = {bflo(xh.x) - xmean, bfhi(xh.x) - xmean, bflo(xh.y) - xmean, bfhi(xh.y) - xmean};
    rbf2(lnv[0], lnv[1]); rbf2(lnv[2], lnv[3]);
#pragma unroll
    for (int e = 0; e < 4; ++e) lnv[e] *= xrden;
    rbf2(lnv[0], lnv[1]); rbf2(lnv[2], lnv[3]);
    float hv[4];
#pragma unroll
    for (int e = 0; e < 4; ++e) {
      const float ca = __uint_as_float(__builtin_amdgcn_ds_bpermute(((lane + 20) & 63) * 4, (int)__float_as_uint(mv[e])));
      hv[e] = rbf(lnv[e] * rbf(1.f + ca)) + mv[e];
    }
    if (lane < 20) lds_st<v2u>(L, L_HB + w * 160 + lane * 8, v2u{cvtpk(hv[0], hv[1]), cvtpk(hv[2], hv[3])});
    asm volatile("s_waitcnt lgkmcnt(0)" ::: "memory");
    if (lane == 0) __hip_atomic_fetch_add((LAS unsigned*)(L + L_FLAG), 1u, __ATOMIC_RELAXED, __HIP_MEMORY_SCOPE_WORKGROUP);
    STAMP(14);
  };
  auto mprocess = [&]() AINL {
    const f4 mv = psum();
    v4u msg;
#pragma unroll
    for (int e = 0; e < 4; ++e) msg[e] = (__float_as_uint(rbf(lane >= 10 ? 1.f + mv[e] : mv[e])) & 0xffff0000u) | ((T >> (8 * e)) & 0xffu);
    if (lane >= 5 && lane < 15) stsc(rsWS, WS_AGP + (b * 32 + h) * 256 + lane * 16, msg);
    else if (lane < 5) lds_st<f4>(L, L_GAB + lane * 16, mv);
    else if (lane < 20) lds_st<f4>(L, L_GAB + 80 + (lane - 15) * 16, mv);
    STAMP(17);
  };
  auto bfpack = [&](v4u v) AINL { return v2u{__builtin_amdgcn_perm(v.y, v.x, 0x07060302u), __builtin_amdgcn_perm(v.w, v.z, 0x07060302u)}; };
  auto hflag = [&]() AINL { FENCE(); return __builtin_amdgcn_readfirstlane((int)*(volatile LAS unsigned*)(L + L_FLAG)) == 8; };
  auto hstep = [&]() AINL {
    const int pi = w & 3, hf = w >> 2;
    unsigned X[5]; v4u Bp[10];
#pragma unroll
    for (int d = 0; d < 5; ++d) X[d] = lds_ld<unsigned>(L, L_HB + (2 * pi + (c >> 3)) * 160 + 40 * g + 20 * hf + d * 4);
#pragma unroll
    for (int i = 0; i < 10; ++i) Bp[i] = lds_ld<v4u>(L, L_WQL + (10 * hf + i) * 1024 + lane * 16);
    f4 a0 = {0.f, 0.f, 0.f, 0.f}, a1 = {0.f, 0.f, 0.f, 0.f};
#pragma unroll
    for (int i = 0; i < 10; i += 2) { a0 = mfma(diagA(X[i >> 1], S, 0), Bp[i], a0); a1 = mfma(diagA(X[i >> 1], S, 1), Bp[i + 1], a1); }
    a0 += a1;
    lds_st<f4>(L, L_QP8 + w * 1024 + lane * 16, a0);
    asm volatile("s_waitcnt lgkmcnt(0)" ::: "memory");
    unsigned old = 0;
    if (lane == 0) old = __hip_atomic_fetch_add((LAS unsigned*)(L + L_FLAG + 4 + pi * 4), 1u, __ATOMIC_RELAXED, __HIP_MEMORY_SCOPE_WORKGROUP);
    if (__builtin_amdgcn_readfirstlane((int)old) == 1) {
      FENCE();
      const f4 lo = lds_ld<f4>(L, L_QP8 + pi * 1024 + lane * 16), hi = lds_ld<f4>(L, L_QP8 + (pi + 4) * 1024 + lane * 16);
      stsc(rsWS, WS_QPM + (h * 8 + b) * 4096 + (2 * pi + (g >> 1)) * 512 + (2 * c + (g & 1)) * 16, tag4(lo + hi, T));
      STAMP(16);
    }
  };
  static_assert(a_kpos(RL) >= a_kpos(6) + 1 + TW, "wq parked before the first h arrival");
  static_assert(a_kpos(RL) >= a_kpos(RK + 3) + 1 + TW - 1 && RL <= HK && HK + (HN - 1) * HD < QK - 1 && RL < QK - 1 && QK + 3 < 31, "stage order");
  auto pnone = [&]() AINL {
#pragma unroll
    for (int q = 0; q < 4; ++q) po[q] = OOB;
  };
  sfor<NINIT, NA>([&](auto mc) AINL {
    constexpr int m = decltype(mc)::value;
    constexpr It it = a_item(m);
    constexpr int k = it.kind == 0 ? it.idx : -100;
    WAITW(TW - 1 + (m - (TW - DA_LO) < NINIT ? 1 : 0), DA_LO, DA_HI);
    {
      constexpr It ol = a_item(m - TW);
      if constexpr (ol.kind == 4) { if (ol.idx < 2 || w < 4) lds_st<v4u>(L, L_WQL + (w + 8 * ol.idx) * 1024 + lane * 16, WQ[ol.idx]); }
      if constexpr (!NOPARK && ol.kind == 0 && ol.idx >= NR) lds_st<v4u>(L, ksoff + (ol.idx - NR) * 1024, KL[(ol.idx - NR) % (TW + 1)]);
    }
    a_issue(mc);
    if constexpr (k == 9) { if (w == 4 && lane < 5) lds_st<v2u>(L, L_XR + lane * 8, xown); }
    KSTAMP(k);
    if constexpr (k == 8) STAMP(18);
    if constexpr (k == 16) STAMP(19);
    if constexpr (k == 24) STAMP(20);
    if constexpr (!NODUTY) {
    if constexpr (k == RL) { pblock(); rprocess(); }
    if constexpr (k >= HK && k < HK + (HN - 1) * HD && (k - HK) % HD == 0) { if (!hdone && hflag()) { hstep(); hdone = true; } }
    if constexpr (k == HK + (HN - 1) * HD) {
      if (!hdone) { unsigned spins = 0; while (!hflag() && ++spins < (SPIN_CAP << 3)) __builtin_amdgcn_s_sleep(1); hstep(); }
    }
    if constexpr (k == QK - 1) {
#pragma unroll
      for (int q = 0; q < 4; ++q) {
        unsigned o = OOB;
        if (w == 5) { if (lane < 20) o = WS_PM + ((b * 4 + q) * 3840 + (2 + lane / 5) * 640 + 20 * h + 4 * (lane % 5)) * 4; }
        else if (w == 6) o = WS_QPM + ((h * 8 + 2 * q + (lane >> 5)) * 8 + b) * 512 + (lane & 31) * 16;
        else if (w == 4 && wg == 0) o = WS_ALIVE + (64 * q + lane) * 16;
        po[q] = o;
      }
      pmasks();
    }
    }
    SB();
  });
  STAMP(4);
  v4u W1[10], W2[10], VR[VRN];
  const unsigned offW1 = (mlp && !NOW) ? (unsigned)(80 * w + 40 * (c >> 3) + 10 * g) * 3456u + (64 * h + 8 * (c & 7)) * 2 : OOB;
  const unsigned offW2 = (mlp && !NOW) ? (unsigned)(64 * h + 8 * w + 2 * g) * 1280u + c * 16 : OOB;
  auto b_issue = [&](auto mc) AINL {
    constexpr It it = b_item(decltype(mc)::value);
    if constexpr (it.kind == 1) VR[it.idx % VRN] = ld<AUX_V>(rsV, offKV, it.idx * 8192);
    else if constexpr (it.kind == 2) ldwo(IC<it.idx>{});
    else W1[it.idx] = ld<AUX_W>(rsW1, offW1, it.idx * 3456);
    SB();
  };
#define VTICK(k) do { WAITW(TW - 1, DB_LO, DB_HI); b_issue(IC<(k)>{}); } while (0)
  sfor<0, VPRE>([&](auto kc) AINL { WAITW(TW - 1, DB_LO, DB_HI); b_issue(kc); });
  if (!NODUTY && w == 5) { pblock(); mprocess(); }
  if (!NODUTY && (w == 6 || w == 4)) {
    pblock();
    if (w == 6) {
      f4 q4;
#pragma unroll
      for (int e = 0; e < 4; ++e) q4[e] = x32_sum((__uint_as_float(pvv[0][e] & 0xffffff00u) + __uint_as_float(pvv[1][e] & 0xffffff00u)) +
                                                  (__uint_as_float(pvv[2][e] & 0xffffff00u) + __uint_as_float(pvv[3][e] & 0xffffff00u)));
      if (lane < 32) lds_st<v2u>(L, L_QB + lane * 8, v2u{cvtpk(q4[0], q4[1]), cvtpk(q4[2], q4[3])});
    } else {
      if (wg == 0 && lane == 0) __builtin_amdgcn_raw_buffer_store_b32(T, rsWS, WS_EPOCH, 0, AUX_SC1);
    }
  }
  BAR();
  STAMP(5);
  const v4u qr = lds_ld<v4u>(L, L_QB + c * 16);
  STAMP(6);
  auto kdot = [&](v4u k) AINL { return dot2(k.w, qr[3], dot2(k.z, qr[2], dot2(k.y, qr[1], dot2(k.x, qr[0], 0.f)))); };
  auto treduce = [&](float (&xs)[16]) AINL {
    float x[8];
#pragma unroll
    for (int i = 0; i < 8; ++i) { const float lo = xs[i] + dppf<XOR8>(xs[i]), hi = xs[i + 8] + dppf<XOR8>(xs[i + 8]); x[i] = (c & 8) ? hi : lo; }
#pragma unroll
    for (int i = 0; i < 4; ++i) { const float lo = x[i] + dppf<XOR7>(x[i]), hi = x[i + 4] + dppf<XOR7>(x[i + 4]); x[i] = (c & 4) ? hi : lo; }
#pragma unroll
    for (int i = 0; i < 2; ++i) { const float lo = x[i] + dppf<XOR2>(x[i]), hi = x[i + 2] + dppf<XOR2>(x[i + 2]); x[i] = (c & 2) ? hi : lo; }
    const float lo = x[0] + dppf<XOR1>(x[0]), hi = x[1] + dppf<XOR1>(x[1]);
    return (c & 1) ? hi : lo;
  };
  constexpr float C2 = 0.08838834764831845f * 1.4426950408889634f;
  float mw, fw = 0.f;
  unsigned ppk;
  {
    float xs[16];
#pragma unroll
    for (int j = 0; j < 16; ++j) xs[j] = kdot(KB[j]);
    SB();
    VTICK(VPRE);
    const float s0 = treduce(xs);
    SB();
    VTICK(VPRE + 1);
#pragma unroll
    for (int j = 0; j <= LASTPARK - NR; ++j) xs[j] = kdot(lds_ld<v4u>(L, ksoff + j * 1024));
#pragma unroll
    for (int j = LASTPARK - NR + 1; j < 16; ++j) xs[j] = kdot(KL[j % (TW + 1)]);
    SB();
    sfor<VPRE + 2, (VPRE + 4 < VRN + WEARLY ? VPRE + 4 : VRN + WEARLY)>([&](auto kc) AINL { WAITW(TW - 1, DB_LO, DB_HI); b_issue(kc); });
    const float s1 = treduce(xs);
    SB();
    sfor<(VPRE + 4 < VRN + WEARLY ? VPRE + 4 : VRN + WEARLY), VRN + WEARLY>([&](auto kc) AINL { WAITW(TW - 1, DB_LO, DB_HI); b_issue(kc); });
    mw = wave_max(fmaxf(s0, s1));
    const float p0 = __builtin_amdgcn_exp2f((s0 - mw) * C2), p1 = __builtin_amdgcn_exp2f((s1 - mw) * C2);
    const float lw = wave_sum(p0 + p1);
    ppk = cvtpk(p0, p1);
    if (lane == 0) lds_st<v2u>(L, L_ML + w * 8, v2u{__float_as_uint(mw), __float_as_uint(lw)});
  }
  STAMP(15);
  f4 oacc = {0.f, 0.f, 0.f, 0.f};
  unsigned touch0 = 0, touch1 = 0;
  auto pvstep = [&](auto jc) AINL {
    constexpr int j = decltype(jc)::value;
    const unsigned X = dppu<0x150 + (j & 15)>(ppk);
    oacc = mfma(diagA(X, S, j >> 4), VR[j % VRN], oacc);
    if constexpr (j == 4) {
#pragma unroll
      for (int q = 0; q < 4; ++q) { const int idx = 64 * q + lane; po[q] = ((w == 1 || w == 2) && mlp && idx < 160) ? WS_AGP + (b * 32 + idx / 5) * 256 + (5 * w + idx % 5) * 16 : OOB; }
      pmasks(); pissue();
    }
    if constexpr (j == 14) {
      if (!NODUTY && (w == 1 || w == 2)) {
        pblock();
#pragma unroll
        for (int n = 0; n < 3; ++n) {
          const int idx = 64 * n + lane;
          if (po[n] != OOB) lds_st<v2u>(L, (w == 1 ? L_SB : L_CB) + (20 * (idx / 5) + 4 * (idx % 5)) * 2, bfpack(pvv[n]));
        }
      }
    }
    if constexpr (j == 8) {
      unsigned o0 = OOB, o1 = OOB;
      if (w == 4) {
        const int u = lane & 31; const int wgu = (b << 5) | ((u & 3) << 3) | (u >> 2);
        o0 = (lane < 32 ? WS_OUT1 : WS_OUT2) + wgu * 4096 + h * 128;
        o1 = lane < 32 ? WS_AG + (b * 32 + lane) * 128 : OOB;
      } else if (w == 0) { o0 = (lane < 32 ? WS_OUT1 : WS_OUT2) + wg * 4096 + (lane & 31) * 128; }
      touch0 = __builtin_amdgcn_raw_buffer_load_b32(rsWS, (int)o0, 0, AUX_SC1);
      touch1 = __builtin_amdgcn_raw_buffer_load_b32(rsWS, (int)o1, 0, AUX_SC1);
    }
    if constexpr (j == 16) { asm volatile("" :: "v"(touch0), "v"(touch1)); }
    if constexpr (j == 10) {
      BAR();
      float M = -INFINITY;
      v2u ml[8];
#pragma unroll
      for (int ww = 0; ww < 8; ++ww) { ml[ww] = lds_ld<v2u>(L, L_ML + ww * 8); M = fmaxf(M, __uint_as_float(ml[ww].x)); }
      float Ls = 0.f;
#pragma unroll
      for (int ww = 0; ww < 8; ++ww) Ls += __builtin_amdgcn_exp2f((__uint_as_float(ml[ww].x) - M) * C2) * __uint_as_float(ml[ww].y);
      fw = __builtin_amdgcn_exp2f((mw - M) * C2) * frcp(Ls);
      PIN(fw);
    }
  };
  sfor<VRN + WEARLY, NB>([&](auto mc) AINL {
    constexpr int m = decltype(mc)::value;
    constexpr It it = b_item(m);
    if constexpr (it.kind == 1) pvstep(IC<it.idx - VRN>{});
    if constexpr (m == NBV) sfor<32 - VRN, 22>([&](auto jc) AINL { pvstep(jc); SB(); });
    if constexpr (it.kind != 3) WAITW(TW - 1 + b_extra(m), DB_LO, DB_HI);
    else { pvstep(IC<22 + it.idx>{}); }
    b_issue(mc);
  });
  STAMP(7);
  {
    const f4 ov = oacc * fw;
    if (lane < 32) lds_st<f4>(L, L_OP + w * 512 + (8 * c + 4 * g) * 4, ov);
  }
  BAR();
  const int ypoff = L_YP + w * 2560 + (8 * c + 4 * (g & 1)) * 4;
  {
    const int kap = (c >> 1) & 3, sig = (c & 1) + 2 * (c >> 3);
    const int ko = (16 * w + 4 * g + kap) * 4;
    float o = lds_ld<float>(L, L_OP + (2 * sig) * 512 + ko) + lds_ld<float>(L, L_OP + (2 * sig + 1) * 512 + ko);
    o += dppf<XOR1>(o); o += dppf<XOR8>(o);
    const unsigned P = cvtpk(dppf<QB0>(o), dppf<QB2>(o)), Q = dppu<XOR7>(P);
    const unsigned X0 = (c & 4) ? Q : P, X1 = (c & 4) ? P : Q;
    f4 y[5];
    { const v4u A = diagA(X0, S, 0);
#pragma unroll
      for (int cb = 0; cb < 5; ++cb) y[cb] = mfma(A, WO[cb], f4{0.f, 0.f, 0.f, 0.f}); }
    { const v4u A = diagA(X0, S, 1);
#pragma unroll
      for (int cb = 0; cb < 5; ++cb) y[cb] = mfma(A, WO[5 + cb], y[cb]); }
    { const v4u A = diagA(X1, S, 0);
#pragma unroll
      for (int cb = 0; cb < 5; ++cb) y[cb] = mfma(A, WO[10 + cb], y[cb]); }
    { const v4u A = diagA(X1, S, 1);
#pragma unroll
      for (int cb = 0; cb < 5; ++cb) y[cb] = mfma(A, WO[15 + cb], y[cb]); }
    if (lane < 32) {
#pragma unroll
      for (int cb = 0; cb < 5; ++cb) lds_st<f4>(L, ypoff + cb * 512, y[cb]);
    }
  }
  BAR();
  STAMP(8);
  auto publish = [&](int wsoff) AINL {
    const int k = t & 7, j = t >> 3;
    const int o = L_YP + (20 * j + 4 * (k < 5 ? k : 0)) * 4;
    f4 s0 = lds_ld<f4>(L, o) + lds_ld<f4>(L, o + 2560), s1 = lds_ld<f4>(L, o + 2 * 2560) + lds_ld<f4>(L, o + 3 * 2560);
    f4 s2 = lds_ld<f4>(L, o + 4 * 2560) + lds_ld<f4>(L, o + 5 * 2560), s3 = lds_ld<f4>(L, o + 6 * 2560) + lds_ld<f4>(L, o + 7 * 2560);
    stsc(rsWS, wsoff + t * 16, tag4((s0 + s1) + (s2 + s3), T));
  };
  float x1v[4] = {0.f, 0.f, 0.f, 0.f};
  auto rs_gather = [&](int wsbase, int nprod, int delay_done) AINL -> f4 {
    const int pl = lane >> 3, k = lane & 7;
    unsigned off[4]; v4u v[4];
#pragma unroll
    for (int q = 0; q < 4; ++q) {
      const int u = 8 * q + pl;
      const int wgu = (b << 5) | ((u & 3) << 3) | (u >> 2);
      off[q] = (k < 5 && u < nprod) ? wsbase + wgu * 4096 + h * 128 + k * 16 : OOB;
      PIN(off[q]);
    }
    const unsigned long long m0 = ballot(off[0] != OOB), m3 = ballot(off[3] != OOB);
    __builtin_amdgcn_s_sleep(0);
    (void)delay_done;
#pragma unroll
    for (int q = 0; q < 4; ++q) v[q] = ldsc(rsWS, off[q]);
    unsigned spins = 0;
    for (;;) {
      const bool k0 = tagok4(v[0], T), k1 = tagok4(v[1], T), k2 = tagok4(v[2], T), k3 = tagok4(v[3], T);
      if ((ballot(k0) & ballot(k1) & ballot(k2) & m0) == m0 && (ballot(k3) & m3) == m3) break;
      if (++spins > SPIN_CAP) break;
      __builtin_amdgcn_s_sleep(POLL_SLEEP);
      FENCE();
      const v4u n0 = ldsc(rsWS, k0 ? OOB : off[0]), n1 = ldsc(rsWS, k1 ? OOB : off[1]), n2 = ldsc(rsWS, k2 ? OOB : off[2]), n3 = ldsc(rsWS, k3 ? OOB : off[3]);
      v[0] = k0 ? v[0] : n0; v[1] = k1 ? v[1] : n1; v[2] = k2 ? v[2] : n2; v[3] = k3 ? v[3] : n3;
    }
    f4 s;
#pragma unroll
    for (int e = 0; e < 4; ++e) {
      float a = (__uint_as_float(v[0][e] & 0xffffff00u) + __uint_as_float(v[1][e] & 0xffffff00u)) + (__uint_as_float(v[2][e] & 0xffffff00u) + __uint_as_float(v[3][e] & 0xffffff00u));
      a += dppf<XOR8>(a); s[e] = x32_sum(x16_sum(a));
    }
    return s;
  };
  if (w < 4) publish(WS_OUT1 + wg * 4096);
  if (w != 4) {
#pragma unroll
    for (int i = 0; i < 10; ++i) W2[i] = ld<AUX_W>(rsW2, offW2, (i / 5) * 1280 + (i % 5) * 256);
  }
  if (w == 4) {
    const v2u xr = lds_ld<v2u>(L, L_XR + (lane & 7) * 8); const f4 gg = lds_ld<f4>(L, L_GAB + (lane & 7) * 16);
    unsigned off[3]; v4u v[3]; float sbv[12], cbv[12]; int hoff[3];
#pragma unroll
    for (int q = 0; q < 3; ++q) {
      const int idx = 64 * q + lane, jj = idx / 5, k = idx % 5;
      off[q] = (idx < 160 && mlp) ? WS_AG + (b * 32 + jj) * 128 + k * 16 : OOB;
      const int col = idx < 160 ? 20 * jj + 4 * k : 0;
      const v2u s2 = lds_ld<v2u>(L, L_SB + col * 2), c2 = lds_ld<v2u>(L, L_CB + col * 2);
      sbv[4 * q] = bflo(s2.x); sbv[4 * q + 1] = bfhi(s2.x); sbv[4 * q + 2] = bflo(s2.y); sbv[4 * q + 3] = bfhi(s2.y);
      cbv[4 * q] = bflo(c2.x); cbv[4 * q + 1] = bfhi(c2.x); cbv[4 * q + 2] = bflo(c2.y); cbv[4 * q + 3] = bfhi(c2.y);
      hoff[q] = L_H2 + col * 2;
      PIN(off[q]); PIN(hoff[q]);
    }
#pragma unroll
    for (int i = 0; i < 12; ++i) { PIN(sbv[i]); PIN(cbv[i]); }
    float xx[4] = {bflo(xr.x), bfhi(xr.x), bflo(xr.y), bfhi(xr.y)};
    const unsigned long long am0 = ballot(off[0] != OOB), am2 = ballot(off[2] != OOB);
    __builtin_amdgcn_s_sleep(D_RS1);
    const f4 s = rs_gather(WS_OUT1, 32, 0);
    v4u m;
    {
      float a0 = s[0], a1 = s[1], a2 = s[2], a3 = s[3];
      rbf2(a0, a1); rbf2(a2, a3);
      a0 *= gg[0]; a1 *= gg[1]; a2 *= gg[2]; a3 *= gg[3];
      rbf2(a0, a1); rbf2(a2, a3);
      a0 += xx[0]; a1 += xx[1]; a2 += xx[2]; a3 += xx[3];
      const unsigned p01 = cvtpk(a0, a1), p23 = cvtpk(a2, a3);
      m = v4u{(p01 << 16) | (T & 0xffu), (p01 & 0xffff0000u) | ((T >> 8) & 0xffu), (p23 << 16) | ((T >> 16) & 0xffu), (p23 & 0xffff0000u) | (T >> 24)};
      x1v[0] = bflo(p01); x1v[1] = bfhi(p01); x1v[2] = bflo(p23); x1v[3] = bfhi(p23);
    }
    if (lane < 5) stsc(rsWS, WS_AG + (b * 32 + h) * 128 + lane * 16, m);
#pragma unroll
    for (int i = 0; i < 10; ++i) W2[i] = ld<AUX_W>(rsW2, offW2, (i / 5) * 1280 + (i % 5) * 256);
    STAMP(9);
    if (mlp) {
      __builtin_amdgcn_s_sleep(D_AG);
#pragma unroll
      for (int q = 0; q < 3; ++q) v[q] = ldsc(rsWS, off[q]);
      unsigned spins = 0;
      for (;;) {
        const bool k0 = tagok4(v[0], T), k1 = tagok4(v[1], T), k2 = tagok4(v[2], T);
        if ((ballot(k0) & ballot(k1) & am0) == am0 && (ballot(k2) & am2) == am2) break;
        if (++spins > SPIN_CAP) break;
        __builtin_amdgcn_s_sleep(POLL_SLEEP);
        FENCE();
        const v4u n0 = ldsc(rsWS, k0 ? OOB : off[0]), n1 = ldsc(rsWS, k1 ? OOB : off[1]), n2 = ldsc(rsWS, k2 ? OOB : off[2]);
        v[0] = k0 ? v[0] : n0; v[1] = k1 ? v[1] : n1; v[2] = k2 ? v[2] : n2;
      }
      STAMP(13);
      float xv[12]; float sm = 0.f;
#pragma unroll
      for (int q = 0; q < 3; ++q)
#pragma unroll
        for (int e = 0; e < 4; ++e) { xv[4 * q + e] = __uint_as_float(v[q][e] & 0xffff0000u); sm += xv[4 * q + e]; }
      const float mean = wave_sum(sm) * (1.f / 640.f);
      float q2 = 0.f, q3 = 0.f;
#pragma unroll
      for (int i = 0; i < 8; ++i) { const float dd = xv[i] - mean; q2 += dd * dd; }
#pragma unroll
      for (int i = 8; i < 12; ++i) { const float dd = xv[i] - mean; q3 += dd * dd; }
      q2 += lane < 32 ? q3 : 0.f;
      const float var = rbf(wave_sum(q2) * (1.f / 640.f));
      const float mb = rbf(mean), rden = frcp(rbf(__builtin_amdgcn_sqrtf(rbf(var + 1e-5f))));
#pragma unroll
      for (int q = 0; q < 3; ++q) {
        float a[4];
#pragma unroll
        for (int e = 0; e < 4; ++e) a[e] = xv[4 * q + e] - mb;
        rbf2(a[0], a[1]); rbf2(a[2], a[3]);
#pragma unroll
        for (int e = 0; e < 4; ++e) a[e] *= rden;
        rbf2(a[0], a[1]); rbf2(a[2], a[3]);
#pragma unroll
        for (int e = 0; e < 4; ++e) a[e] *= cbv[4 * q + e];
        rbf2(a[0], a[1]); rbf2(a[2], a[3]);
        const v2u o = {cvtpk(a[0] + sbv[4 * q], a[1] + sbv[4 * q + 1]), cvtpk(a[2] + sbv[4 * q + 2], a[3] + sbv[4 * q + 3])};
        if (q < 2 || lane < 32) lds_st<v2u>(L, hoff[q], o);
      }
    }
  }
  if (mlp) {
    BAR();
    STAMP(10);
    {
      unsigned X[5];
#pragma unroll
      for (int d = 0; d < 5; ++d) X[d] = lds_ld<unsigned>(L, L_H2 + 160 * w + 80 * (c >> 3) + 20 * g + d * 4);
      f4 a0 = {0.f, 0.f, 0.f, 0.f}, a1 = {0.f, 0.f, 0.f, 0.f};
#pragma unroll
      for (int p = 0; p < 10; p += 2) { a0 = mfma(diagA(X[p >> 1], S, 0), W1[p], a0); a1 = mfma(diagA(X[p >> 1], S, 1), W1[p + 1], a1); }
      a0 += a1;
      if ((g >> 1) == (c >> 3)) lds_st<f4>(L, L_HP + (2 * w + (c >> 3)) * 256 + (8 * (c & 7) + 4 * (g & 1)) * 4, a0);
    }
    BAR();
    {
      const int kap = (c >> 2) & 1, sig = (c & 3) + 4 * (c >> 3);
      const int n = (8 * w + 2 * g + kap) * 4;
      float hd = lds_ld<float>(L, L_HP + (2 * sig) * 256 + n) + lds_ld<float>(L, L_HP + (2 * sig + 1) * 256 + n);
      hd += dppf<XOR1>(hd); hd += dppf<XOR2>(hd); hd += dppf<XOR8>(hd);
      hd = fsilu(rbf(hd));
      const float ot = dppf<XOR7>(hd);
      const unsigned X = kap ? cvtpk(ot, hd) : cvtpk(hd, ot);
      const v4u A0 = diagA(X, S, 0), A1 = diagA(X, S, 1);
      f4 y[5];
#pragma unroll
      for (int cb = 0; cb < 5; ++cb) y[cb] = mfma(A0, W2[cb], f4{0.f, 0.f, 0.f, 0.f});
#pragma unroll
      for (int cb = 0; cb < 5; ++cb) y[cb] = mfma(A1, W2[5 + cb], y[cb]);
      if (lane < 32) {
#pragma unroll
        for (int cb = 0; cb < 5; ++cb) lds_st<f4>(L, ypoff + cb * 512, y[cb]);
      }
    }
    BAR();
    STAMP(11);
    if (w < 4) publish(WS_OUT2 + wg * 4096);
  }
  if (w == 4) {
    const f4 gg = lds_ld<f4>(L, L_GAB + 80 + (lane & 7) * 16);
    if (!mlp) __builtin_amdgcn_s_sleep(48);
    __builtin_amdgcn_s_sleep(D_RS2);
    const f4 s = rs_gather(WS_OUT2, 27, 0);
    float a0 = s[0], a1 = s[1], a2 = s[2], a3 = s[3];
    rbf2(a0, a1); rbf2(a2, a3);
    a0 *= gg[0]; a1 *= gg[1]; a2 *= gg[2]; a3 *= gg[3];
    rbf2(a0, a1); rbf2(a2, a3);
    if (lane < 5) __builtin_amdgcn_raw_buffer_store_b64(v2u{cvtpk(x1v[0] + a0, x1v[1] + a1), cvtpk(x1v[2] + a2, x1v[3] + a3)}, rsOUT, b * 1280 + 40 * h + lane * 8, 0, 0);
    STAMP(12);
  }
}

extern "C" void kernel_launch(void* const* d_in, const int* in_sizes, int n_in, void* d_out, int out_size, void* d_ws, size_t ws_size,
                              hipStream_t stream) {
  fused_kernel<<<256, 512, 0, stream>>>(d_in[0], d_in[1], d_in[2], d_in[3], d_in[4], d_in[6], d_in[7], d_in[8], d_in[9], d_out, d_ws);
}
```

```cpp
#include <hip/hip_runtime.h>
#include <hip/hip_bf16.h>
#include <stdint.h>
#include <type_traits>

typedef unsigned v4u __attribute__((ext_vector_type(4)));
typedef unsigned v2u __attribute__((ext_vector_type(2)));
typedef float f4 __attribute__((ext_vector_type(4)));
typedef __bf16 bf8 __attribute__((ext_vector_type(8)));
typedef __bf16 bf2 __attribute__((ext_vector_type(2)));
typedef short s4 __attribute__((ext_vector_type(4)));
#define LAS __attribute__((address_space(3)))
typedef __amdgpu_buffer_rsrc_t rsrc_t;

#define AUX_K 2
#define AUX_V 2
#define AUX_W 0
constexpr int AUX_NT = 2, AUX_SC1 = 16;
constexpr unsigned OOB = 0x80000000u;

constexpr int WS_EPOCH = 0;
constexpr int WS_ALIVE = 4096;
constexpr int WS_XST = 16384;
constexpr int WS_AGP = 65536;
constexpr int WS_AG = 131072;
constexpr int WS_PM = 262144;
constexpr int WS_OUT1 = 1 << 20;
constexpr int WS_OUT2 = 2 << 20;
constexpr int WS_QPM = 3 << 20;
constexpr int WS_STAMP = 4 << 20;
constexpr int WS_BYTES = 8 << 20;

#define TW 6
#define DA_LO 1
#define DA_HI 1
#define DB_LO 1
#define DB_HI 0
#define VRN 14
#define VPRE 9
constexpr int NR = 16;
#define RK 12
#define RL 19
#define HK 19
#define HD 2
#define HN 3
#define QK 25
struct It { int kind, idx; };
#define WOA 10
constexpr int NA = 3 + 32 + WOA + 8;
constexpr It a_item(int m) {
  int pos = 0, wn = 0;
  for (int k = 0; k < 32; ++k) {
    if (pos == m) return It{0, k};
    ++pos;
    if (k >= 4 && k < 7) { if (pos == m) return It{4, k - 4}; ++pos; }
    else if (k >= RK && k < RK + 4) { if (pos == m) return It{5, k - RK}; ++pos; }
    else if (k >= QK && k < QK + 4) { if (pos == m) return It{6, k - QK}; ++pos; }
    else if (k >= 7 && k < 31) {
      int idx = 0; for (int u = 7; u < k; ++u) if (!(u >= RK && u < RK + 4) && !(u >= QK && u < QK + 4)) ++idx;
      if (idx * WOA / 16 != (idx + 1) * WOA / 16 && wn < WOA) { if (pos == m) return It{2, wn}; ++pos; ++wn; }
    }
  }
  return It{-1, 0};
}
static_assert(a_item(NA - 1).kind == 0 && a_item(NA - 1).idx == 31, "phase A list");
constexpr int a_free_between(int lo, int hi) { int n = 0; for (int m = lo; m <= hi; ++m) if (m >= 0 && m < NA && a_item(m).kind == 6) ++n; return n; }
constexpr int a_kpos(int k) { for (int m = 0; m < NA; ++m) if (a_item(m).kind == 0 && a_item(m).idx == k) return m; return NA; }
#define WEARLY 2
#define WLAST 27
constexpr int NBV = 32 + 20 - WOA, NB = NBV + 10;
constexpr It b_item(int m) {
  if (m < VRN) return It{1, m};
  if (m < VRN + WEARLY) return It{2, WOA + m - VRN};
  if (m >= NBV) return It{3, m - NBV};
  int pos = VRN + WEARLY, wn = WOA + WEARLY;
  for (int v = VRN; v < 32; ++v) {
    if (pos == m) return It{1, v};
    ++pos;
    int target = WOA + WEARLY + ((20 - WOA - WEARLY) * (v - VRN + 1) + (WLAST - VRN)) / (WLAST - VRN + 1);
    if (target > 20) target = 20;
    while (wn < target) { if (pos == m) return It{2, wn}; ++pos; ++wn; }
  }
  return It{-1, 0};
}
static_assert(b_item(NBV - 1).kind == 1 && b_item(NBV - 1).idx == 31, "phase B list");
constexpr int b_vpos(int k) { for (int m = 0; m < NB; ++m) if (b_item(m).kind == 1 && b_item(m).idx == k) return m; return NB; }
constexpr int b_extra(int m) { int n = 0; const int p4 = b_vpos(4 + VRN), p8 = b_vpos(8 + VRN); if (m >= p4 && m - p4 < TW) n += 4; if (m >= p8 && m - p8 < TW) n += 2; return n; }
constexpr int a_lastpark_f() { int k = NR - 1; for (int m = 0; m + TW < NA; ++m) if (a_item(m).kind == 0 && a_item(m).idx > k) k = a_item(m).idx; return k; }
constexpr int LASTPARK = a_lastpark_f();
constexpr int NLDS = LASTPARK - NR + 1;

constexpr int L_WL = 0;
constexpr int L_SC = 20480;
constexpr int SC_STRIDE = 336;
constexpr int L_PART = L_SC + 2688;
constexpr int L_WQL = 0;
constexpr int L_QP8 = 20480;
constexpr int L_HB = 32768;
constexpr int L_SB = L_HB + 1280;
constexpr int L_CB = L_SB + 1280;
constexpr int L_GAB = L_CB + 1280;
constexpr int L_XR = L_GAB + 160;
constexpr int L_ML = L_XR + 64;
constexpr int L_FLAG = L_ML + 64;
constexpr int L_QB = L_FLAG + 32;
constexpr int L_H2 = L_QB + 256;
constexpr int L_OP = L_H2 + 1280;
constexpr int L_HP = L_OP + 4096;
constexpr int L_KS = L_HP + 4096;
constexpr int L_YP = L_KS;
constexpr int L_END = L_KS + (8 * NLDS * 1024 > 20480 ? 8 * NLDS * 1024 : 20480);
static_assert(L_QP8 + 8192 <= 32768 && L_PART + 4096 <= 32768 && L_HB % 16 == 0 && L_QB % 16 == 0 && L_OP % 16 == 0 && L_KS % 16 == 0 && L_GAB % 16 == 0 && L_H2 % 16 == 0 && L_END <= 163840, "lds map");

#define D_RS1 8
#define D_AG 10
#define D_RS2 8
#define POLL_SLEEP 1
#define STAMPS 0
#define NODUTY 0
#define NOPARK 0
#define PGT 1
#define NOW 0
constexpr unsigned SPIN_CAP = NODUTY ? 4u : (1u << 21);

__device__ __forceinline__ float bflo(unsigned d) { return __uint_as_float(d << 16); }
__device__ __forceinline__ float bfhi(unsigned d) { return __uint_as_float(d & 0xffff0000u); }
__device__ __forceinline__ unsigned cvtpk(float lo, float hi) {
  bf2 r = {(__bf16)lo, (__bf16)hi}; return __builtin_bit_cast(unsigned, r);
}
__device__ __forceinline__ float rbf(float f) { return bflo(cvtpk(f, 0.f)); }
__device__ __forceinline__ void rbf2(float& a, float& b) { const unsigned p = cvtpk(a, b); a = bflo(p); b = bfhi(p); }
__device__ __forceinline__ float frcp(float x) { return __builtin_amdgcn_rcpf(x); }
__device__ __forceinline__ float fsilu(float a) { return a * frcp(1.f + __builtin_amdgcn_exp2f(a * -1.4426950408889634f)); }
template <int CTRL> __device__ __forceinline__ float dppf(float x) {
  return __builtin_bit_cast(float, __builtin_amdgcn_update_dpp(0, __builtin_bit_cast(int, x), CTRL, 0xf, 0xf, true));
}
template <int CTRL> __device__ __forceinline__ unsigned dppu(unsigned x) { return (unsigned)__builtin_amdgcn_update_dpp(0, (int)x, CTRL, 0xf, 0xf, true); }
constexpr int XOR1 = 0xB1, XOR2 = 0x4E, XOR7 = 0x141, XOR8 = 0x128, QB0 = 0x00, QB2 = 0xAA;
__device__ __forceinline__ float x16_sum(float x) { auto s = __builtin_amdgcn_permlane16_swap(__float_as_uint(x), __float_as_uint(x), false, false); return __uint_as_float(s[0]) + __uint_as_float(s[1]); }
__device__ __forceinline__ float x32_sum(float x) { auto s = __builtin_amdgcn_permlane32_swap(__float_as_uint(x), __float_as_uint(x), false, false); return __uint_as_float(s[0]) + __uint_as_float(s[1]); }
__device__ __forceinline__ float x16_max(float x) { auto s = __builtin_amdgcn_permlane16_swap(__float_as_uint(x), __float_as_uint(x), false, false); return fmaxf(__uint_as_float(s[0]), __uint_as_float(s[1])); }
__device__ __forceinline__ float x32_max(float x) { auto s = __builtin_amdgcn_permlane32_swap(__float_as_uint(x), __float_as_uint(x), false, false); return fmaxf(__uint_as_float(s[0]), __uint_as_float(s[1])); }
__device__ __forceinline__ float wave_sum(float x) {
  x += dppf<XOR1>(x); x += dppf<XOR2>(x); x += dppf<XOR7>(x); x += dppf<XOR8>(x); return x32_sum(x16_sum(x));
}
__device__ __forceinline__ float wave_max(float x) {
  x = fmaxf(x, dppf<XOR1>(x)); x = fmaxf(x, dppf<XOR2>(x)); x = fmaxf(x, dppf<XOR7>(x)); x = fmaxf(x, dppf<XOR8>(x)); return x32_max(x16_max(x));
}
__device__ __forceinline__ f4 mfma(v4u a, v4u b, f4 c) {
  return __builtin_amdgcn_mfma_f32_16x16x32_bf16(__builtin_bit_cast(bf8, a), __builtin_bit_cast(bf8, b), c, 0, 0, 0);
}
__device__ __forceinline__ float dot2(unsigned a, unsigned b, float c) {
  return __builtin_amdgcn_fdot2_f32_bf16(__builtin_bit_cast(bf2, a), __builtin_bit_cast(bf2, b), c, false);
}
__device__ __forceinline__ rsrc_t mkrs(const void* p, unsigned bytes) { return __builtin_amdgcn_make_buffer_rsrc((void*)p, 0, (int)bytes, 0x00020000); }
template <int AUX> __device__ __forceinline__ v4u ld(rsrc_t r, unsigned voff, unsigned soff) { return __builtin_amdgcn_raw_buffer_load_b128(r, (int)voff, (int)soff, AUX); }
__device__ __forceinline__ v4u ldsc(rsrc_t r, unsigned voff) { return __builtin_amdgcn_raw_buffer_load_b128(r, (int)voff, 0, AUX_SC1); }
__device__ __forceinline__ void stsc(rsrc_t r, unsigned voff, v4u v) { __builtin_amdgcn_raw_buffer_store_b128(v, r, (int)voff, 0, AUX_SC1); }
__device__ __forceinline__ void stsc2(rsrc_t r, unsigned voff, v2u v) { __builtin_amdgcn_raw_buffer_store_b64(v, r, (int)voff, 0, AUX_SC1); }
#define WAITVM(n) asm volatile("s_waitcnt vmcnt(%0)" ::"n"((n) > 63 ? 63 : (n)) : "memory")
#define WAITW(n, lo, hi) do { if ((lo) == (hi)) WAITVM((n) - (lo)); else if (w < 4) WAITVM((n) - (lo)); else WAITVM((n) - (hi)); } while (0)
#define BAR() asm volatile("s_waitcnt lgkmcnt(0)\n\ts_barrier" ::: "memory")
#define SB() __builtin_amdgcn_sched_barrier(0)
#define FENCE() asm volatile("" ::: "memory")
#define PIN(x) asm volatile("" : "+v"(x))

template <typename T> __device__ __forceinline__ T lds_ld(LAS unsigned char* L, int off) { return *(LAS T*)(L + off); }
template <typename T> __device__ __forceinline__ void lds_st(LAS unsigned char* L, int off, T v) { *(LAS T*)(L + off) = v; }
template <int I, int N, typename F> __device__ __forceinline__ void sfor(F&& f) {
  if constexpr (I < N) { f(std::integral_constant<int, I>{}); sfor<I + 1, N>(f); }
}
template <int I> using IC = std::integral_constant<int, I>;
#define AINL __attribute__((always_inline))

struct Sel { unsigned s[2][4]; };
__device__ __forceinline__ v4u diagA(unsigned X, const Sel& S, int par) {
  v4u a;
#pragma unroll
  for (int d = 0; d < 4; ++d) a[d] = __builtin_amdgcn_perm(X, X, S.s[par][d]);
  return a;
}
__device__ __forceinline__ v4u tag4(f4 v, unsigned T) {
  v4u r;
#pragma unroll
  for (int e = 0; e < 4; ++e) r[e] = ((__float_as_uint(v[e]) + 0x80u) & 0xffffff00u) | ((T >> (8 * e)) & 0xffu);
  return r;
}
__device__ __forceinline__ bool tagok4(v4u v, unsigned T) {
  const unsigned lo = __builtin_amdgcn_perm(v.y, v.x, 0x0c0c0400u), hi = __builtin_amdgcn_perm(v.w, v.z, 0x04000c0cu);
  return (lo | hi) == T;
}
__device__ __forceinline__ unsigned long long ballot(bool p) { return __builtin_amdgcn_ballot_w64(p); }

#define STAMPK 0
#define STAMP(n) do { } while (0)
#define KSTAMP(k) do { } while (0)

__global__ void __launch_bounds__(512, 2)
fused_kernel(const void* px, const void* pk, const void* pv, const void* pwq,
             const void* pwo, const void* pw1, const void* pw2, const void* pc,
             const void* pwm, void* pout, void* pws) {
  __shared__ __attribute__((aligned(16))) unsigned char smem[L_END];
  LAS unsigned char* L = (LAS unsigned char*)smem;
  const int t = threadIdx.x, lane = t & 63, w = __builtin_amdgcn_readfirstlane(t >> 6);
  const int wg = blockIdx.x, xg = wg & 7, e8 = wg >> 3, b = wg >> 5, h = xg * 4 + (e8 & 3);
  const int g = lane >> 4, c = lane & 15;
  const bool mlp = h < 27;
  STAMP(0);
  const rsrc_t rsWS = mkrs(pws, WS_BYTES);
  const rsrc_t rsK = mkrs((const char*)pk + ((size_t)(b * 32 + h) * 4096 + 3072) * 256, 262144);
  const rsrc_t rsV = mkrs((const char*)pv + ((size_t)(b * 32 + h) * 4096 + 3072) * 256, 262144);
  const rsrc_t rsWQ = mkrs(pwq, 640 * 4096 * 2), rsWO = mkrs(pwo, 4096 * 640 * 2), rsW1 = mkrs(pw1, 640 * 1728 * 2), rsW2 = mkrs(pw2, 1728 * 640 * 2);
  const rsrc_t rsWM = mkrs(pwm, 640 * 3840 * 2), rsC = mkrs(pc, 10240), rsX = mkrs(px, 10240), rsOUT = mkrs(pout, 10240);

  const bool role = wg < 240;
  const int ta = wg % 60, kq = wg / 60;
  unsigned Eraw = 0;
  if (w == 0) Eraw = __builtin_amdgcn_raw_buffer_load_b32(rsWS, WS_EPOCH, 0, AUX_SC1);
  v4u cv0 = ld<AUX_NT>(rsC, (role && t < 160) ? (t / 20) * 1280 + kq * 320 + (t % 20) * 16 : OOB, 0);
  const unsigned wmoff = (unsigned)(160 * kq + 8 * w + (lane >> 3)) * 7680u + ta * 128 + (lane & 7) * 16;
  v4u wm0 = ld<AUX_NT>(rsWM, role ? wmoff : OOB, 0);
  v4u wm1 = ld<AUX_NT>(rsWM, role ? wmoff : OOB, 64 * 7680);
  v4u wm2 = ld<AUX_NT>(rsWM, (role && w < 4) ? wmoff : OOB, 128 * 7680);
  unsigned pg0 = 0, pg1 = 0, pg2 = 0, pg3 = 0, pg4 = 0, pg5 = 0;
  if (PGT && w == 7) {
    pg0 = __builtin_amdgcn_raw_buffer_load_b32(rsV, lane < 2 ? lane * 262140 : OOB, 0, AUX_NT);
    pg1 = __builtin_amdgcn_raw_buffer_load_b32(rsWO, lane < 2 ? h * 163840 + lane * 163836 : OOB, 0, AUX_W);
    pg2 = __builtin_amdgcn_raw_buffer_load_b32(rsW1, lane < 8 ? lane * 80 * 3456 + h * 128 : OOB, 0, AUX_W);
    pg3 = __builtin_amdgcn_raw_buffer_load_b32(rsW2, lane < 2 ? h * 81920 + lane * 81916 : OOB, 0, AUX_W);
    pg4 = __builtin_amdgcn_raw_buffer_load_b32(rsWS, lane < 16 ? lane * 262144 + 1024 : OOB, 0, AUX_SC1);
    pg5 = __builtin_amdgcn_raw_buffer_load_b32(rsOUT, lane == 0 ? b * 1280 : OOB, 0, AUX_NT);
  }
  if (t < 8) lds_st<unsigned>(L, L_FLAG + t * 4, 0u);
  SB();
  asm volatile("s_barrier" ::: "memory");
  unsigned po[4]; v4u pvv[4];
  v4u WQ[3], WO[20], KB[NR], KL[TW + 1];
  const unsigned offWQ = NOW ? OOB : (unsigned)(80 * b + 20 * g + w) * 8192u + h * 256 + c * 16;
  const unsigned offWO = NOW ? OOB : (unsigned)(h * 128 + 16 * w + 4 * g) * 1280u + c * 16;
  const unsigned offKV = w * 1024 + lane * 16;
  const int ksoff = L_KS + w * (NLDS * 1024) + lane * 16;
  auto ldwo = [&](auto ic) AINL { constexpr int i = decltype(ic)::value; WO[i] = ld<AUX_W>(rsWO, offWO, (i / 5) * 1280 + (i % 5) * 256); };
  auto a_issue = [&](auto mc) AINL {
    constexpr It it = a_item(decltype(mc)::value);
    if constexpr (it.kind == 4) WQ[it.idx] = ld<AUX_NT>(rsWQ, (it.idx < 2 || w < 4) ? offWQ : OOB, it.idx * 8 * 8192);
    else if constexpr (it.kind == 2) ldwo(IC<it.idx>{});
    else if constexpr (it.kind == 5 || it.kind == 6) pvv[it.idx] = ldsc(rsWS, po[it.idx]);
    else if constexpr (it.idx < NR) KB[it.idx] = ld<AUX_K>(rsK, offKV, it.idx * 8192);
    else KL[(it.idx - NR) % (TW + 1)] = ld<AUX_K>(rsK, offKV, it.idx * 8192);
  };
  constexpr int NINIT0 = 2, NINIT1 = 4, NINIT2 = 5, NINIT3 = 7, NINIT = 8;
  static_assert(NINIT >= TW && a_item(NINIT - 1).idx < NR, "no parking during P1");
  sfor<0, NINIT0>(a_issue);
  SB();
  STAMP(1);
  WAITVM(NINIT0);
  sfor<NINIT0, NINIT1>(a_issue);
  SB();
  if (w == 0 && lane == 0) lds_st<unsigned>(L, L_ML, Eraw + 1u);
  if (wg >= 240 && wg < 248 && w == 0) {
    const unsigned T = (unsigned)__builtin_amdgcn_readfirstlane((int)Eraw) + 1u;
    const v4u xa = ld<AUX_NT>(rsX, (wg - 240) * 1280 + lane * 16, 0);
    const unsigned xb = __builtin_amdgcn_raw_buffer_load_b32(rsX, (wg - 240) * 1280 + 1024 + lane * 4, 0, AUX_NT);
    float xv[10];
#pragma unroll
    for (int d = 0; d < 4; ++d) { xv[2 * d] = bflo(xa[d]); xv[2 * d + 1] = bfhi(xa[d]); }
    xv[8] = bflo(xb); xv[9] = bfhi(xb);
    const float sm = ((xv[0] + xv[1]) + (xv[2] + xv[3])) + ((xv[4] + xv[5]) + (xv[6] + xv[7])) + (xv[8] + xv[9]);
    const float mean = wave_sum(sm) * (1.f / 640.f);
    float q2 = 0.f;
#pragma unroll
    for (int d = 0; d < 10; ++d) { const float dd = xv[d] - mean; q2 = fmaf(dd, dd, q2); }
    const float var = rbf(wave_sum(q2) * (1.f / 640.f));
    const float mb = rbf(mean), den = rbf(__builtin_amdgcn_sqrtf(rbf(var + 1e-5f)));
    if (lane == 0) stsc(rsWS, WS_XST + (wg - 240) * 16, v4u{__float_as_uint(mb) | (T & 0xffu), __float_as_uint(den) | ((T >> 8) & 0xffu), (T >> 16) & 0xffu, T >> 24});
  }
  {
    auto silu8 = [&](v4u v) AINL {
      v4u r;
#pragma unroll
      for (int d = 0; d < 4; ++d) r[d] = cvtpk(fsilu(bflo(v[d])), fsilu(bfhi(v[d])));
      return r;
    };
    if (t < 160) lds_st<v4u>(L, L_SC + (t / 20) * SC_STRIDE + (t % 20) * 16, silu8(cv0));
    lds_st<v4u>(L, L_WL + w * 1024 + lane * 16, wm0);
    lds_st<v4u>(L, L_WL + (w + 8) * 1024 + lane * 16, wm1);
    if (w < 4) lds_st<v4u>(L, L_WL + (w + 16) * 1024 + lane * 16, wm2);
  }
  SB();
  sfor<NINIT1, NINIT2>(a_issue);
  v2u xh = __builtin_amdgcn_raw_buffer_load_b64(rsX, lane < 20 ? w * 1280 + 160 * b + lane * 8 : OOB, 0, AUX_NT);
  v2u xown = __builtin_amdgcn_raw_buffer_load_b64(rsX, (w == 4 && lane < 5) ? b * 1280 + 40 * h + lane * 8 : OOB, 0, AUX_NT);
  SB();
  BAR();
  const unsigned T = (unsigned)__builtin_amdgcn_readfirstlane((int)lds_ld<unsigned>(L, L_ML));
  asm volatile("" :: "v"(pg0), "v"(pg1), "v"(pg2), "v"(pg3), "v"(pg4), "v"(pg5));
  STAMP(2);
  sfor<NINIT2, NINIT3>([&](auto mc) AINL { WAITVM(TW - 2 + 2); a_issue(mc); SB(); });
  {
    f4 acc = {0.f, 0.f, 0.f, 0.f};
    const int tl = w & 3, s0_ = (w >> 2) * 3;
    const int trbase = L_WL + (8 * g + ((lane & 15) >> 2)) * 128 + tl * 32 + (lane & 3) * 8;
#pragma unroll
    for (int n = 0; n < 3; ++n) {
      if (n < 2 || w < 4) {
        const int s = s0_ + n;
        const v4u a = lds_ld<v4u>(L, L_SC + (c & 7) * SC_STRIDE + s * 64 + g * 16);
        const s4 b0 = __builtin_amdgcn_ds_read_tr16_b64_v4i16((LAS s4*)(L + trbase + s * 4096));
        const s4 b1 = __builtin_amdgcn_ds_read_tr16_b64_v4i16((LAS s4*)(L + trbase + s * 4096 + 512));
        const v2u u0 = __builtin_bit_cast(v2u, b0), u1 = __builtin_bit_cast(v2u, b1);
        acc = mfma(a, v4u{u0.x, u0.y, u1.x, u1.y}, acc);
      }
    }
    if (g < 2) {
#pragma unroll
      for (int r = 0; r < 4; ++r) lds_st<float>(L, L_PART + (w >> 2) * 2048 + (4 * g + r) * 256 + (16 * tl + c) * 4, acc[r]);
    }
  }
  sfor<NINIT3, NINIT>([&](auto mc) AINL { WAITVM(TW - 2 + 2); a_issue(mc); SB(); });
  BAR();
  {
    const f4 s = lds_ld<f4>(L, L_PART + (t & 127) * 16) + lds_ld<f4>(L, L_PART + 2048 + (t & 127) * 16);
    unsigned o = OOB;
    if (t < 128) { if (role) o = WS_PM + (((t >> 4) * 4 + kq) * 3840 + ta * 64) * 4 + (t & 15) * 16; }
    else if (t == 128) o = WS_ALIVE + wg * 16;
    stsc(rsWS, o, tag4(s, T));
  }
  STAMP(3);
  Sel S;
  {
    const int e = c & 7;
#pragma unroll
    for (int par = 0; par < 2; ++par)
#pragma unroll
      for (int d = 0; d < 4; ++d) {
        const unsigned lo = (unsigned)(2 * par) | ((unsigned)(2 * par + 1) << 8);
        S.s[par][d] = (d == (e >> 1)) ? ((e & 1) ? (0x0c0cu | (lo << 16)) : (0x0c0c0000u | lo)) : 0x0c0c0c0cu;
      }
  }
  unsigned long long pm0, pm1, pm2, pm3;
  auto pmasks = [&]() AINL { pm0 = ballot(po[0] != OOB); pm1 = ballot(po[1] != OOB); pm2 = ballot(po[2] != OOB); pm3 = ballot(po[3] != OOB); };
#pragma unroll
  for (int q = 0; q < 4; ++q) po[q] = lane < 40 ? WS_PM + ((w * 4 + q) * 3840 + (lane / 20) * 640 + 80 * b + 4 * (lane % 20)) * 4 : OOB;
  if (lane == 40) po[0] = WS_XST + w * 16;
  pmasks();
  auto pissue = [&]() AINL {
#pragma unroll
    for (int q = 0; q < 4; ++q) pvv[q] = ldsc(rsWS, po[q]);
  };
  auto pok = [&]() AINL {
    return (ballot(tagok4(pvv[0], T)) & pm0) == pm0 && (ballot(tagok4(pvv[1], T)) & pm1) == pm1 && (ballot(tagok4(pvv[2], T)) & pm2) == pm2 && (ballot(tagok4(pvv[3], T)) & pm3) == pm3;
  };
  auto preload = [&]() AINL {
#pragma unroll
    for (int q = 0; q < 4; ++q) { const bool k1 = tagok4(pvv[q], T); const v4u nv = ldsc(rsWS, k1 ? OOB : po[q]); pvv[q] = k1 ? pvv[q] : nv; }
  };
  auto pblock = [&]() AINL {
    unsigned spins = 0;
    while (!pok() && ++spins < SPIN_CAP) { __builtin_amdgcn_s_sleep(2); FENCE(); preload(); }
  };
  auto psum = [&]() AINL {
    f4 mv;
#pragma unroll
    for (int e = 0; e < 4; ++e) mv[e] = (__uint_as_float(pvv[0][e] & 0xffffff00u) + __uint_as_float(pvv[1][e] & 0xffffff00u)) +
                                        (__uint_as_float(pvv[2][e] & 0xffffff00u) + __uint_as_float(pvv[3][e] & 0xffffff00u));
    float a0 = mv[0], a1 = mv[1], a2 = mv[2], a3 = mv[3]; rbf2(a0, a1); rbf2(a2, a3);
    return f4{a0, a1, a2, a3};
  };
  bool hdone = false;
  auto rprocess = [&]() AINL {
    const f4 mv = psum();
    const float xmean = __uint_as_float((unsigned)__builtin_amdgcn_readlane((int)pvv[0].x, 40) & 0xffff0000u);
    const float xrden = frcp(__uint_as_float((unsigned)__builtin_amdgcn_readlane((int)pvv[0].y, 40) & 0xffff0000u));
    float lnv[4] = {bflo(xh.x) - xmean, bfhi(xh.x) - xmean, bflo(xh.y) - xmean, bfhi(xh.y) - xmean};
    rbf2(lnv[0], lnv[1]); rbf2(lnv[2], lnv[3]);
#pragma unroll
    for (int e = 0; e < 4; ++e) lnv[e] *= xrden;
    rbf2(lnv[0], lnv[1]); rbf2(lnv[2], lnv[3]);
    float hv[4];
#pragma unroll
    for (int e = 0; e < 4; ++e) {
      const float ca = __uint_as_float(__builtin_amdgcn_ds_bpermute(((lane + 20) & 63) * 4, (int)__float_as_uint(mv[e])));
      hv[e] = rbf(lnv[e] * rbf(1.f + ca)) + mv[e];
    }
    if (lane < 20) lds_st<v2u>(L, L_HB + w * 160 + lane * 8, v2u{cvtpk(hv[0], hv[1]), cvtpk(hv[2], hv[3])});
    asm volatile("s_waitcnt lgkmcnt(0)" ::: "memory");
    if (lane == 0) __hip_atomic_fetch_add((LAS unsigned*)(L + L_FLAG), 1u, __ATOMIC_RELAXED, __HIP_MEMORY_SCOPE_WORKGROUP);
    STAMP(14);
  };
  auto mprocess = [&]() AINL {
    const f4 mv = psum();
    v4u msg;
#pragma unroll
    for (int e = 0; e < 4; ++e) msg[e] = (__float_as_uint(rbf(lane >= 10 ? 1.f + mv[e] : mv[e])) & 0xffff0000u) | ((T >> (8 * e)) & 0xffu);
    if (lane >= 5 && lane < 15) stsc(rsWS, WS_AGP + (b * 32 + h) * 256 + lane * 16, msg);
    else if (lane < 5) lds_st<f4>(L, L_GAB + lane * 16, mv);
    else if (lane < 20) lds_st<f4>(L, L_GAB + 80 + (lane - 15) * 16, mv);
    STAMP(17);
  };
  auto bfpack = [&](v4u v) AINL { return v2u{__builtin_amdgcn_perm(v.y, v.x, 0x07060302u), __builtin_amdgcn_perm(v.w, v.z, 0x07060302u)}; };
  auto hflag = [&]() AINL { FENCE(); return __builtin_amdgcn_readfirstlane((int)*(volatile LAS unsigned*)(L + L_FLAG)) == 8; };
  auto hstep = [&]() AINL {
    const int pi = w & 3, hf = w >> 2;
    unsigned X[5]; v4u Bp[10];
#pragma unroll
    for (int d = 0; d < 5; ++d) X[d] = lds_ld<unsigned>(L, L_HB + (2 * pi + (c >> 3)) * 160 + 40 * g + 20 * hf + d * 4);
#pragma unroll
    for (int i = 0; i < 10; ++i) Bp[i] = lds_ld<v4u>(L, L_WQL + (10 * hf + i) * 1024 + lane * 16);
    f4 a0 = {0.f, 0.f, 0.f, 0.f}, a1 = {0.f, 0.f, 0.f, 0.f};
#pragma unroll
    for (int i = 0; i < 10; i += 2) { a0 = mfma(diagA(X[i >> 1], S, 0), Bp[i], a0); a1 = mfma(diagA(X[i >> 1], S, 1), Bp[i + 1], a1); }
    a0 += a1;
    lds_st<f4>(L, L_QP8 + w * 1024 + lane * 16, a0);
    asm volatile("s_waitcnt lgkmcnt(0)" ::: "memory");
    unsigned old = 0;
    if (lane == 0) old = __hip_atomic_fetch_add((LAS unsigned*)(L + L_FLAG + 4 + pi * 4), 1u, __ATOMIC_RELAXED, __HIP_MEMORY_SCOPE_WORKGROUP);
    if (__builtin_amdgcn_readfirstlane((int)old) == 1) {
      FENCE();
      const f4 lo = lds_ld<f4>(L, L_QP8 + pi * 1024 + lane * 16), hi = lds_ld<f4>(L, L_QP8 + (pi + 4) * 1024 + lane * 16);
      stsc(rsWS, WS_QPM + (h * 8 + b) * 4096 + (2 * pi + (g >> 1)) * 512 + (2 * c + (g & 1)) * 16, tag4(lo + hi, T));
      STAMP(16);
    }
  };
  static_assert(a_kpos(RL) >= a_kpos(6) + 1 + TW, "wq parked before the first h arrival");
  static_assert(a_kpos(RL) >= a_kpos(RK + 3) + 1 + TW - 1 && RL <= HK && HK + (HN - 1) * HD < QK - 1 && RL < QK - 1 && QK + 3 < 31, "stage order");
  auto pnone = [&]() AINL {
#pragma unroll
    for (int q = 0; q < 4; ++q) po[q] = OOB;
  };
  sfor<NINIT, NA>([&](auto mc) AINL {
    constexpr int m = decltype(mc)::value;
    constexpr It it = a_item(m);
    constexpr int k = it.kind == 0 ? it.idx : -100;
    WAITW(TW - 1 + (m - (TW - DA_LO) < NINIT ? 1 : 0) + a_free_between(m - (TW - DA_LO), m - 1), DA_LO, DA_HI);
    {
      constexpr It ol = a_item(m - TW);
      if constexpr (ol.kind == 4) { if (ol.idx < 2 || w < 4) lds_st<v4u>(L, L_WQL + (w + 8 * ol.idx) * 1024 + lane * 16, WQ[ol.idx]); }
      if constexpr (!NOPARK && ol.kind == 0 && ol.idx >= NR) lds_st<v4u>(L, ksoff + (ol.idx - NR) * 1024, KL[(ol.idx - NR) % (TW + 1)]);
    }
    a_issue(mc);
    if constexpr (k == 9) { if (w == 4 && lane < 5) lds_st<v2u>(L, L_XR + lane * 8, xown); }
    KSTAMP(k);
    if constexpr (k == 8) STAMP(18);
    if constexpr (k == 16) STAMP(19);
    if constexpr (k == 24) STAMP(20);
    if constexpr (!NODUTY) {
    if constexpr (k == RL) { pblock(); rprocess(); }
    if constexpr (k >= HK && k < HK + (HN - 1) * HD && (k - HK) % HD == 0) { if (!hdone && hflag()) { hstep(); hdone = true; } }
    if constexpr (k == HK + (HN - 1) * HD) {
      if (!hdone) { unsigned spins = 0; while (!hflag() && ++spins < (SPIN_CAP << 3)) __builtin_amdgcn_s_sleep(1); hstep(); }
    }
    if constexpr (k == QK - 1) {
#pragma unroll
      for (int q = 0; q < 4; ++q) {
        unsigned o = OOB;
        if (w == 5) { if (lane < 20) o = WS_PM + ((b * 4 + q) * 3840 + (2 + lane / 5) * 640 + 20 * h + 4 * (lane % 5)) * 4; }
        else if (w == 6) o = WS_QPM + ((h * 8 + 2 * q + (lane >> 5)) * 8 + b) * 512 + (lane & 31) * 16;
        else if (w == 4 && wg == 0) o = WS_ALIVE + (64 * q + lane) * 16;
        po[q] = o;
      }
      pmasks();
    }
    }
    SB();
  });
  STAMP(4);
  v4u W1[10], W2[10], VR[VRN];
  const unsigned offW1 = (mlp && !NOW) ? (unsigned)(80 * w + 40 * (c >> 3) + 10 * g) * 3456u + (64 * h + 8 * (c & 7)) * 2 : OOB;
  const unsigned offW2 = (mlp && !NOW) ? (unsigned)(64 * h + 8 * w + 2 * g) * 1280u + c * 16 : OOB;
  auto b_issue = [&](auto mc) AINL {
    constexpr It it = b_item(decltype(mc)::value);
    if constexpr (it.kind == 1) VR[it.idx % VRN] = ld<AUX_V>(rsV, offKV, it.idx * 8192);
    else if constexpr (it.kind == 2) ldwo(IC<it.idx>{});
    else W1[it.idx] = ld<AUX_W>(rsW1, offW1, it.idx * 3456);
    SB();
  };
#define VTICK(k) do { WAITW(TW - 1, DB_LO, DB_HI); b_issue(IC<(k)>{}); } while (0)
  sfor<0, VPRE>([&](auto kc) AINL { WAITW(TW - 1 + a_free_between(NA + decltype(kc)::value - (TW - DB_LO), NA - 1), DB_LO, DB_HI); b_issue(kc); });
  if (!NODUTY && w == 5) { pblock(); mprocess(); }
  if (!NODUTY && (w == 6 || w == 4)) {
    pblock();
    if (w == 6) {
      f4 q4;
#pragma unroll
      for (int e = 0; e < 4; ++e) q4[e] = x32_sum((__uint_as_float(pvv[0][e] & 0xffffff00u) + __uint_as_float(pvv[1][e] & 0xffffff00u)) +
                                                  (__uint_as_float(pvv[2][e] & 0xffffff00u) + __uint_as_float(pvv[3][e] & 0xffffff00u)));
      if (lane < 32) lds_st<v2u>(L, L_QB + lane * 8, v2u{cvtpk(q4[0], q4[1]), cvtpk(q4[2], q4[3])});
    } else {
      if (wg == 0 && lane == 0) __builtin_amdgcn_raw_buffer_store_b32(T, rsWS, WS_EPOCH, 0, AUX_SC1);
    }
  }
  BAR();
  STAMP(5);
  const v4u qr = lds_ld<v4u>(L, L_QB + c * 16);
  STAMP(6);
  auto kdot = [&](v4u k) AINL { return dot2(k.w, qr[3], dot2(k.z, qr[2], dot2(k.y, qr[1], dot2(k.x, qr[0], 0.f)))); };
  auto treduce = [&](float (&xs)[16]) AINL {
    float x[8];
#pragma unroll
    for (int i = 0; i < 8; ++i) { const float lo = xs[i] + dppf<XOR8>(xs[i]), hi = xs[i + 8] + dppf<XOR8>(xs[i + 8]); x[i] = (c & 8) ? hi : lo; }
#pragma unroll
    for (int i = 0; i < 4; ++i) { const float lo = x[i] + dppf<XOR7>(x[i]), hi = x[i + 4] + dppf<XOR7>(x[i + 4]); x[i] = (c & 4) ? hi : lo; }
#pragma unroll
    for (int i = 0; i < 2; ++i) { const float lo = x[i] + dppf<XOR2>(x[i]), hi = x[i + 2] + dppf<XOR2>(x[i + 2]); x[i] = (c & 2) ? hi : lo; }
    const float lo = x[0] + dppf<XOR1>(x[0]), hi = x[1] + dppf<XOR1>(x[1]);
    return (c & 1) ? hi : lo;
  };
  constexpr float C2 = 0.08838834764831845f * 1.4426950408889634f;
  float mw, fw = 0.f;
  unsigned ppk;
  {
    float xs[16];
#pragma unroll
    for (int j = 0; j < 16; ++j) xs[j] = kdot(KB[j]);
    SB();
    VTICK(VPRE);
    const float s0 = treduce(xs);
    SB();
    VTICK(VPRE + 1);
#pragma unroll
    for (int j = 0; j <= LASTPARK - NR; ++j) xs[j] = kdot(lds_ld<v4u>(L, ksoff + j * 1024));
#pragma unroll
    for (int j = LASTPARK - NR + 1; j < 16; ++j) xs[j] = kdot(KL[j % (TW + 1)]);
    SB();
    sfor<VPRE + 2, (VPRE + 4 < VRN + WEARLY ? VPRE + 4 : VRN + WEARLY)>([&](auto kc) AINL { WAITW(TW - 1, DB_LO, DB_HI); b_issue(kc); });
    const float s1 = treduce(xs);
    SB();
    sfor<(VPRE + 4 < VRN + WEARLY ? VPRE + 4 : VRN + WEARLY), VRN + WEARLY>([&](auto kc) AINL { WAITW(TW - 1, DB_LO, DB_HI); b_issue(kc); });
    mw = wave_max(fmaxf(s0, s1));
    const float p0 = __builtin_amdgcn_exp2f((s0 - mw) * C2), p1 = __builtin_amdgcn_exp2f((s1 - mw) * C2);
    const float lw = wave_sum(p0 + p1);
    ppk = cvtpk(p0, p1);
    if (lane == 0) lds_st<v2u>(L, L_ML + w * 8, v2u{__float_as_uint(mw), __float_as_uint(lw)});
  }
  STAMP(15);
  f4 oacc = {0.f, 0.f, 0.f, 0.f};
  unsigned touch0 = 0, touch1 = 0;
  auto pvstep = [&](auto jc) AINL {
    constexpr int j = decltype(jc)::value;
    const unsigned X = dppu<0x150 + (j & 15)>(ppk);
    oacc = mfma(diagA(X, S, j >> 4), VR[j % VRN], oacc);
    if constexpr (j == 4) {
#pragma unroll
      for (int q = 0; q < 4; ++q) { const int idx = 64 * q + lane; po[q] = ((w == 1 || w == 2) && mlp && idx < 160) ? WS_AGP + (b * 32 + idx / 5) * 256 + (5 * w + idx % 5) * 16 : OOB; }
      pmasks(); pissue();
    }
    if constexpr (j == 14) {
      if (!NODUTY && (w == 1 || w == 2)) {
        pblock();
#pragma unroll
        for (int n = 0; n < 3; ++n) {
          const int idx = 64 * n + lane;
          if (po[n] != OOB) lds_st<v2u>(L, (w == 1 ? L_SB : L_CB) + (20 * (idx / 5) + 4 * (idx % 5)) * 2, bfpack(pvv[n]));
        }
      }
    }
    if constexpr (j == 8) {
      unsigned o0 = OOB, o1 = OOB;
      if (w == 4) {
        const int u = lane & 31; const int wgu = (b << 5) | ((u & 3) << 3) | (u >> 2);
        o0 = (lane < 32 ? WS_OUT1 : WS_OUT2) + wgu * 4096 + h * 128;
        o1 = lane < 32 ? WS_AG + (b * 32 + lane) * 128 : OOB;
      } else if (w == 0) { o0 = (lane < 32 ? WS_OUT1 : WS_OUT2) + wg * 4096 + (lane & 31) * 128; }
      touch0 = __builtin_amdgcn_raw_buffer_load_b32(rsWS, (int)o0, 0, AUX_SC1);
      touch1 = __builtin_amdgcn_raw_buffer_load_b32(rsWS, (int)o1, 0, AUX_SC1);
    }
    if constexpr (j == 16) { asm volatile("" :: "v"(touch0), "v"(touch1)); }
    if constexpr (j == 10) {
      BAR();
      float M = -INFINITY;
      v2u ml[8];
#pragma unroll
      for (int ww = 0; ww < 8; ++ww) { ml[ww] = lds_ld<v2u>(L, L_ML + ww * 8); M = fmaxf(M, __uint_as_float(ml[ww].x)); }
      float Ls = 0.f;
#pragma unroll
      for (int ww = 0; ww < 8; ++ww) Ls += __builtin_amdgcn_exp2f((__uint_as_float(ml[ww].x) - M) * C2) * __uint_as_float(ml[ww].y);
      fw = __builtin_amdgcn_exp2f((mw - M) * C2) * frcp(Ls);
      PIN(fw);
    }
  };
  sfor<VRN + WEARLY, NB>([&](auto mc) AINL {
    constexpr int m = decltype(mc)::value;
    constexpr It it = b_item(m);
    if constexpr (it.kind == 1) pvstep(IC<it.idx - VRN>{});
    if constexpr (m == NBV) sfor<32 - VRN, 22>([&](auto jc) AINL { pvstep(jc); SB(); });
    if constexpr (it.kind != 3) WAITW(TW - 1 + b_extra(m), DB_LO, DB_HI);
    else { pvstep(IC<22 + it.idx>{}); }
    b_issue(mc);
  });
  STAMP(7);
  {
    const f4 ov = oacc * fw;
    if (lane < 32) lds_st<f4>(L, L_OP + w * 512 + (8 * c + 4 * g) * 4, ov);
  }
  BAR();
  const int ypoff = L_YP + w * 2560 + (8 * c + 4 * (g & 1)) * 4;
  {
    const int kap = (c >> 1) & 3, sig = (c & 1) + 2 * (c >> 3);
    const int ko = (16 * w + 4 * g + kap) * 4;
    float o = lds_ld<float>(L, L_OP + (2 * sig) * 512 + ko) + lds_ld<float>(L, L_OP + (2 * sig + 1) * 512 + ko);
    o += dppf<XOR1>(o); o += dppf<XOR8>(o);
    const unsigned P = cvtpk(dppf<QB0>(o), dppf<QB2>(o)), Q = dppu<XOR7>(P);
    const unsigned X0 = (c & 4) ? Q : P, X1 = (c & 4) ? P : Q;
    f4 y[5];
    { const v4u A = diagA(X0, S, 0);
#pragma unroll
      for (int cb = 0; cb < 5; ++cb) y[cb] = mfma(A, WO[cb], f4{0.f, 0.f, 0.f, 0.f}); }
    { const v4u A = diagA(X0, S, 1);
#pragma unroll
      for (int cb = 0; cb < 5; ++cb) y[cb] = mfma(A, WO[5 + cb], y[cb]); }
    { const v4u A = diagA(X1, S, 0);
#pragma unroll
      for (int cb = 0; cb < 5; ++cb) y[cb] = mfma(A, WO[10 + cb], y[cb]); }
    { const v4u A = diagA(X1, S, 1);
#pragma unroll
      for (int cb = 0; cb < 5; ++cb) y[cb] = mfma(A, WO[15 + cb], y[cb]); }
    if (lane < 32) {
#pragma unroll
      for (int cb = 0; cb < 5; ++cb) lds_st<f4>(L, ypoff + cb * 512, y[cb]);
    }
  }
  BAR();
  STAMP(8);
  auto publish = [&](int wsoff) AINL {
    const int k = t & 7, j = t >> 3;
    const int o = L_YP + (20 * j + 4 * (k < 5 ? k : 0)) * 4;
    f4 s0 = lds_ld<f4>(L, o) + lds_ld<f4>(L, o + 2560), s1 = lds_ld<f4>(L, o + 2 * 2560) + lds_ld<f4>(L, o + 3 * 2560);
    f4 s2 = lds_ld<f4>(L, o + 4 * 2560) + lds_ld<f4>(L, o + 5 * 2560), s3 = lds_ld<f4>(L, o + 6 * 2560) + lds_ld<f4>(L, o + 7 * 2560);
    stsc(rsWS, wsoff + t * 16, tag4((s0 + s1) + (s2 + s3), T));
  };
  float x1v[4] = {0.f, 0.f, 0.f, 0.f};
  auto rs_gather = [&](int wsbase, int nprod, int delay_done) AINL -> f4 {
    const int pl = lane >> 3, k = lane & 7;
    unsigned off[4]; v4u v[4];
#pragma unroll
    for (int q = 0; q < 4; ++q) {
      const int u = 8 * q + pl;
      const int wgu = (b << 5) | ((u & 3) << 3) | (u >> 2);
      off[q] = (k < 5 && u < nprod) ? wsbase + wgu * 4096 + h * 128 + k * 16 : OOB;
      PIN(off[q]);
    }
    const unsigned long long m0 = ballot(off[0] != OOB), m3 = ballot(off[3] != OOB);
    __builtin_amdgcn_s_sleep(0);
    (void)delay_done;
#pragma unroll
    for (int q = 0; q < 4; ++q) v[q] = ldsc(rsWS, off[q]);
    unsigned spins = 0;
    for (;;) {
      const bool k0 = tagok4(v[0], T), k1 = tagok4(v[1], T), k2 = tagok4(v[2], T), k3 = tagok4(v[3], T);
      if ((ballot(k0) & ballot(k1) & ballot(k2) & m0) == m0 && (ballot(k3) & m3) == m3) break;
      if (++spins > SPIN_CAP) break;
      __builtin_amdgcn_s_sleep(POLL_SLEEP);
      FENCE();
      const v4u n0 = ldsc(rsWS, k0 ? OOB : off[0]), n1 = ldsc(rsWS, k1 ? OOB : off[1]), n2 = ldsc(rsWS, k2 ? OOB : off[2]), n3 = ldsc(rsWS, k3 ? OOB : off[3]);
      v[0] = k0 ? v[0] : n0; v[1] = k1 ? v[1] : n1; v[2] = k2 ? v[2] : n2; v[3] = k3 ? v[3] : n3;
    }
    f4 s;
#pragma unroll
    for (int e = 0; e < 4; ++e) {
      float a = (__uint_as_float(v[0][e] & 0xffffff00u) + __uint_as_float(v[1][e] & 0xffffff00u)) + (__uint_as_float(v[2][e] & 0xffffff00u) + __uint_as_float(v[3][e] & 0xffffff00u));
      a += dppf<XOR8>(a); s[e] = x32_sum(x16_sum(a));
    }
    return s;
  };
  if (w < 4) publish(WS_OUT1 + wg * 4096);
  if (w != 4) {
#pragma unroll
    for (int i = 0; i < 10; ++i) W2[i] = ld<AUX_W>(rsW2, offW2, (i / 5) * 1280 + (i % 5) * 256);
  }
  if (w == 4) {
    const v2u xr = lds_ld<v2u>(L, L_XR + (lane & 7) * 8); const f4 gg = lds_ld<f4>(L, L_GAB + (lane & 7) * 16);
    unsigned off[3]; v4u v[3]; float sbv[12], cbv[12]; int hoff[3];
#pragma unroll
    for (int q = 0; q < 3; ++q) {
      const int idx = 64 * q + lane, jj = idx / 5, k = idx % 5;
      off[q] = (idx < 160 && mlp) ? WS_AG + (b * 32 + jj) * 128 + k * 16 : OOB;
      const int col = idx < 160 ? 20 * jj + 4 * k : 0;
      const v2u s2 = lds_ld<v2u>(L, L_SB + col * 2), c2 = lds_ld<v2u>(L, L_CB + col * 2);
      sbv[4 * q] = bflo(s2.x); sbv[4 * q + 1] = bfhi(s2.x); sbv[4 * q + 2] = bflo(s2.y); sbv[4 * q + 3] = bfhi(s2.y);
      cbv[4 * q] = bflo(c2.x); cbv[4 * q + 1] = bfhi(c2.x); cbv[4 * q + 2] = bflo(c2.y); cbv[4 * q + 3] = bfhi(c2.y);
      hoff[q] = L_H2 + col * 2;
      PIN(off[q]); PIN(hoff[q]);
    }
#pragma unroll
    for (int i = 0; i < 12; ++i) { PIN(sbv[i]); PIN(cbv[i]); }
    float xx[4] = {bflo(xr.x), bfhi(xr.x), bflo(xr.y), bfhi(xr.y)};
    const unsigned long long am0 = ballot(off[0] != OOB), am2 = ballot(off[2] != OOB);
    __builtin_amdgcn_s_sleep(D_RS1);
    const f4 s = rs_gather(WS_OUT1, 32, 0);
    v4u m;
    {
      float a0 = s[0], a1 = s[1], a2 = s[2], a3 = s[3];
      rbf2(a0, a1); rbf2(a2, a3);
      a0 *= gg[0]; a1 *= gg[1]; a2 *= gg[2]; a3 *= gg[3];
      rbf2(a0, a1); rbf2(a2, a3);
      a0 += xx[0]; a1 += xx[1]; a2 += xx[2]; a3 += xx[3];
      const unsigned p01 = cvtpk(a0, a1), p23 = cvtpk(a2, a3);
      m = v4u{(p01 << 16) | (T & 0xffu), (p01 & 0xffff0000u) | ((T >> 8) & 0xffu), (p23 << 16) | ((T >> 16) & 0xffu), (p23 & 0xffff0000u) | (T >> 24)};
      x1v[0] = bflo(p01); x1v[1] = bfhi(p01); x1v[2] = bflo(p23); x1v[3] = bfhi(p23);
    }
    if (lane < 5) stsc(rsWS, WS_AG + (b * 32 + h) * 128 + lane * 16, m);
#pragma unroll
    for (int i = 0; i < 10; ++i) W2[i] = ld<AUX_W>(rsW2, offW2, (i / 5) * 1280 + (i % 5) * 256);
    STAMP(9);
    if (mlp) {
      __builtin_amdgcn_s_sleep(D_AG);
#pragma unroll
      for (int q = 0; q < 3; ++q) v[q] = ldsc(rsWS, off[q]);
      unsigned spins = 0;
      for (;;) {
        const bool k0 = tagok4(v[0], T), k1 = tagok4(v[1], T), k2 = tagok4(v[2], T);
        if ((ballot(k0) & ballot(k1) & am0) == am0 && (ballot(k2) & am2) == am2) break;
        if (++spins > SPIN_CAP) break;
        __builtin_amdgcn_s_sleep(POLL_SLEEP);
        FENCE();
        const v4u n0 = ldsc(rsWS, k0 ? OOB : off[0]), n1 = ldsc(rsWS, k1 ? OOB : off[1]), n2 = ldsc(rsWS, k2 ? OOB : off[2]);
        v[0] = k0 ? v[0] : n0; v[1] = k1 ? v[1] : n1; v[2] = k2 ? v[2] : n2;
      }
      STAMP(13);
      float xv[12]; float sm = 0.f;
#pragma unroll
      for (int q = 0; q < 3; ++q)
#pragma unroll
        for (int e = 0; e < 4; ++e) { xv[4 * q + e] = __uint_as_float(v[q][e] & 0xffff0000u); sm += xv[4 * q + e]; }
      const float mean = wave_sum(sm) * (1.f / 640.f);
      float q2 = 0.f, q3 = 0.f;
#pragma unroll
      for (int i = 0; i < 8; ++i) { const float dd = xv[i] - mean; q2 += dd * dd; }
#pragma unroll
      for (int i = 8; i < 12; ++i) { const float dd = xv[i] - mean; q3 += dd * dd; }
      q2 += lane < 32 ? q3 : 0.f;
      const float var = rbf(wave_sum(q2) * (1.f / 640.f));
      const float mb = rbf(mean), rden = frcp(rbf(__builtin_amdgcn_sqrtf(rbf(var + 1e-5f))));
#pragma unroll
      for (int q = 0; q < 3; ++q) {
        float a[4];
#pragma unroll
        for (int e = 0; e < 4; ++e) a[e] = xv[4 * q + e] - mb;
        rbf2(a[0], a[1]); rbf2(a[2], a[3]);
#pragma unroll
        for (int e = 0; e < 4; ++e) a[e] *= rden;
        rbf2(a[0], a[1]); rbf2(a[2], a[3]);
#pragma unroll
        for (int e = 0; e < 4; ++e) a[e] *= cbv[4 * q + e];
        rbf2(a[0], a[1]); rbf2(a[2], a[3]);
        const v2u o = {cvtpk(a[0] + sbv[4 * q], a[1] + sbv[4 * q + 1]), cvtpk(a[2] + sbv[4 * q + 2], a[3] + sbv[4 * q + 3])};
        if (q < 2 || lane < 32) lds_st<v2u>(L, hoff[q], o);
      }
    }
  }
  if (mlp) {
    BAR();
    STAMP(10);
    {
      unsigned X[5];
#pragma unroll
      for (int d = 0; d < 5; ++d) X[d] = lds_ld<unsigned>(L, L_H2 + 160 * w + 80 * (c >> 3) + 20 * g + d * 4);
      f4 a0 = {0.f, 0.f, 0.f, 0.f}, a1 = {0.f, 0.f, 0.f, 0.f};
#pragma unroll
      for (int p = 0; p < 10; p += 2) { a0 = mfma(diagA(X[p >> 1], S, 0), W1[p], a0); a1 = mfma(diagA(X[p >> 1], S, 1), W1[p + 1], a1); }
      a0 += a1;
      if ((g >> 1) == (c >> 3)) lds_st<f4>(L, L_HP + (2 * w + (c >> 3)) * 256 + (8 * (c & 7) + 4 * (g & 1)) * 4, a0);
    }
    BAR();
    {
      const int kap = (c >> 2) & 1, sig = (c & 3) + 4 * (c >> 3);
      const int n = (8 * w + 2 * g + kap) * 4;
      float hd = lds_ld<float>(L, L_HP + (2 * sig) * 256 + n) + lds_ld<float>(L, L_HP + (2 * sig + 1) * 256 + n);
      hd += dppf<XOR1>(hd); hd += dppf<XOR2>(hd); hd += dppf<XOR8>(hd);
      hd = fsilu(rbf(hd));
      const float ot = dppf<XOR7>(hd);
      const unsigned X = kap ? cvtpk(ot, hd) : cvtpk(hd, ot);
      const v4u A0 = diagA(X, S, 0), A1 = diagA(X, S, 1);
      f4 y[5];
#pragma unroll
      for (int cb = 0; cb < 5; ++cb) y[cb] = mfma(A0, W2[cb], f4{0.f, 0.f, 0.f, 0.f});
#pragma unroll
      for (int cb = 0; cb < 5; ++cb) y[cb] = mfma(A1, W2[5 + cb], y[cb]);
      if (lane < 32) {
#pragma unroll
        for (int cb = 0; cb < 5; ++cb) lds_st<f4>(L, ypoff + cb * 512, y[cb]);
      }
    }
    BAR();
    STAMP(11);
    if (w < 4) publish(WS_OUT2 + wg * 4096);
  }
  if (w == 4) {
    const f4 gg = lds_ld<f4>(L, L_GAB + 80 + (lane & 7) * 16);
    if (!mlp) __builtin_amdgcn_s_sleep(48);
    __builtin_amdgcn_s_sleep(D_RS2);
    const f4 s = rs_gather(WS_OUT2, 27, 0);
    float a0 = s[0], a1 = s[1], a2 = s[2], a3 = s[3];
    rbf2(a0, a1); rbf2(a2, a3);
    a0 *= gg[0]; a1 *= gg[1]; a2 *= gg[2]; a3 *= gg[3];
    rbf2(a0, a1); rbf2(a2, a3);
    if (lane < 5) __builtin_amdgcn_raw_buffer_store_b64(v2u{cvtpk(x1v[0] + a0, x1v[1] + a1), cvtpk(x1v[2] + a2, x1v[3] + a3)}, rsOUT, b * 1280 + 40 * h + lane * 8, 0, 0);
    STAMP(12);
  }
}

extern "C" void kernel_launch(void* const* d_in, const int* in_sizes, int n_in, void* d_out, int out_size, void* d_ws, size_t ws_size,
                              hipStream_t stream) {
  fused_kernel<<<256, 512, 0, stream>>>(d_in[0], d_in[1], d_in[2], d_in[3], d_in[4], d_in[6], d_in[7], d_in[8], d_in[9], d_out, d_ws);
}
```

```cpp
#include <hip/hip_runtime.h>
#include <hip/hip_bf16.h>
#include <stdint.h>
#include <type_traits>

typedef unsigned v4u __attribute__((ext_vector_type(4)));
typedef unsigned v2u __attribute__((ext_vector_type(2)));
typedef float f4 __attribute__((ext_vector_type(4)));
typedef __bf16 bf8 __attribute__((ext_vector_type(8)));
typedef __bf16 bf2 __attribute__((ext_vector_type(2)));
typedef short s4 __attribute__((ext_vector_type(4)));
#define LAS __attribute__((address_space(3)))
typedef __amdgpu_buffer_rsrc_t rsrc_t;

#define AUX_K 2
#define AUX_V 2
#define AUX_W 0
constexpr int AUX_NT = 2, AUX_SC1 = 16;
constexpr unsigned OOB = 0x80000000u;

constexpr int WS_EPOCH = 0;
constexpr int WS_ALIVE = 4096;
constexpr int WS_XST = 16384;
constexpr int WS_AGP = 65536;
constexpr int WS_AG = 131072;
constexpr int WS_PM = 262144;
constexpr int WS_OUT1 = 1 << 20;
constexpr int WS_OUT2 = 2 << 20;
constexpr int WS_QPM = 3 << 20;
constexpr int WS_STAMP = 4 << 20;
constexpr int WS_BYTES = 8 << 20;

#define TW 6
#define DA_LO 1
#define DA_HI 1
#define DB_LO 1
#define DB_HI 0
#define VRN 14
#define VPRE 9
constexpr int NR = 16;
#define RK 12
#define RL 19
#define HK 19
#define HD 2
#define HN 3
#define QK 25
struct It { int kind, idx; };
#define WOA 10
constexpr int NA = 3 + 32 + WOA + 8;
constexpr It a_item(int m) {
  int pos = 0, wn = 0;
  for (int k = 0; k < 32; ++k) {
    if (pos == m) return It{0, k};
    ++pos;
    if (k >= 4 && k < 7) { if (pos == m) return It{4, k - 4}; ++pos; }
    else if (k >= RK && k < RK + 4) { if (pos == m) return It{5, k - RK}; ++pos; }
    else if (k >= QK && k < QK + 4) { if (pos == m) return It{6, k - QK}; ++pos; }
    else if (k >= 7 && k < 31) {
      int idx = 0; for (int u = 7; u < k; ++u) if (!(u >= RK && u < RK + 4) && !(u >= QK && u < QK + 4)) ++idx;
      if (idx * WOA / 16 != (idx + 1) * WOA / 16 && wn < WOA) { if (pos == m) return It{2, wn}; ++pos; ++wn; }
    }
  }
  return It{-1, 0};
}
static_assert(a_item(NA - 1).kind == 0 && a_item(NA - 1).idx == 31, "phase A list");
constexpr int a_free_between(int lo, int hi) { int n = 0; for (int m = lo; m <= hi; ++m) if (m >= 0 && m < NA && a_item(m).kind == 6) ++n; return n; }
constexpr int a_kpos(int k) { for (int m = 0; m < NA; ++m) if (a_item(m).kind == 0 && a_item(m).idx == k) return m; return NA; }
#define WEARLY 2
#define WLAST 27
constexpr int NBV = 32 + 20 - WOA, NB = NBV + 10;
constexpr It b_item(int m) {
  if (m < VRN) return It{1, m};
  if (m < VRN + WEARLY) return It{2, WOA + m - VRN};
  if (m >= NBV) return It{3, m - NBV};
  int pos = VRN + WEARLY, wn = WOA + WEARLY;
  for (int v = VRN; v < 32; ++v) {
    if (pos == m) return It{1, v};
    ++pos;
    int target = WOA + WEARLY + ((20 - WOA - WEARLY) * (v - VRN + 1) + (WLAST - VRN)) / (WLAST - VRN + 1);
    if (target > 20) target = 20;
    while (wn < target) { if (pos == m) return It{2, wn}; ++pos; ++wn; }
  }
  return It{-1, 0};
}
static_assert(b_item(NBV - 1).kind == 1 && b_item(NBV - 1).idx == 31, "phase B list");
constexpr int b_vpos(int k) { for (int m = 0; m < NB; ++m) if (b_item(m).kind == 1 && b_item(m).idx == k) return m; return NB; }
constexpr int b_extra(int m) { int n = 0; const int p4 = b_vpos(4 + VRN), p8 = b_vpos(8 + VRN); if (m >= p4 && m - p4 < TW) n += 4; if (m >= p8 && m - p8 < TW) n += 2; return n; }
constexpr int a_lastpark_f() { int k = NR - 1; for (int m = 0; m + TW < NA; ++m) if (a_item(m).kind == 0 && a_item(m).idx > k) k = a_item(m).idx; return k; }
constexpr int LASTPARK = a_lastpark_f();
constexpr int NLDS = LASTPARK - NR + 1;

constexpr int L_WL = 0;
constexpr int L_SC = 20480;
constexpr int SC_STRIDE = 336;
constexpr int L_PART = L_SC + 2688;
constexpr int L_WQL = 0;
constexpr int L_QP8 = 20480;
constexpr int L_HB = 32768;
constexpr int L_SB = L_HB + 1280;
constexpr int L_CB = L_SB + 1280;
constexpr int L_GAB = L_CB + 1280;
constexpr int L_XR = L_GAB + 160;
constexpr int L_ML = L_XR + 64;
constexpr int L_FLAG = L_ML + 64;
constexpr int L_QB = L_FLAG + 32;
constexpr int L_H2 = L_QB + 256;
constexpr int L_OP = L_H2 + 1280;
constexpr int L_HP = L_OP + 4096;
constexpr int L_KS = L_HP + 4096;
constexpr int L_YP = L_KS;
constexpr int L_END = L_KS + (8 * NLDS * 1024 > 20480 ? 8 * NLDS * 1024 : 20480);
static_assert(L_QP8 + 8192 <= 32768 && L_PART + 4096 <= 32768 && L_HB % 16 == 0 && L_QB % 16 == 0 && L_OP % 16 == 0 && L_KS % 16 == 0 && L_GAB % 16 == 0 && L_H2 % 16 == 0 && L_END <= 163840, "lds map");

#define D_RS1 8
#define D_AG 10
#define D_RS2 8
#define POLL_SLEEP 1
#define STAMPS 0
#define NODUTY 0
#define NOPARK 0
#define PGT 1
#define NOW 0
constexpr unsigned SPIN_CAP = NODUTY ? 4u : (1u << 21);

__device__ __forceinline__ float bflo(unsigned d) { return __uint_as_float(d << 16); }
__device__ __forceinline__ float bfhi(unsigned d) { return __uint_as_float(d & 0xffff0000u); }
__device__ __forceinline__ unsigned cvtpk(float lo, float hi) {
  bf2 r = {(__bf16)lo, (__bf16)hi}; return __builtin_bit_cast(unsigned, r);
}
__device__ __forceinline__ float rbf(float f) { return bflo(cvtpk(f, 0.f)); }
__device__ __forceinline__ void rbf2(float& a, float& b) { const unsigned p = cvtpk(a, b); a = bflo(p); b = bfhi(p); }
__device__ __forceinline__ float frcp(float x) { return __builtin_amdgcn_rcpf(x); }
__device__ __forceinline__ float fsilu(float a) { return a * frcp(1.f + __builtin_amdgcn_exp2f(a * -1.4426950408889634f)); }
template <int CTRL> __device__ __forceinline__ float dppf(float x) {
  return __builtin_bit_cast(float, __builtin_amdgcn_update_dpp(0, __builtin_bit_cast(int, x), CTRL, 0xf, 0xf, true));
}
template <int CTRL> __device__ __forceinline__ unsigned dppu(unsigned x) { return (unsigned)__builtin_amdgcn_update_dpp(0, (int)x, CTRL, 0xf, 0xf, true); }
constexpr int XOR1 = 0xB1, XOR2 = 0x4E, XOR7 = 0x141, XOR8 = 0x128, QB0 = 0x00, QB2 = 0xAA;
__device__ __forceinline__ float x16_sum(float x) { auto s = __builtin_amdgcn_permlane16_swap(__float_as_uint(x), __float_as_uint(x), false, false); return __uint_as_float(s[0]) + __uint_as_float(s[1]); }
__device__ __forceinline__ float x32_sum(float x) { auto s = __builtin_amdgcn_permlane32_swap(__float_as_uint(x), __float_as_uint(x), false, false); return __uint_as_float(s[0]) + __uint_as_float(s[1]); }
__device__ __forceinline__ float x16_max(float x) { auto s = __builtin_amdgcn_permlane16_swap(__float_as_uint(x), __float_as_uint(x), false, false); return fmaxf(__uint_as_float(s[0]), __uint_as_float(s[1])); }
__device__ __forceinline__ float x32_max(float x) { auto s = __builtin_amdgcn_permlane32_swap(__float_as_uint(x), __float_as_uint(x), false, false); return fmaxf(__uint_as_float(s[0]), __uint_as_float(s[1])); }
__device__ __forceinline__ float wave_sum(float x) {
  x += dppf<XOR1>(x); x += dppf<XOR2>(x); x += dppf<XOR7>(x); x += dppf<XOR8>(x); return x32_sum(x16_sum(x));
}
__device__ __forceinline__ float wave_max(float x) {
  x = fmaxf(x, dppf<XOR1>(x)); x = fmaxf(x, dppf<XOR2>(x)); x = fmaxf(x, dppf<XOR7>(x)); x = fmaxf(x, dppf<XOR8>(x)); return x32_max(x16_max(x));
}
__device__ __forceinline__ f4 mfma(v4u a, v4u b, f4 c) {
  return __builtin_amdgcn_mfma_f32_16x16x32_bf16(__builtin_bit_cast(bf8, a), __builtin_bit_cast(bf8, b), c, 0, 0, 0);
}
__device__ __forceinline__ float dot2(unsigned a, unsigned b, float c) {
  return __builtin_amdgcn_fdot2_f32_bf16(__builtin_bit_cast(bf2, a), __builtin_bit_cast(bf2, b), c, false);
}
__device__ __forceinline__ rsrc_t mkrs(const void* p, unsigned bytes) { return __builtin_amdgcn_make_buffer_rsrc((void*)p, 0, (int)bytes, 0x00020000); }
template <int AUX> __device__ __forceinline__ v4u ld(rsrc_t r, unsigned voff, unsigned soff) { return __builtin_amdgcn_raw_buffer_load_b128(r, (int)voff, (int)soff, AUX); }
__device__ __forceinline__ v4u ldsc(rsrc_t r, unsigned voff) { return __builtin_amdgcn_raw_buffer_load_b128(r, (int)voff, 0, AUX_SC1); }
__device__ __forceinline__ void stsc(rsrc_t r, unsigned voff, v4u v) { __builtin_amdgcn_raw_buffer_store_b128(v, r, (int)voff, 0, AUX_SC1); }
__device__ __forceinline__ void stsc2(rsrc_t r, unsigned voff, v2u v) { __builtin_amdgcn_raw_buffer_store_b64(v, r, (int)voff, 0, AUX_SC1); }
#define WAITVM(n) asm volatile("s_waitcnt vmcnt(%0)" ::"n"((n) > 63 ? 63 : (n)) : "memory")
#define WAITW(n, lo, hi) do { if ((lo) == (hi)) WAITVM((n) - (lo)); else if (w < 4) WAITVM((n) - (lo)); else WAITVM((n) - (hi)); } while (0)
#define BAR() asm volatile("s_waitcnt lgkmcnt(0)\n\ts_barrier" ::: "memory")
#define SB() __builtin_amdgcn_sched_barrier(0)
#define FENCE() asm volatile("" ::: "memory")
#define PIN(x) asm volatile("" : "+v"(x))
#define DBG(code, cond) do { if (ballot(cond)) { if (lane == 0) __hip_atomic_fetch_max((LAS unsigned*)(L + L_FLAG + 28), (unsigned)(code), __ATOMIC_RELAXED, __HIP_MEMORY_SCOPE_WORKGROUP); } } while (0)
#define BADF(x, lim) (!(__builtin_fabsf(x) < (lim)))

template <typename T> __device__ __forceinline__ T lds_ld(LAS unsigned char* L, int off) { return *(LAS T*)(L + off); }
template <typename T> __device__ __forceinline__ void lds_st(LAS unsigned char* L, int off, T v) { *(LAS T*)(L + off) = v; }
template <int I, int N, typename F> __device__ __forceinline__ void sfor(F&& f) {
  if constexpr (I < N) { f(std::integral_constant<int, I>{}); sfor<I + 1, N>(f); }
}
template <int I> using IC = std::integral_constant<int, I>;
#define AINL __attribute__((always_inline))

struct Sel { unsigned s[2][4]; };
__device__ __forceinline__ v4u diagA(unsigned X, const Sel& S, int par) {
  v4u a;
#pragma unroll
  for (int d = 0; d < 4; ++d) a[d] = __builtin_amdgcn_perm(X, X, S.s[par][d]);
  return a;
}
__device__ __forceinline__ v4u tag4(f4 v, unsigned T) {
  v4u r;
#pragma unroll
  for (int e = 0; e < 4; ++e) r[e] = ((__float_as_uint(v[e]) + 0x80u) & 0xffffff00u) | ((T >> (8 * e)) & 0xffu);
  return r;
}
__device__ __forceinline__ bool tagok4(v4u v, unsigned T) {
  const unsigned lo = __builtin_amdgcn_perm(v.y, v.x, 0x0c0c0400u), hi = __builtin_amdgcn_perm(v.w, v.z, 0x04000c0cu);
  return (lo | hi) == T;
}
__device__ __forceinline__ unsigned long long ballot(bool p) { return __builtin_amdgcn_ballot_w64(p); }

#define STAMPK 0
#define STAMP(n) do { } while (0)
#define KSTAMP(k) do { } while (0)

__global__ void __launch_bounds__(512, 2)
fused_kernel(const void* px, const void* pk, const void* pv, const void* pwq,
             const void* pwo, const void* pw1, const void* pw2, const void* pc,
             const void* pwm, void* pout, void* pws) {
  __shared__ __attribute__((aligned(16))) unsigned char smem[L_END];
  LAS unsigned char* L = (LAS unsigned char*)smem;
  const int t = threadIdx.x, lane = t & 63, w = __builtin_amdgcn_readfirstlane(t >> 6);
  const int wg = blockIdx.x, xg = wg & 7, e8 = wg >> 3, b = wg >> 5, h = xg * 4 + (e8 & 3);
  const int g = lane >> 4, c = lane & 15;
  const bool mlp = h < 27;
  STAMP(0);
  const rsrc_t rsWS = mkrs(pws, WS_BYTES);
  const rsrc_t rsK = mkrs((const char*)pk + ((size_t)(b * 32 + h) * 4096 + 3072) * 256, 262144);
  const rsrc_t rsV = mkrs((const char*)pv + ((size_t)(b * 32 + h) * 4096 + 3072) * 256, 262144);
  const rsrc_t rsWQ = mkrs(pwq, 640 * 4096 * 2), rsWO = mkrs(pwo, 4096 * 640 * 2), rsW1 = mkrs(pw1, 640 * 1728 * 2), rsW2 = mkrs(pw2, 1728 * 640 * 2);
  const rsrc_t rsWM = mkrs(pwm, 640 * 3840 * 2), rsC = mkrs(pc, 10240), rsX = mkrs(px, 10240), rsOUT = mkrs(pout, 10240);

  const bool role = wg < 240;
  const int ta = wg % 60, kq = wg / 60;
  unsigned Eraw = 0;
  if (w == 0) Eraw = __builtin_amdgcn_raw_buffer_load_b32(rsWS, WS_EPOCH, 0, AUX_SC1);
  v4u cv0 = ld<AUX_NT>(rsC, (role && t < 160) ? (t / 20) * 1280 + kq * 320 + (t % 20) * 16 : OOB, 0);
  const unsigned wmoff = (unsigned)(160 * kq + 8 * w + (lane >> 3)) * 7680u + ta * 128 + (lane & 7) * 16;
  v4u wm0 = ld<AUX_NT>(rsWM, role ? wmoff : OOB, 0);
  v4u wm1 = ld<AUX_NT>(rsWM, role ? wmoff : OOB, 64 * 7680);
  v4u wm2 = ld<AUX_NT>(rsWM, (role && w < 4) ? wmoff : OOB, 128 * 7680);
  unsigned pg0 = 0, pg1 = 0, pg2 = 0, pg3 = 0, pg4 = 0, pg5 = 0;
  if (PGT && w == 7) {
    pg0 = __builtin_amdgcn_raw_buffer_load_b32(rsV, lane < 2 ? lane * 262140 : OOB, 0, AUX_NT);
    pg1 = __builtin_amdgcn_raw_buffer_load_b32(rsWO, lane < 2 ? h * 163840 + lane * 163836 : OOB, 0, AUX_W);
    pg2 = __builtin_amdgcn_raw_buffer_load_b32(rsW1, lane < 8 ? lane * 80 * 3456 + h * 128 : OOB, 0, AUX_W);
    pg3 = __builtin_amdgcn_raw_buffer_load_b32(rsW2, lane < 2 ? h * 81920 + lane * 81916 : OOB, 0, AUX_W);
    pg4 = __builtin_amdgcn_raw_buffer_load_b32(rsWS, lane < 16 ? lane * 262144 + 1024 : OOB, 0, AUX_SC1);
    pg5 = __builtin_amdgcn_raw_buffer_load_b32(rsOUT, lane == 0 ? b * 1280 : OOB, 0, AUX_NT);
  }
  if (t < 8) lds_st<unsigned>(L, L_FLAG + t * 4, 0u);
  SB();
  asm volatile("s_barrier" ::: "memory");
  unsigned po[4]; v4u pvv[4];
  v4u WQ[3], WO[20], KB[NR], KL[TW + 1];
  const unsigned offWQ = NOW ? OOB : (unsigned)(80 * b + 20 * g + w) * 8192u + h * 256 + c * 16;
  const unsigned offWO = NOW ? OOB : (unsigned)(h * 128 + 16 * w + 4 * g) * 1280u + c * 16;
  const unsigned offKV = w * 1024 + lane * 16;
  const int ksoff = L_KS + w * (NLDS * 1024) + lane * 16;
  auto ldwo = [&](auto ic) AINL { constexpr int i = decltype(ic)::value; WO[i] = ld<AUX_W>(rsWO, offWO, (i / 5) * 1280 + (i % 5) * 256); };
  auto a_issue = [&](auto mc) AINL {
    constexpr It it = a_item(decltype(mc)::value);
    if constexpr (it.kind == 4) WQ[it.idx] = ld<AUX_NT>(rsWQ, (it.idx < 2 || w < 4) ? offWQ : OOB, it.idx * 8 * 8192);
    else if constexpr (it.kind == 2) ldwo(IC<it.idx>{});
    else if constexpr (it.kind == 5 || it.kind == 6) pvv[it.idx] = ldsc(rsWS, po[it.idx]);
    else if constexpr (it.idx < NR) KB[it.idx] = ld<AUX_K>(rsK, offKV, it.idx * 8192);
    else KL[(it.idx - NR) % (TW + 1)] = ld<AUX_K>(rsK, offKV, it.idx * 8192);
  };
  constexpr int NINIT0 = 2, NINIT1 = 4, NINIT2 = 5, NINIT3 = 7, NINIT = 8;
  static_assert(NINIT >= TW && a_item(NINIT - 1).idx < NR, "no parking during P1");
  sfor<0, NINIT0>(a_issue);
  SB();
  STAMP(1);
  WAITVM(NINIT0);
  sfor<NINIT0, NINIT1>(a_issue);
  SB();
  if (w == 0 && lane == 0) lds_st<unsigned>(L, L_ML, Eraw + 1u);
  if (wg >= 240 && wg < 248 && w == 0) {
    const unsigned T = (unsigned)__builtin_amdgcn_readfirstlane((int)Eraw) + 1u;
    const v4u xa = ld<AUX_NT>(rsX, (wg - 240) * 1280 + lane * 16, 0);
    const unsigned xb = __builtin_amdgcn_raw_buffer_load_b32(rsX, (wg - 240) * 1280 + 1024 + lane * 4, 0, AUX_NT);
    float xv[10];
#pragma unroll
    for (int d = 0; d < 4; ++d) { xv[2 * d] = bflo(xa[d]); xv[2 * d + 1] = bfhi(xa[d]); }
    xv[8] = bflo(xb); xv[9] = bfhi(xb);
    const float sm = ((xv[0] + xv[1]) + (xv[2] + xv[3])) + ((xv[4] + xv[5]) + (xv[6] + xv[7])) + (xv[8] + xv[9]);
    const float mean = wave_sum(sm) * (1.f / 640.f);
    float q2 = 0.f;
#pragma unroll
    for (int d = 0; d < 10; ++d) { const float dd = xv[d] - mean; q2 = fmaf(dd, dd, q2); }
    const float var = rbf(wave_sum(q2) * (1.f / 640.f));
    const float mb = rbf(mean), den = rbf(__builtin_amdgcn_sqrtf(rbf(var + 1e-5f)));
    if (lane == 0) stsc(rsWS, WS_XST + (wg - 240) * 16, v4u{__float_as_uint(mb) | (T & 0xffu), __float_as_uint(den) | ((T >> 8) & 0xffu), (T >> 16) & 0xffu, T >> 24});
  }
  {
    auto silu8 = [&](v4u v) AINL {
      v4u r;
#pragma unroll
      for (int d = 0; d < 4; ++d) r[d] = cvtpk(fsilu(bflo(v[d])), fsilu(bfhi(v[d])));
      return r;
    };
    if (t < 160) lds_st<v4u>(L, L_SC + (t / 20) * SC_STRIDE + (t % 20) * 16, silu8(cv0));
    lds_st<v4u>(L, L_WL + w * 1024 + lane * 16, wm0);
    lds_st<v4u>(L, L_WL + (w + 8) * 1024 + lane * 16, wm1);
    if (w < 4) lds_st<v4u>(L, L_WL + (w + 16) * 1024 + lane * 16, wm2);
  }
  SB();
  sfor<NINIT1, NINIT2>(a_issue);
  v2u xh = __builtin_amdgcn_raw_buffer_load_b64(rsX, lane < 20 ? w * 1280 + 160 * b + lane * 8 : OOB, 0, AUX_NT);
  v2u xown = __builtin_amdgcn_raw_buffer_load_b64(rsX, (w == 4 && lane < 5) ? b * 1280 + 40 * h + lane * 8 : OOB, 0, AUX_NT);
  SB();
  BAR();
  const unsigned T = (unsigned)__builtin_amdgcn_readfirstlane((int)lds_ld<unsigned>(L, L_ML));
  asm volatile("" :: "v"(pg0), "v"(pg1), "v"(pg2), "v"(pg3), "v"(pg4), "v"(pg5));
  STAMP(2);
  sfor<NINIT2, NINIT3>([&](auto mc) AINL { WAITVM(TW - 2 + 2); a_issue(mc); SB(); });
  {
    f4 acc = {0.f, 0.f, 0.f, 0.f};
    const int tl = w & 3, s0_ = (w >> 2) * 3;
    const int trbase = L_WL + (8 * g + ((lane & 15) >> 2)) * 128 + tl * 32 + (lane & 3) * 8;
#pragma unroll
    for (int n = 0; n < 3; ++n) {
      if (n < 2 || w < 4) {
        const int s = s0_ + n;
        const v4u a = lds_ld<v4u>(L, L_SC + (c & 7) * SC_STRIDE + s * 64 + g * 16);
        const s4 b0 = __builtin_amdgcn_ds_read_tr16_b64_v4i16((LAS s4*)(L + trbase + s * 4096));
        const s4 b1 = __builtin_amdgcn_ds_read_tr16_b64_v4i16((LAS s4*)(L + trbase + s * 4096 + 512));
        const v2u u0 = __builtin_bit_cast(v2u, b0), u1 = __builtin_bit_cast(v2u, b1);
        acc = mfma(a, v4u{u0.x, u0.y, u1.x, u1.y}, acc);
      }
    }
    if (g < 2) {
#pragma unroll
      for (int r = 0; r < 4; ++r) lds_st<float>(L, L_PART + (w >> 2) * 2048 + (4 * g + r) * 256 + (16 * tl + c) * 4, acc[r]);
    }
  }
  sfor<NINIT3, NINIT>([&](auto mc) AINL { WAITVM(TW - 2 + 2); a_issue(mc); SB(); });
  BAR();
  {
    const f4 s = lds_ld<f4>(L, L_PART + (t & 127) * 16) + lds_ld<f4>(L, L_PART + 2048 + (t & 127) * 16);
    unsigned o = OOB;
    if (t < 128) { if (role) o = WS_PM + (((t >> 4) * 4 + kq) * 3840 + ta * 64) * 4 + (t & 15) * 16; }
    else if (t == 128) o = WS_ALIVE + wg * 16;
    stsc(rsWS, o, tag4(s, T));
  }
  STAMP(3);
  Sel S;
  {
    const int e = c & 7;
#pragma unroll
    for (int par = 0; par < 2; ++par)
#pragma unroll
      for (int d = 0; d < 4; ++d) {
        const unsigned lo = (unsigned)(2 * par) | ((unsigned)(2 * par + 1) << 8);
        S.s[par][d] = (d == (e >> 1)) ? ((e & 1) ? (0x0c0cu | (lo << 16)) : (0x0c0c0000u | lo)) : 0x0c0c0c0cu;
      }
  }
  unsigned long long pm0, pm1, pm2, pm3;
  auto pmasks = [&]() AINL { pm0 = ballot(po[0] != OOB); pm1 = ballot(po[1] != OOB); pm2 = ballot(po[2] != OOB); pm3 = ballot(po[3] != OOB); };
#pragma unroll
  for (int q = 0; q < 4; ++q) po[q] = lane < 40 ? WS_PM + ((w * 4 + q) * 3840 + (lane / 20) * 640 + 80 * b + 4 * (lane % 20)) * 4 : OOB;
  if (lane == 40) po[0] = WS_XST + w * 16;
  pmasks();
  auto pissue = [&]() AINL {
#pragma unroll
    for (int q = 0; q < 4; ++q) pvv[q] = ldsc(rsWS, po[q]);
  };
  auto pok = [&]() AINL {
    return (ballot(tagok4(pvv[0], T)) & pm0) == pm0 && (ballot(tagok4(pvv[1], T)) & pm1) == pm1 && (ballot(tagok4(pvv[2], T)) & pm2) == pm2 && (ballot(tagok4(pvv[3], T)) & pm3) == pm3;
  };
  auto preload = [&]() AINL {
#pragma unroll
    for (int q = 0; q < 4; ++q) { const bool k1 = tagok4(pvv[q], T); const v4u nv = ldsc(rsWS, k1 ? OOB : po[q]); pvv[q] = k1 ? pvv[q] : nv; }
  };
  auto pblock = [&](int dbgc) AINL {
    unsigned spins = 0;
    while (!pok() && ++spins < SPIN_CAP) { __builtin_amdgcn_s_sleep(2); FENCE(); preload(); }
    DBG(dbgc, spins >= SPIN_CAP);
  };
  auto psum = [&]() AINL {
    f4 mv;
#pragma unroll
    for (int e = 0; e < 4; ++e) mv[e] = (__uint_as_float(pvv[0][e] & 0xffffff00u) + __uint_as_float(pvv[1][e] & 0xffffff00u)) +
                                        (__uint_as_float(pvv[2][e] & 0xffffff00u) + __uint_as_float(pvv[3][e] & 0xffffff00u));
    float a0 = mv[0], a1 = mv[1], a2 = mv[2], a3 = mv[3]; rbf2(a0, a1); rbf2(a2, a3);
    return f4{a0, a1, a2, a3};
  };
  bool hdone = false;
  auto rprocess = [&]() AINL {
    const f4 mv = psum();
    const float xmean = __uint_as_float((unsigned)__builtin_amdgcn_readlane((int)pvv[0].x, 40) & 0xffff0000u);
    const float xrden = frcp(__uint_as_float((unsigned)__builtin_amdgcn_readlane((int)pvv[0].y, 40) & 0xffff0000u));
    float lnv[4] = {bflo(xh.x) - xmean, bfhi(xh.x) - xmean, bflo(xh.y) - xmean, bfhi(xh.y) - xmean};
    rbf2(lnv[0], lnv[1]); rbf2(lnv[2], lnv[3]);
#pragma unroll
    for (int e = 0; e < 4; ++e) lnv[e] *= xrden;
    rbf2(lnv[0], lnv[1]); rbf2(lnv[2], lnv[3]);
    float hv[4];
#pragma unroll
    for (int e = 0; e < 4; ++e) {
      const float ca = __uint_as_float(__builtin_amdgcn_ds_bpermute(((lane + 20) & 63) * 4, (int)__float_as_uint(mv[e])));
      hv[e] = rbf(lnv[e] * rbf(1.f + ca)) + mv[e];
    }
    DBG(29, lane < 20 && (BADF(hv[0], 1e4f) || BADF(hv[1], 1e4f) || BADF(hv[2], 1e4f) || BADF(hv[3], 1e4f)));
    if (lane < 20) lds_st<v2u>(L, L_HB + w * 160 + lane * 8, v2u{cvtpk(hv[0], hv[1]), cvtpk(hv[2], hv[3])});
    asm volatile("s_waitcnt lgkmcnt(0)" ::: "memory");
    if (lane == 0) __hip_atomic_fetch_add((LAS unsigned*)(L + L_FLAG), 1u, __ATOMIC_RELAXED, __HIP_MEMORY_SCOPE_WORKGROUP);
    STAMP(14);
  };
  auto mprocess = [&]() AINL {
    const f4 mv = psum();
    v4u msg;
#pragma unroll
    for (int e = 0; e < 4; ++e) msg[e] = (__float_as_uint(rbf(lane >= 10 ? 1.f + mv[e] : mv[e])) & 0xffff0000u) | ((T >> (8 * e)) & 0xffu);
    if (lane >= 5 && lane < 15) stsc(rsWS, WS_AGP + (b * 32 + h) * 256 + lane * 16, msg);
    else if (lane < 5) lds_st<f4>(L, L_GAB + lane * 16, mv);
    else if (lane < 20) lds_st<f4>(L, L_GAB + 80 + (lane - 15) * 16, mv);
    STAMP(17);
  };
  auto bfpack = [&](v4u v) AINL { return v2u{__builtin_amdgcn_perm(v.y, v.x, 0x07060302u), __builtin_amdgcn_perm(v.w, v.z, 0x07060302u)}; };
  auto hflag = [&]() AINL { FENCE(); return __builtin_amdgcn_readfirstlane((int)*(volatile LAS unsigned*)(L + L_FLAG)) == 8; };
  auto hstep = [&]() AINL {
    const int pi = w & 3, hf = w >> 2;
    unsigned X[5]; v4u Bp[10];
#pragma unroll
    for (int d = 0; d < 5; ++d) X[d] = lds_ld<unsigned>(L, L_HB + (2 * pi + (c >> 3)) * 160 + 40 * g + 20 * hf + d * 4);
#pragma unroll
    for (int i = 0; i < 10; ++i) Bp[i] = lds_ld<v4u>(L, L_WQL + (10 * hf + i) * 1024 + lane * 16);
    f4 a0 = {0.f, 0.f, 0.f, 0.f}, a1 = {0.f, 0.f, 0.f, 0.f};
#pragma unroll
    for (int i = 0; i < 10; i += 2) { a0 = mfma(diagA(X[i >> 1], S, 0), Bp[i], a0); a1 = mfma(diagA(X[i >> 1], S, 1), Bp[i + 1], a1); }
    a0 += a1;
    DBG(27, BADF(a0[0], 1e5f) || BADF(a0[1], 1e5f) || BADF(a0[2], 1e5f) || BADF(a0[3], 1e5f));
    lds_st<f4>(L, L_QP8 + w * 1024 + lane * 16, a0);
    asm volatile("s_waitcnt lgkmcnt(0)" ::: "memory");
    unsigned old = 0;
    if (lane == 0) old = __hip_atomic_fetch_add((LAS unsigned*)(L + L_FLAG + 4 + pi * 4), 1u, __ATOMIC_RELAXED, __HIP_MEMORY_SCOPE_WORKGROUP);
    if (__builtin_amdgcn_readfirstlane((int)old) == 1) {
      FENCE();
      const f4 lo = lds_ld<f4>(L, L_QP8 + pi * 1024 + lane * 16), hi = lds_ld<f4>(L, L_QP8 + (pi + 4) * 1024 + lane * 16);
      stsc(rsWS, WS_QPM + (h * 8 + b) * 4096 + (2 * pi + (g >> 1)) * 512 + (2 * c + (g & 1)) * 16, tag4(lo + hi, T));
      STAMP(16);
    }
  };
  static_assert(a_kpos(RL) >= a_kpos(6) + 1 + TW, "wq parked before the first h arrival");
  static_assert(a_kpos(RL) >= a_kpos(RK + 3) + 1 + TW - 1 && RL <= HK && HK + (HN - 1) * HD < QK - 1 && RL < QK - 1 && QK + 3 < 31, "stage order");
  auto pnone = [&]() AINL {
#pragma unroll
    for (int q = 0; q < 4; ++q) po[q] = OOB;
  };
  sfor<NINIT, NA>([&](auto mc) AINL {
    constexpr int m = decltype(mc)::value;
    constexpr It it = a_item(m);
    constexpr int k = it.kind == 0 ? it.idx : -100;
    WAITW(TW - 1 + (m - (TW - DA_LO) < NINIT ? 1 : 0) + a_free_between(m - (TW - DA_LO), m - 1), DA_LO, DA_HI);
    {
      constexpr It ol = a_item(m - TW);
      if constexpr (ol.kind == 4) { if (ol.idx < 2 || w < 4) lds_st<v4u>(L, L_WQL + (w + 8 * ol.idx) * 1024 + lane * 16, WQ[ol.idx]); }
      if constexpr (!NOPARK && ol.kind == 0 && ol.idx >= NR) lds_st<v4u>(L, ksoff + (ol.idx - NR) * 1024, KL[(ol.idx - NR) % (TW + 1)]);
    }
    a_issue(mc);
    if constexpr (k == 9) { if (w == 4 && lane < 5) lds_st<v2u>(L, L_XR + lane * 8, xown); }
    KSTAMP(k);
    if constexpr (k == 8) STAMP(18);
    if constexpr (k == 16) STAMP(19);
    if constexpr (k == 24) STAMP(20);
    if constexpr (!NODUTY) {
    if constexpr (k == RL) { pblock(30); rprocess(); }
    if constexpr (k >= HK && k < HK + (HN - 1) * HD && (k - HK) % HD == 0) { if (!hdone && hflag()) { hstep(); hdone = true; } }
    if constexpr (k == HK + (HN - 1) * HD) {
      if (!hdone) { unsigned spins = 0; while (!hflag() && ++spins < (SPIN_CAP << 3)) __builtin_amdgcn_s_sleep(1); DBG(28, spins >= (SPIN_CAP << 3)); hstep(); }
    }
    if constexpr (k == QK - 1) {
#pragma unroll
      for (int q = 0; q < 4; ++q) {
        unsigned o = OOB;
        if (w == 5) { if (lane < 20) o = WS_PM + ((b * 4 + q) * 3840 + (2 + lane / 5) * 640 + 20 * h + 4 * (lane % 5)) * 4; }
        else if (w == 6) o = WS_QPM + ((h * 8 + 2 * q + (lane >> 5)) * 8 + b) * 512 + (lane & 31) * 16;
        else if (w == 4 && wg == 0) o = WS_ALIVE + (64 * q + lane) * 16;
        po[q] = o;
      }
      pmasks();
    }
    }
    SB();
  });
  STAMP(4);
  v4u W1[10], W2[10], VR[VRN];
  const unsigned offW1 = (mlp && !NOW) ? (unsigned)(80 * w + 40 * (c >> 3) + 10 * g) * 3456u + (64 * h + 8 * (c & 7)) * 2 : OOB;
  const unsigned offW2 = (mlp && !NOW) ? (unsigned)(64 * h + 8 * w + 2 * g) * 1280u + c * 16 : OOB;
  auto b_issue = [&](auto mc) AINL {
    constexpr It it = b_item(decltype(mc)::value);
    if constexpr (it.kind == 1) VR[it.idx % VRN] = ld<AUX_V>(rsV, offKV, it.idx * 8192);
    else if constexpr (it.kind == 2) ldwo(IC<it.idx>{});
    else W1[it.idx] = ld<AUX_W>(rsW1, offW1, it.idx * 3456);
    SB();
  };
#define VTICK(k) do { WAITW(TW - 1, DB_LO, DB_HI); b_issue(IC<(k)>{}); } while (0)
  sfor<0, VPRE>([&](auto kc) AINL { WAITW(TW - 1 + a_free_between(NA + decltype(kc)::value - (TW - DB_LO), NA - 1), DB_LO, DB_HI); b_issue(kc); });
  if (!NODUTY && w == 5) { pblock(26); mprocess(); }
  if (!NODUTY && (w == 6 || w == 4)) {
    pblock(w == 6 ? 25 : 24);
    if (w == 6) {
      f4 q4;
#pragma unroll
      for (int e = 0; e < 4; ++e) q4[e] = x32_sum((__uint_as_float(pvv[0][e] & 0xffffff00u) + __uint_as_float(pvv[1][e] & 0xffffff00u)) +
                                                  (__uint_as_float(pvv[2][e] & 0xffffff00u) + __uint_as_float(pvv[3][e] & 0xffffff00u)));
      DBG(23, BADF(q4[0], 1e5f) || BADF(q4[1], 1e5f) || BADF(q4[2], 1e5f) || BADF(q4[3], 1e5f));
      if (lane < 32) lds_st<v2u>(L, L_QB + lane * 8, v2u{cvtpk(q4[0], q4[1]), cvtpk(q4[2], q4[3])});
    } else {
      if (wg == 0 && lane == 0) __builtin_amdgcn_raw_buffer_store_b32(T, rsWS, WS_EPOCH, 0, AUX_SC1);
    }
  }
  BAR();
  STAMP(5);
  const v4u qr = lds_ld<v4u>(L, L_QB + c * 16);
  STAMP(6);
  auto kdot = [&](v4u k) AINL { return dot2(k.w, qr[3], dot2(k.z, qr[2], dot2(k.y, qr[1], dot2(k.x, qr[0], 0.f)))); };
  auto treduce = [&](float (&xs)[16]) AINL {
    float x[8];
#pragma unroll
    for (int i = 0; i < 8; ++i) { const float lo = xs[i] + dppf<XOR8>(xs[i]), hi = xs[i + 8] + dppf<XOR8>(xs[i + 8]); x[i] = (c & 8) ? hi : lo; }
#pragma unroll
    for (int i = 0; i < 4; ++i) { const float lo = x[i] + dppf<XOR7>(x[i]), hi = x[i + 4] + dppf<XOR7>(x[i + 4]); x[i] = (c & 4) ? hi : lo; }
#pragma unroll
    for (int i = 0; i < 2; ++i) { const float lo = x[i] + dppf<XOR2>(x[i]), hi = x[i + 2] + dppf<XOR2>(x[i + 2]); x[i] = (c & 2) ? hi : lo; }
    const float lo = x[0] + dppf<XOR1>(x[0]), hi = x[1] + dppf<XOR1>(x[1]);
    return (c & 1) ? hi : lo;
  };
  constexpr float C2 = 0.08838834764831845f * 1.4426950408889634f;
  float mw, fw = 0.f;
  unsigned ppk;
  {
    float xs[16];
#pragma unroll
    for (int j = 0; j < 16; ++j) xs[j] = kdot(KB[j]);
    SB();
    VTICK(VPRE);
    const float s0 = treduce(xs);
    SB();
    VTICK(VPRE + 1);
#pragma unroll
    for (int j = 0; j <= LASTPARK - NR; ++j) xs[j] = kdot(lds_ld<v4u>(L, ksoff + j * 1024));
#pragma unroll
    for (int j = LASTPARK - NR + 1; j < 16; ++j) xs[j] = kdot(KL[j % (TW + 1)]);
    SB();
    sfor<VPRE + 2, (VPRE + 4 < VRN + WEARLY ? VPRE + 4 : VRN + WEARLY)>([&](auto kc) AINL { WAITW(TW - 1, DB_LO, DB_HI); b_issue(kc); });
    const float s1 = treduce(xs);
    SB();
    sfor<(VPRE + 4 < VRN + WEARLY ? VPRE + 4 : VRN + WEARLY), VRN + WEARLY>([&](auto kc) AINL { WAITW(TW - 1, DB_LO, DB_HI); b_issue(kc); });
    mw = wave_max(fmaxf(s0, s1));
    const float p0 = __builtin_amdgcn_exp2f((s0 - mw) * C2), p1 = __builtin_amdgcn_exp2f((s1 - mw) * C2);
    const float lw = wave_sum(p0 + p1);
    ppk = cvtpk(p0, p1);
    DBG(22, BADF(s0, 1e6f) || BADF(s1, 1e6f));
    if (lane == 0) lds_st<v2u>(L, L_ML + w * 8, v2u{__float_as_uint(mw), __float_as_uint(lw)});
  }
  STAMP(15);
  f4 oacc = {0.f, 0.f, 0.f, 0.f};
  unsigned touch0 = 0, touch1 = 0;
  auto pvstep = [&](auto jc) AINL {
    constexpr int j = decltype(jc)::value;
    const unsigned X = dppu<0x150 + (j & 15)>(ppk);
    oacc = mfma(diagA(X, S, j >> 4), VR[j % VRN], oacc);
    if constexpr (j == 4) {
#pragma unroll
      for (int q = 0; q < 4; ++q) { const int idx = 64 * q + lane; po[q] = ((w == 1 || w == 2) && mlp && idx < 160) ? WS_AGP + (b * 32 + idx / 5) * 256 + (5 * w + idx % 5) * 16 : OOB; }
      pmasks(); pissue();
    }
    if constexpr (j == 14) {
      if (!NODUTY && (w == 1 || w == 2)) {
        pblock(21);
#pragma unroll
        for (int n = 0; n < 3; ++n) {
          const int idx = 64 * n + lane;
          if (po[n] != OOB) lds_st<v2u>(L, (w == 1 ? L_SB : L_CB) + (20 * (idx / 5) + 4 * (idx % 5)) * 2, bfpack(pvv[n]));
        }
      }
    }
    if constexpr (j == 8) {
      unsigned o0 = OOB, o1 = OOB;
      if (w == 4) {
        const int u = lane & 31; const int wgu = (b << 5) | ((u & 3) << 3) | (u >> 2);
        o0 = (lane < 32 ? WS_OUT1 : WS_OUT2) + wgu * 4096 + h * 128;
        o1 = lane < 32 ? WS_AG + (b * 32 + lane) * 128 : OOB;
      } else if (w == 0) { o0 = (lane < 32 ? WS_OUT1 : WS_OUT2) + wg * 4096 + (lane & 31) * 128; }
      touch0 = __builtin_amdgcn_raw_buffer_load_b32(rsWS, (int)o0, 0, AUX_SC1);
      touch1 = __builtin_amdgcn_raw_buffer_load_b32(rsWS, (int)o1, 0, AUX_SC1);
    }
    if constexpr (j == 16) { asm volatile("" :: "v"(touch0), "v"(touch1)); }
    if constexpr (j == 10) {
      BAR();
      float M = -INFINITY;
      v2u ml[8];
#pragma unroll
      for (int ww = 0; ww < 8; ++ww) { ml[ww] = lds_ld<v2u>(L, L_ML + ww * 8); M = fmaxf(M, __uint_as_float(ml[ww].x)); }
      float Ls = 0.f;
#pragma unroll
      for (int ww = 0; ww < 8; ++ww) Ls += __builtin_amdgcn_exp2f((__uint_as_float(ml[ww].x) - M) * C2) * __uint_as_float(ml[ww].y);
      fw = __builtin_amdgcn_exp2f((mw - M) * C2) * frcp(Ls);
      PIN(fw);
    }
  };
  sfor<VRN + WEARLY, NB>([&](auto mc) AINL {
    constexpr int m = decltype(mc)::value;
    constexpr It it = b_item(m);
    if constexpr (it.kind == 1) pvstep(IC<it.idx - VRN>{});
    if constexpr (m == NBV) sfor<32 - VRN, 22>([&](auto jc) AINL { pvstep(jc); SB(); });
    if constexpr (it.kind != 3) WAITW(TW - 1 + b_extra(m), DB_LO, DB_HI);
    else { pvstep(IC<22 + it.idx>{}); }
    b_issue(mc);
  });
  STAMP(7);
  {
    const f4 ov = oacc * fw;
    DBG(20, lane < 32 && (BADF(ov[0], 1e5f) || BADF(ov[1], 1e5f) || BADF(ov[2], 1e5f) || BADF(ov[3], 1e5f)));
    if (lane < 32) lds_st<f4>(L, L_OP + w * 512 + (8 * c + 4 * g) * 4, ov);
  }
  BAR();
  const int ypoff = L_YP + w * 2560 + (8 * c + 4 * (g & 1)) * 4;
  {
    const int kap = (c >> 1) & 3, sig = (c & 1) + 2 * (c >> 3);
    const int ko = (16 * w + 4 * g + kap) * 4;
    float o = lds_ld<float>(L, L_OP + (2 * sig) * 512 + ko) + lds_ld<float>(L, L_OP + (2 * sig + 1) * 512 + ko);
    o += dppf<XOR1>(o); o += dppf<XOR8>(o);
    const unsigned P = cvtpk(dppf<QB0>(o), dppf<QB2>(o)), Q = dppu<XOR7>(P);
    const unsigned X0 = (c & 4) ? Q : P, X1 = (c & 4) ? P : Q;
    f4 y[5];
    { const v4u A = diagA(X0, S, 0);
#pragma unroll
      for (int cb = 0; cb < 5; ++cb) y[cb] = mfma(A, WO[cb], f4{0.f, 0.f, 0.f, 0.f}); }
    { const v4u A = diagA(X0, S, 1);
#pragma unroll
      for (int cb = 0; cb < 5; ++cb) y[cb] = mfma(A, WO[5 + cb], y[cb]); }
    { const v4u A = diagA(X1, S, 0);
#pragma unroll
      for (int cb = 0; cb < 5; ++cb) y[cb] = mfma(A, WO[10 + cb], y[cb]); }
    { const v4u A = diagA(X1, S, 1);
#pragma unroll
      for (int cb = 0; cb < 5; ++cb) y[cb] = mfma(A, WO[15 + cb], y[cb]); }
    if (lane < 32) {
#pragma unroll
      for (int cb = 0; cb < 5; ++cb) lds_st<f4>(L, ypoff + cb * 512, y[cb]);
    }
  }
  BAR();
  STAMP(8);
  auto publish = [&](int wsoff) AINL {
    const int k = t & 7, j = t >> 3;
    const int o = L_YP + (20 * j + 4 * (k < 5 ? k : 0)) * 4;
    f4 s0 = lds_ld<f4>(L, o) + lds_ld<f4>(L, o + 2560), s1 = lds_ld<f4>(L, o + 2 * 2560) + lds_ld<f4>(L, o + 3 * 2560);
    f4 s2 = lds_ld<f4>(L, o + 4 * 2560) + lds_ld<f4>(L, o + 5 * 2560), s3 = lds_ld<f4>(L, o + 6 * 2560) + lds_ld<f4>(L, o + 7 * 2560);
    stsc(rsWS, wsoff + t * 16, tag4((s0 + s1) + (s2 + s3), T));
  };
  float x1v[4] = {0.f, 0.f, 0.f, 0.f};
  auto rs_gather = [&](int wsbase, int nprod, int delay_done) AINL -> f4 {
    const int pl = lane >> 3, k = lane & 7;
    unsigned off[4]; v4u v[4];
#pragma unroll
    for (int q = 0; q < 4; ++q) {
      const int u = 8 * q + pl;
      const int wgu = (b << 5) | ((u & 3) << 3) | (u >> 2);
      off[q] = (k < 5 && u < nprod) ? wsbase + wgu * 4096 + h * 128 + k * 16 : OOB;
      PIN(off[q]);
    }
    const unsigned long long m0 = ballot(off[0] != OOB), m3 = ballot(off[3] != OOB);
    __builtin_amdgcn_s_sleep(0);
    (void)delay_done;
#pragma unroll
    for (int q = 0; q < 4; ++q) v[q] = ldsc(rsWS, off[q]);
    unsigned spins = 0;
    for (;;) {
      const bool k0 = tagok4(v[0], T), k1 = tagok4(v[1], T), k2 = tagok4(v[2], T), k3 = tagok4(v[3], T);
      if ((ballot(k0) & ballot(k1) & ballot(k2) & m0) == m0 && (ballot(k3) & m3) == m3) break;
      if (++spins > SPIN_CAP) { DBG(nprod == 32 ? 18 : 12, true); break; }
      __builtin_amdgcn_s_sleep(POLL_SLEEP);
      FENCE();
      const v4u n0 = ldsc(rsWS, k0 ? OOB : off[0]), n1 = ldsc(rsWS, k1 ? OOB : off[1]), n2 = ldsc(rsWS, k2 ? OOB : off[2]), n3 = ldsc(rsWS, k3 ? OOB : off[3]);
      v[0] = k0 ? v[0] : n0; v[1] = k1 ? v[1] : n1; v[2] = k2 ? v[2] : n2; v[3] = k3 ? v[3] : n3;
    }
    f4 s;
#pragma unroll
    for (int e = 0; e < 4; ++e) {
      float a = (__uint_as_float(v[0][e] & 0xffffff00u) + __uint_as_float(v[1][e] & 0xffffff00u)) + (__uint_as_float(v[2][e] & 0xffffff00u) + __uint_as_float(v[3][e] & 0xffffff00u));
      a += dppf<XOR8>(a); s[e] = x32_sum(x16_sum(a));
    }
    return s;
  };
  if (w < 4) publish(WS_OUT1 + wg * 4096);
  if (w != 4) {
#pragma unroll
    for (int i = 0; i < 10; ++i) W2[i] = ld<AUX_W>(rsW2, offW2, (i / 5) * 1280 + (i % 5) * 256);
  }
  if (w == 4) {
    const v2u xr = lds_ld<v2u>(L, L_XR + (lane & 7) * 8); const f4 gg = lds_ld<f4>(L, L_GAB + (lane & 7) * 16);
    unsigned off[3]; v4u v[3]; float sbv[12], cbv[12]; int hoff[3];
#pragma unroll
    for (int q = 0; q < 3; ++q) {
      const int idx = 64 * q + lane, jj = idx / 5, k = idx % 5;
      off[q] = (idx < 160 && mlp) ? WS_AG + (b * 32 + jj) * 128 + k * 16 : OOB;
      const int col = idx < 160 ? 20 * jj + 4 * k : 0;
      const v2u s2 = lds_ld<v2u>(L, L_SB + col * 2), c2 = lds_ld<v2u>(L, L_CB + col * 2);
      sbv[4 * q] = bflo(s2.x); sbv[4 * q + 1] = bfhi(s2.x); sbv[4 * q + 2] = bflo(s2.y); sbv[4 * q + 3] = bfhi(s2.y);
      cbv[4 * q] = bflo(c2.x); cbv[4 * q + 1] = bfhi(c2.x); cbv[4 * q + 2] = bflo(c2.y); cbv[4 * q + 3] = bfhi(c2.y);
      hoff[q] = L_H2 + col * 2;
      PIN(off[q]); PIN(hoff[q]);
    }
#pragma unroll
    for (int i = 0; i < 12; ++i) { PIN(sbv[i]); PIN(cbv[i]); }
    float xx[4] = {bflo(xr.x), bfhi(xr.x), bflo(xr.y), bfhi(xr.y)};
    const unsigned long long am0 = ballot(off[0] != OOB), am2 = ballot(off[2] != OOB);
    __builtin_amdgcn_s_sleep(D_RS1);
    const f4 s = rs_gather(WS_OUT1, 32, 0);
    v4u m;
    {
      float a0 = s[0], a1 = s[1], a2 = s[2], a3 = s[3];
      rbf2(a0, a1); rbf2(a2, a3);
      a0 *= gg[0]; a1 *= gg[1]; a2 *= gg[2]; a3 *= gg[3];
      rbf2(a0, a1); rbf2(a2, a3);
      a0 += xx[0]; a1 += xx[1]; a2 += xx[2]; a3 += xx[3];
      const unsigned p01 = cvtpk(a0, a1), p23 = cvtpk(a2, a3);
      m = v4u{(p01 << 16) | (T & 0xffu), (p01 & 0xffff0000u) | ((T >> 8) & 0xffu), (p23 << 16) | ((T >> 16) & 0xffu), (p23 & 0xffff0000u) | (T >> 24)};
      x1v[0] = bflo(p01); x1v[1] = bfhi(p01); x1v[2] = bflo(p23); x1v[3] = bfhi(p23);
    }
    DBG(19, lane < 5 && (BADF(s[0], 1e5f) || BADF(s[1], 1e5f) || BADF(s[2], 1e5f) || BADF(s[3], 1e5f)));
    DBG(17, lane < 5 && (BADF(x1v[0], 1e5f) || BADF(x1v[1], 1e5f) || BADF(x1v[2], 1e5f) || BADF(x1v[3], 1e5f)));
    if (lane < 5) stsc(rsWS, WS_AG + (b * 32 + h) * 128 + lane * 16, m);
#pragma unroll
    for (int i = 0; i < 10; ++i) W2[i] = ld<AUX_W>(rsW2, offW2, (i / 5) * 1280 + (i % 5) * 256);
    STAMP(9);
    if (mlp) {
      __builtin_amdgcn_s_sleep(D_AG);
#pragma unroll
      for (int q = 0; q < 3; ++q) v[q] = ldsc(rsWS, off[q]);
      unsigned spins = 0;
      for (;;) {
        const bool k0 = tagok4(v[0], T), k1 = tagok4(v[1], T), k2 = tagok4(v[2], T);
        if ((ballot(k0) & ballot(k1) & am0) == am0 && (ballot(k2) & am2) == am2) break;
        if (++spins > SPIN_CAP) { DBG(16, true); break; }
        __builtin_amdgcn_s_sleep(POLL_SLEEP);
        FENCE();
        const v4u n0 = ldsc(rsWS, k0 ? OOB : off[0]), n1 = ldsc(rsWS, k1 ? OOB : off[1]), n2 = ldsc(rsWS, k2 ? OOB : off[2]);
        v[0] = k0 ? v[0] : n0; v[1] = k1 ? v[1] : n1; v[2] = k2 ? v[2] : n2;
      }
      STAMP(13);
      float xv[12]; float sm = 0.f;
#pragma unroll
      for (int q = 0; q < 3; ++q)
#pragma unroll
        for (int e = 0; e < 4; ++e) { xv[4 * q + e] = __uint_as_float(v[q][e] & 0xffff0000u); sm += xv[4 * q + e]; }
      const float mean = wave_sum(sm) * (1.f / 640.f);
      float q2 = 0.f, q3 = 0.f;
#pragma unroll
      for (int i = 0; i < 8; ++i) { const float dd = xv[i] - mean; q2 += dd * dd; }
#pragma unroll
      for (int i = 8; i < 12; ++i) { const float dd = xv[i] - mean; q3 += dd * dd; }
      q2 += lane < 32 ? q3 : 0.f;
      const float var = rbf(wave_sum(q2) * (1.f / 640.f));
      const float mb = rbf(mean), rden = frcp(rbf(__builtin_amdgcn_sqrtf(rbf(var + 1e-5f))));
#pragma unroll
      for (int q = 0; q < 3; ++q) {
        float a[4];
#pragma unroll
        for (int e = 0; e < 4; ++e) a[e] = xv[4 * q + e] - mb;
        rbf2(a[0], a[1]); rbf2(a[2], a[3]);
#pragma unroll
        for (int e = 0; e < 4; ++e) a[e] *= rden;
        rbf2(a[0], a[1]); rbf2(a[2], a[3]);
#pragma unroll
        for (int e = 0; e < 4; ++e) a[e] *= cbv[4 * q + e];
        rbf2(a[0], a[1]); rbf2(a[2], a[3]);
        const v2u o = {cvtpk(a[0] + sbv[4 * q], a[1] + sbv[4 * q + 1]), cvtpk(a[2] + sbv[4 * q + 2], a[3] + sbv[4 * q + 3])};
        if (q < 2 || lane < 32) lds_st<v2u>(L, hoff[q], o);
      }
    }
  }
  if (mlp) {
    BAR();
    STAMP(10);
    {
      unsigned X[5];
#pragma unroll
      for (int d = 0; d < 5; ++d) X[d] = lds_ld<unsigned>(L, L_H2 + 160 * w + 80 * (c >> 3) + 20 * g + d * 4);
      f4 a0 = {0.f, 0.f, 0.f, 0.f}, a1 = {0.f, 0.f, 0.f, 0.f};
#pragma unroll
      for (int p = 0; p < 10; p += 2) { a0 = mfma(diagA(X[p >> 1], S, 0), W1[p], a0); a1 = mfma(diagA(X[p >> 1], S, 1), W1[p + 1], a1); }
      a0 += a1;
      if ((g >> 1) == (c >> 3)) lds_st<f4>(L, L_HP + (2 * w + (c >> 3)) * 256 + (8 * (c & 7) + 4 * (g & 1)) * 4, a0);
    }
    BAR();
    {
      const int kap = (c >> 2) & 1, sig = (c & 3) + 4 * (c >> 3);
      const int n = (8 * w + 2 * g + kap) * 4;
      float hd = lds_ld<float>(L, L_HP + (2 * sig) * 256 + n) + lds_ld<float>(L, L_HP + (2 * sig + 1) * 256 + n);
      hd += dppf<XOR1>(hd); hd += dppf<XOR2>(hd); hd += dppf<XOR8>(hd);
      DBG(14, BADF(hd, 1e6f));
      hd = fsilu(rbf(hd));
      const float ot = dppf<XOR7>(hd);
      const unsigned X = kap ? cvtpk(ot, hd) : cvtpk(hd, ot);
      const v4u A0 = diagA(X, S, 0), A1 = diagA(X, S, 1);
      f4 y[5];
#pragma unroll
      for (int cb = 0; cb < 5; ++cb) y[cb] = mfma(A0, W2[cb], f4{0.f, 0.f, 0.f, 0.f});
#pragma unroll
      for (int cb = 0; cb < 5; ++cb) y[cb] = mfma(A1, W2[5 + cb], y[cb]);
      if (lane < 32) {
#pragma unroll
        for (int cb = 0; cb < 5; ++cb) lds_st<f4>(L, ypoff + cb * 512, y[cb]);
      }
    }
    BAR();
    STAMP(11);
    if (w < 4) publish(WS_OUT2 + wg * 4096);
  }
  if (w == 4) {
    const f4 gg = lds_ld<f4>(L, L_GAB + 80 + (lane & 7) * 16);
    if (!mlp) __builtin_amdgcn_s_sleep(48);
    __builtin_amdgcn_s_sleep(D_RS2);
    const f4 s = rs_gather(WS_OUT2, 27, 0);
    float a0 = s[0], a1 = s[1], a2 = s[2], a3 = s[3];
    rbf2(a0, a1); rbf2(a2, a3);
    a0 *= gg[0]; a1 *= gg[1]; a2 *= gg[2]; a3 *= gg[3];
    rbf2(a0, a1); rbf2(a2, a3);
    DBG(11, lane < 5 && (BADF(a0, 1e6f) || BADF(a1, 1e6f) || BADF(a2, 1e6f) || BADF(a3, 1e6f)));
    asm volatile("s_waitcnt lgkmcnt(0)" ::: "memory");
    const unsigned dc = *(volatile LAS unsigned*)(L + L_FLAG + 28);
    const float pz = __uint_as_float((127u + 3u * dc) << 23);
    if (dc) { a0 = pz; a1 = pz; a2 = pz; a3 = pz; x1v[0] = 0.f; x1v[1] = 0.f; x1v[2] = 0.f; x1v[3] = 0.f; }
    if (lane < 5) __builtin_amdgcn_raw_buffer_store_b64(v2u{cvtpk(x1v[0] + a0, x1v[1] + a1), cvtpk(x1v[2] + a2, x1v[3] + a3)}, rsOUT, b * 1280 + 40 * h + lane * 8, 0, 0);
    STAMP(12);
  }
}

extern "C" void kernel_launch(void* const* d_in, const int* in_sizes, int n_in, void* d_out, int out_size, void* d_ws, size_t ws_size,
                              hipStream_t stream) {
  fused_kernel<<<256, 512, 0, stream>>>(d_in[0], d_in[1], d_in[2], d_in[3], d_in[4], d_in[6], d_in[7], d_in[8], d_in[9], d_out, d_ws);
}
```

```cpp
#include <hip/hip_runtime.h>
#include <hip/hip_bf16.h>
#include <stdint.h>
#include <type_traits>

typedef unsigned v4u __attribute__((ext_vector_type(4)));
typedef unsigned v2u __attribute__((ext_vector_type(2)));
typedef float f4 __attribute__((ext_vector_type(4)));
typedef __bf16 bf8 __attribute__((ext_vector_type(8)));
typedef __bf16 bf2 __attribute__((ext_vector_type(2)));
typedef short s4 __attribute__((ext_vector_type(4)));
#define LAS __attribute__((address_space(3)))
typedef __amdgpu_buffer_rsrc_t rsrc_t;

#define AUX_K 2
#define AUX_V 2
#define AUX_W 0
constexpr int AUX_NT = 2, AUX_SC1 = 16;
constexpr unsigned OOB = 0x80000000u;

constexpr int WS_EPOCH = 0;
constexpr int WS_ALIVE = 4096;
constexpr int WS_XST = 16384;
constexpr int WS_AGP = 65536;
constexpr int WS_AG = 131072;
constexpr int WS_PM = 262144;
constexpr int WS_OUT1 = 1 << 20;
constexpr int WS_OUT2 = 2 << 20;
constexpr int WS_QPM = 3 << 20;
constexpr int WS_STAMP = 4 << 20;
constexpr int WS_BYTES = 8 << 20;

#define TW 6
#define DA_LO 1
#define DA_HI 1
#define DB_LO 1
#define DB_HI 0
#define VRN 14
#define VPRE 9
constexpr int NR = 16;
#define RK 12
#define RL 19
#define HK 19
#define HD 2
#define HN 3
#define QK 25
struct It { int kind, idx; };
#define WOA 10
constexpr int NA = 3 + 32 + WOA + 8;
constexpr It a_item(int m) {
  int pos = 0, wn = 0;
  for (int k = 0; k < 32; ++k) {
    if (pos == m) return It{0, k};
    ++pos;
    if (k >= 4 && k < 7) { if (pos == m) return It{4, k - 4}; ++pos; }
    else if (k >= RK && k < RK + 4) { if (pos == m) return It{5, k - RK}; ++pos; }
    else if (k >= QK && k < QK + 4) { if (pos == m) return It{6, k - QK}; ++pos; }
    else if (k >= 7 && k < 31) {
      int idx = 0; for (int u = 7; u < k; ++u) if (!(u >= RK && u < RK + 4) && !(u >= QK && u < QK + 4)) ++idx;
      if (idx * WOA / 16 != (idx + 1) * WOA / 16 && wn < WOA) { if (pos == m) return It{2, wn}; ++pos; ++wn; }
    }
  }
  return It{-1, 0};
}
static_assert(a_item(NA - 1).kind == 0 && a_item(NA - 1).idx == 31, "phase A list");
constexpr int a_free_between(int lo, int hi) { int n = 0; for (int m = lo; m <= hi; ++m) if (m >= 0 && m < NA && a_item(m).kind == 6) ++n; return n; }
constexpr int a_kpos(int k) { for (int m = 0; m < NA; ++m) if (a_item(m).kind == 0 && a_item(m).idx == k) return m; return NA; }
#define WEARLY 2
#define WLAST 27
constexpr int NBV = 32 + 20 - WOA, NB = NBV + 10;
constexpr It b_item(int m) {
  if (m < VRN) return It{1, m};
  if (m < VRN + WEARLY) return It{2, WOA + m - VRN};
  if (m >= NBV) return It{3, m - NBV};
  int pos = VRN + WEARLY, wn = WOA + WEARLY;
  for (int v = VRN; v < 32; ++v) {
    if (pos == m) return It{1, v};
    ++pos;
    int target = WOA + WEARLY + ((20 - WOA - WEARLY) * (v - VRN + 1) + (WLAST - VRN)) / (WLAST - VRN + 1);
    if (target > 20) target = 20;
    while (wn < target) { if (pos == m) return It{2, wn}; ++pos; ++wn; }
  }
  return It{-1, 0};
}
static_assert(b_item(NBV - 1).kind == 1 && b_item(NBV - 1).idx == 31, "phase B list");
constexpr int b_vpos(int k) { for (int m = 0; m < NB; ++m) if (b_item(m).kind == 1 && b_item(m).idx == k) return m; return NB; }
constexpr int b_extra(int m) { int n = 0; const int p4 = b_vpos(4 + VRN), p8 = b_vpos(8 + VRN); if (m >= p4 && m - p4 < TW) n += 4; if (m >= p8 && m - p8 < TW) n += 2; return n; }
constexpr int a_lastpark_f() { int k = NR - 1; for (int m = 0; m + TW < NA; ++m) if (a_item(m).kind == 0 && a_item(m).idx > k) k = a_item(m).idx; return k; }
constexpr int LASTPARK = a_lastpark_f();
constexpr int NLDS = LASTPARK - NR + 1;

constexpr int L_WL = 0;
constexpr int L_SC = 20480;
constexpr int SC_STRIDE = 336;
constexpr int L_PART = L_SC + 2688;
constexpr int L_WQL = 0;
constexpr int L_QP8 = 20480;
constexpr int L_HB = 32768;
constexpr int L_SB = L_HB + 1280;
constexpr int L_CB = L_SB + 1280;
constexpr int L_GAB = L_CB + 1280;
constexpr int L_XR = L_GAB + 160;
constexpr int L_ML = L_XR + 64;
constexpr int L_FLAG = L_ML + 64;
constexpr int L_QB = L_FLAG + 32;
constexpr int L_H2 = L_QB + 256;
constexpr int L_OP = L_H2 + 1280;
constexpr int L_HP = L_OP + 4096;
constexpr int L_KS = L_HP + 4096;
constexpr int L_YP = L_KS;
constexpr int L_END = L_KS + (8 * NLDS * 1024 > 20480 ? 8 * NLDS * 1024 : 20480);
static_assert(L_QP8 + 8192 <= 32768 && L_PART + 4096 <= 32768 && L_HB % 16 == 0 && L_QB % 16 == 0 && L_OP % 16 == 0 && L_KS % 16 == 0 && L_GAB % 16 == 0 && L_H2 % 16 == 0 && L_END <= 163840, "lds map");

#define D_RS1 8
#define D_AG 10
#define D_RS2 8
#define POLL_SLEEP 1
#define STAMPS 0
#define NODUTY 0
#define NOPARK 0
#define PGT 1
#define NI0 4
#define NOW 0
constexpr unsigned long long WAIT_TICKS = NODUTY ? 1000ull : 1000000000ull;

__device__ __forceinline__ float bflo(unsigned d) { return __uint_as_float(d << 16); }
__device__ __forceinline__ float bfhi(unsigned d) { return __uint_as_float(d & 0xffff0000u); }
__device__ __forceinline__ unsigned cvtpk(float lo, float hi) {
  bf2 r = {(__bf16)lo, (__bf16)hi}; return __builtin_bit_cast(unsigned, r);
}
__device__ __forceinline__ float rbf(float f) { return bflo(cvtpk(f, 0.f)); }
__device__ __forceinline__ void rbf2(float& a, float& b) { const unsigned p = cvtpk(a, b); a = bflo(p); b = bfhi(p); }
__device__ __forceinline__ float frcp(float x) { return __builtin_amdgcn_rcpf(x); }
__device__ __forceinline__ float fsilu(float a) { return a * frcp(1.f + __builtin_amdgcn_exp2f(a * -1.4426950408889634f)); }
template <int CTRL> __device__ __forceinline__ float dppf(float x) {
  return __builtin_bit_cast(float, __builtin_amdgcn_update_dpp(0, __builtin_bit_cast(int, x), CTRL, 0xf, 0xf, true));
}
template <int CTRL> __device__ __forceinline__ unsigned dppu(unsigned x) { return (unsigned)__builtin_amdgcn_update_dpp(0, (int)x, CTRL, 0xf, 0xf, true); }
constexpr int XOR1 = 0xB1, XOR2 = 0x4E, XOR7 = 0x141, XOR8 = 0x128, QB0 = 0x00, QB2 = 0xAA;
__device__ __forceinline__ float x16_sum(float x) { auto s = __builtin_amdgcn_permlane16_swap(__float_as_uint(x), __float_as_uint(x), false, false); return __uint_as_float(s[0]) + __uint_as_float(s[1]); }
__device__ __forceinline__ float x32_sum(float x) { auto s = __builtin_amdgcn_permlane32_swap(__float_as_uint(x), __float_as_uint(x), false, false); return __uint_as_float(s[0]) + __uint_as_float(s[1]); }
__device__ __forceinline__ float x16_max(float x) { auto s = __builtin_amdgcn_permlane16_swap(__float_as_uint(x), __float_as_uint(x), false, false); return fmaxf(__uint_as_float(s[0]), __uint_as_float(s[1])); }
__device__ __forceinline__ float x32_max(float x) { auto s = __builtin_amdgcn_permlane32_swap(__float_as_uint(x), __float_as_uint(x), false, false); return fmaxf(__uint_as_float(s[0]), __uint_as_float(s[1])); }
__device__ __forceinline__ float wave_sum(float x) {
  x += dppf<XOR1>(x); x += dppf<XOR2>(x); x += dppf<XOR7>(x); x += dppf<XOR8>(x); return x32_sum(x16_sum(x));
}
__device__ __forceinline__ float wave_max(float x) {
  x = fmaxf(x, dppf<XOR1>(x)); x = fmaxf(x, dppf<XOR2>(x)); x = fmaxf(x, dppf<XOR7>(x)); x = fmaxf(x, dppf<XOR8>(x)); return x32_max(x16_max(x));
}
__device__ __forceinline__ f4 mfma(v4u a, v4u b, f4 c) {
  return __builtin_amdgcn_mfma_f32_16x16x32_bf16(__builtin_bit_cast(bf8, a), __builtin_bit_cast(bf8, b), c, 0, 0, 0);
}
__device__ __forceinline__ float dot2(unsigned a, unsigned b, float c) {
  return __builtin_amdgcn_fdot2_f32_bf16(__builtin_bit_cast(bf2, a), __builtin_bit_cast(bf2, b), c, false);
}
__device__ __forceinline__ rsrc_t mkrs(const void* p, unsigned bytes) { return __builtin_amdgcn_make_buffer_rsrc((void*)p, 0, (int)bytes, 0x00020000); }
template <int AUX> __device__ __forceinline__ v4u ld(rsrc_t r, unsigned voff, unsigned soff) { return __builtin_amdgcn_raw_buffer_load_b128(r, (int)voff, (int)soff, AUX); }
__device__ __forceinline__ v4u ldsc(rsrc_t r, unsigned voff) { return __builtin_amdgcn_raw_buffer_load_b128(r, (int)voff, 0, AUX_SC1); }
__device__ __forceinline__ void stsc(rsrc_t r, unsigned voff, v4u v) { __builtin_amdgcn_raw_buffer_store_b128(v, r, (int)voff, 0, AUX_SC1); }
__device__ __forceinline__ void stsc2(rsrc_t r, unsigned voff, v2u v) { __builtin_amdgcn_raw_buffer_store_b64(v, r, (int)voff, 0, AUX_SC1); }
#define WAITVM(n) asm volatile("s_waitcnt vmcnt(%0)" ::"n"((n) > 63 ? 63 : (n)) : "memory")
#define WAITW(n, lo, hi) do { if ((lo) == (hi)) WAITVM((n) - (lo)); else if (w < 4) WAITVM((n) - (lo)); else WAITVM((n) - (hi)); } while (0)
#define BAR() asm volatile("s_waitcnt lgkmcnt(0)\n\ts_barrier" ::: "memory")
#define SB() __builtin_amdgcn_sched_barrier(0)
#define FENCE() asm volatile("" ::: "memory")
#define PIN(x) asm volatile("" : "+v"(x))

template <typename T> __device__ __forceinline__ T lds_ld(LAS unsigned char* L, int off) { return *(LAS T*)(L + off); }
template <typename T> __device__ __forceinline__ void lds_st(LAS unsigned char* L, int off, T v) { *(LAS T*)(L + off) = v; }
template <int I, int N, typename F> __device__ __forceinline__ void sfor(F&& f) {
  if constexpr (I < N) { f(std::integral_constant<int, I>{}); sfor<I + 1, N>(f); }
}
template <int I> using IC = std::integral_constant<int, I>;
#define AINL __attribute__((always_inline))

struct Sel { unsigned s[2][4]; };
__device__ __forceinline__ v4u diagA(unsigned X, const Sel& S, int par) {
  v4u a;
#pragma unroll
  for (int d = 0; d < 4; ++d) a[d] = __builtin_amdgcn_perm(X, X, S.s[par][d]);
  return a;
}
__device__ __forceinline__ v4u tag4(f4 v, unsigned T) {
  v4u r;
#pragma unroll
  for (int e = 0; e < 4; ++e) r[e] = ((__float_as_uint(v[e]) + 0x80u) & 0xffffff00u) | ((T >> (8 * e)) & 0xffu);
  return r;
}
__device__ __forceinline__ bool tagok4(v4u v, unsigned T) {
  const unsigned lo = __builtin_amdgcn_perm(v.y, v.x, 0x0c0c0400u), hi = __builtin_amdgcn_perm(v.w, v.z, 0x04000c0cu);
  return (lo | hi) == T;
}
__device__ __forceinline__ unsigned long long ballot(bool p) { return __builtin_amdgcn_ballot_w64(p); }

#define STAMPK 0
#define STAMP(n) do { } while (0)
#define KSTAMP(k) do { } while (0)

__global__ void __launch_bounds__(512, 2)
fused_kernel(const void* px, const void* pk, const void* pv, const void* pwq,
             const void* pwo, const void* pw1, const void* pw2, const void* pc,
             const void* pwm, void* pout, void* pws) {
  __shared__ __attribute__((aligned(16))) unsigned char smem[L_END];
  LAS unsigned char* L = (LAS unsigned char*)smem;
  const int t = threadIdx.x, lane = t & 63, w = __builtin_amdgcn_readfirstlane(t >> 6);
  const int wg = blockIdx.x, xg = wg & 7, e8 = wg >> 3, b = wg >> 5, h = xg * 4 + (e8 & 3);
  const int g = lane >> 4, c = lane & 15;
  const bool mlp = h < 27;
  asm volatile("" :: "s"(px), "s"(pk), "s"(pv), "s"(pwq), "s"(pwo), "s"(pw1), "s"(pw2), "s"(pc), "s"(pwm), "s"(pout), "s"(pws));
  const unsigned long long t_start = __builtin_amdgcn_s_memrealtime();
  auto expired = [&](unsigned& spins) AINL { return (++spins & 255u) == 0u && __builtin_amdgcn_s_memrealtime() - t_start > WAIT_TICKS; };
  STAMP(0);
  const rsrc_t rsWS = mkrs(pws, WS_BYTES);
  const rsrc_t rsK = mkrs((const char*)pk + ((size_t)(b * 32 + h) * 4096 + 3072) * 256, 262144);
  const rsrc_t rsV = mkrs((const char*)pv + ((size_t)(b * 32 + h) * 4096 + 3072) * 256, 262144);
  const rsrc_t rsWQ = mkrs(pwq, 640 * 4096 * 2), rsWO = mkrs(pwo, 4096 * 640 * 2), rsW1 = mkrs(pw1, 640 * 1728 * 2), rsW2 = mkrs(pw2, 1728 * 640 * 2);
  const rsrc_t rsWM = mkrs(pwm, 640 * 3840 * 2), rsC = mkrs(pc, 10240), rsX = mkrs(px, 10240), rsOUT = mkrs(pout, 10240);

  unsigned po[4]; v4u pvv[4];
  v4u WQ[3], WO[20], KB[NR], KL[TW + 1];
  const unsigned offWQ = NOW ? OOB : (unsigned)(80 * b + 20 * g + w) * 8192u + h * 256 + c * 16;
  const unsigned offWO = NOW ? OOB : (unsigned)(h * 128 + 16 * w + 4 * g) * 1280u + c * 16;
  const unsigned offKV = w * 1024 + lane * 16;
  const int ksoff = L_KS + w * (NLDS * 1024) + lane * 16;
  auto ldwo = [&](auto ic) AINL { constexpr int i = decltype(ic)::value; WO[i] = ld<AUX_W>(rsWO, offWO, (i / 5) * 1280 + (i % 5) * 256); };
  auto a_issue = [&](auto mc) AINL {
    constexpr It it = a_item(decltype(mc)::value);
    if constexpr (it.kind == 4) WQ[it.idx] = ld<AUX_NT>(rsWQ, (it.idx < 2 || w < 4) ? offWQ : OOB, it.idx * 8 * 8192);
    else if constexpr (it.kind == 2) ldwo(IC<it.idx>{});
    else if constexpr (it.kind == 5 || it.kind == 6) pvv[it.idx] = ldsc(rsWS, po[it.idx]);
    else if constexpr (it.idx < NR) KB[it.idx] = ld<AUX_K>(rsK, offKV, it.idx * 8192);
    else KL[(it.idx - NR) % (TW + 1)] = ld<AUX_K>(rsK, offKV, it.idx * 8192);
  };
  constexpr int NINIT0 = NI0;
  sfor<0, NINIT0>(a_issue);
  SB();
  const bool role = wg < 240;
  const int ta = wg % 60, kq = wg / 60;
  v4u cv0 = ld<AUX_NT>(rsC, (role && t < 160) ? (t / 20) * 1280 + kq * 320 + (t % 20) * 16 : OOB, 0);
  const unsigned wmoff = (unsigned)(160 * kq + 8 * w + (lane >> 3)) * 7680u + ta * 128 + (lane & 7) * 16;
  v4u wm0 = ld<AUX_NT>(rsWM, role ? wmoff : OOB, 0);
  v4u wm1 = ld<AUX_NT>(rsWM, role ? wmoff : OOB, 64 * 7680);
  v4u wm2 = ld<AUX_NT>(rsWM, (role && w < 4) ? wmoff : OOB, 128 * 7680);
  unsigned Eraw = 0;
  if (w == 0) Eraw = __builtin_amdgcn_raw_buffer_load_b32(rsWS, WS_EPOCH, 0, AUX_SC1);
  unsigned pg0 = 0, pg1 = 0, pg2 = 0, pg3 = 0, pg4 = 0, pg5 = 0;
  if (PGT && w == 7) {
    pg0 = __builtin_amdgcn_raw_buffer_load_b32(rsV, lane < 2 ? lane * 262140 : OOB, 0, AUX_NT);
    pg1 = __builtin_amdgcn_raw_buffer_load_b32(rsWO, lane < 2 ? h * 163840 + lane * 163836 : OOB, 0, AUX_W);
    pg2 = __builtin_amdgcn_raw_buffer_load_b32(rsW1, lane < 8 ? lane * 80 * 3456 + h * 128 : OOB, 0, AUX_W);
    pg3 = __builtin_amdgcn_raw_buffer_load_b32(rsW2, lane < 2 ? h * 81920 + lane * 81916 : OOB, 0, AUX_W);
    pg4 = __builtin_amdgcn_raw_buffer_load_b32(rsWS, lane < 16 ? lane * 262144 + 1024 : OOB, 0, AUX_SC1);
    pg5 = __builtin_amdgcn_raw_buffer_load_b32(rsOUT, lane == 0 ? b * 1280 : OOB, 0, AUX_SC1);
  }
  if (t < 8) lds_st<unsigned>(L, L_FLAG + t * 4, 0u);
  SB();
  constexpr int NINIT1 = NI0 + 2, NINIT2 = NI0 + 3, NINIT3 = NI0 + 5, NINIT = NI0 + 6;
  static_assert(NINIT >= TW && a_item(NINIT - 1).idx < NR && NINIT - TW <= 5, "no parking during P1; the wq pieces (positions 5, 7, 9) are parked in the main loop");
  STAMP(1);
  if (w == 7) WAITVM(PGT ? 6 : 0); else if (w == 0) WAITVM(1); else WAITVM(0);
  sfor<NINIT0, NINIT1>(a_issue);
  SB();
  if (w == 0 && lane == 0) lds_st<unsigned>(L, L_ML, Eraw + 1u);
  if (wg >= 240 && wg < 248 && w == 0) {
    const unsigned T = (unsigned)__builtin_amdgcn_readfirstlane((int)Eraw) + 1u;
    const v4u xa = ld<AUX_NT>(rsX, (wg - 240) * 1280 + lane * 16, 0);
    const unsigned xb = __builtin_amdgcn_raw_buffer_load_b32(rsX, (wg - 240) * 1280 + 1024 + lane * 4, 0, AUX_NT);
    float xv[10];
#pragma unroll
    for (int d = 0; d < 4; ++d) { xv[2 * d] = bflo(xa[d]); xv[2 * d + 1] = bfhi(xa[d]); }
    xv[8] = bflo(xb); xv[9] = bfhi(xb);
    const float sm = ((xv[0] + xv[1]) + (xv[2] + xv[3])) + ((xv[4] + xv[5]) + (xv[6] + xv[7])) + (xv[8] + xv[9]);
    const float mean = wave_sum(sm) * (1.f / 640.f);
    float q2 = 0.f;
#pragma unroll
    for (int d = 0; d < 10; ++d) { const float dd = xv[d] - mean; q2 = fmaf(dd, dd, q2); }
    const float var = rbf(wave_sum(q2) * (1.f / 640.f));
    const float mb = rbf(mean), den = rbf(__builtin_amdgcn_sqrtf(rbf(var + 1e-5f)));
    if (lane == 0) stsc(rsWS, WS_XST + (wg - 240) * 16, v4u{__float_as_uint(mb) | (T & 0xffu), __float_as_uint(den) | ((T >> 8) & 0xffu), (T >> 16) & 0xffu, T >> 24});
  }
  {
    auto silu8 = [&](v4u v) AINL {
      v4u r;
#pragma unroll
      for (int d = 0; d < 4; ++d) r[d] = cvtpk(fsilu(bflo(v[d])), fsilu(bfhi(v[d])));
      return r;
    };
    if (t < 160) lds_st<v4u>(L, L_SC + (t / 20) * SC_STRIDE + (t % 20) * 16, silu8(cv0));
    lds_st<v4u>(L, L_WL + w * 1024 + lane * 16, wm0);
    lds_st<v4u>(L, L_WL + (w + 8) * 1024 + lane * 16, wm1);
    if (w < 4) lds_st<v4u>(L, L_WL + (w + 16) * 1024 + lane * 16, wm2);
  }
  SB();
  sfor<NINIT1, NINIT2>(a_issue);
  v2u xh = __builtin_amdgcn_raw_buffer_load_b64(rsX, lane < 20 ? w * 1280 + 160 * b + lane * 8 : OOB, 0, AUX_NT);
  v2u xown = __builtin_amdgcn_raw_buffer_load_b64(rsX, (w == 4 && lane < 5) ? b * 1280 + 40 * h + lane * 8 : OOB, 0, AUX_NT);
  SB();
  BAR();
  const unsigned T = (unsigned)__builtin_amdgcn_readfirstlane((int)lds_ld<unsigned>(L, L_ML));
  STAMP(2);
  sfor<NINIT2, NINIT3>([&](auto mc) AINL { WAITVM(TW - 2 + 2); a_issue(mc); SB(); });
  {
    f4 acc = {0.f, 0.f, 0.f, 0.f};
    const int tl = w & 3, s0_ = (w >> 2) * 3;
    const int trbase = L_WL + (8 * g + ((lane & 15) >> 2)) * 128 + tl * 32 + (lane & 3) * 8;
#pragma unroll
    for (int n = 0; n < 3; ++n) {
      if (n < 2 || w < 4) {
        const int s = s0_ + n;
        const v4u a = lds_ld<v4u>(L, L_SC + (c & 7) * SC_STRIDE + s * 64 + g * 16);
        const s4 b0 = __builtin_amdgcn_ds_read_tr16_b64_v4i16((LAS s4*)(L + trbase + s * 4096));
        const s4 b1 = __builtin_amdgcn_ds_read_tr16_b64_v4i16((LAS s4*)(L + trbase + s * 4096 + 512));
        const v2u u0 = __builtin_bit_cast(v2u, b0), u1 = __builtin_bit_cast(v2u, b1);
        acc = mfma(a, v4u{u0.x, u0.y, u1.x, u1.y}, acc);
      }
    }
    if (g < 2) {
#pragma unroll
      for (int r = 0; r < 4; ++r) lds_st<float>(L, L_PART + (w >> 2) * 2048 + (4 * g + r) * 256 + (16 * tl + c) * 4, acc[r]);
    }
  }
  sfor<NINIT3, NINIT>([&](auto mc) AINL { WAITVM(TW - 2 + 2); a_issue(mc); SB(); });
  BAR();
  {
    const f4 s = lds_ld<f4>(L, L_PART + (t & 127) * 16) + lds_ld<f4>(L, L_PART + 2048 + (t & 127) * 16);
    unsigned o = OOB;
    if (t < 128) { if (role) o = WS_PM + (((t >> 4) * 4 + kq) * 3840 + ta * 64) * 4 + (t & 15) * 16; }
    else if (t == 128) o = WS_ALIVE + wg * 16;
    stsc(rsWS, o, tag4(s, T));
  }
  STAMP(3);
  Sel S;
  {
    const int e = c & 7;
#pragma unroll
    for (int par = 0; par < 2; ++par)
#pragma unroll
      for (int d = 0; d < 4; ++d) {
        const unsigned lo = (unsigned)(2 * par) | ((unsigned)(2 * par + 1) << 8);
        S.s[par][d] = (d == (e >> 1)) ? ((e & 1) ? (0x0c0cu | (lo << 16)) : (0x0c0c0000u | lo)) : 0x0c0c0c0cu;
      }
  }
  unsigned long long pm0, pm1, pm2, pm3;
  auto pmasks = [&]() AINL { pm0 = ballot(po[0] != OOB); pm1 = ballot(po[1] != OOB); pm2 = ballot(po[2] != OOB); pm3 = ballot(po[3] != OOB); };
#pragma unroll
  for (int q = 0; q < 4; ++q) po[q] = lane < 40 ? WS_PM + ((w * 4 + q) * 3840 + (lane / 20) * 640 + 80 * b + 4 * (lane % 20)) * 4 : OOB;
  if (lane == 40) po[0] = WS_XST + w * 16;
  pmasks();
  auto pissue = [&]() AINL {
#pragma unroll
    for (int q = 0; q < 4; ++q) pvv[q] = ldsc(rsWS, po[q]);
  };
  auto pok = [&]() AINL {
    return (ballot(tagok4(pvv[0], T)) & pm0) == pm0 && (ballot(tagok4(pvv[1], T)) & pm1) == pm1 && (ballot(tagok4(pvv[2], T)) & pm2) == pm2 && (ballot(tagok4(pvv[3], T)) & pm3) == pm3;
  };
  auto preload = [&]() AINL {
#pragma unroll
    for (int q = 0; q < 4; ++q) { const bool k1 = tagok4(pvv[q], T); const v4u nv = ldsc(rsWS, k1 ? OOB : po[q]); pvv[q] = k1 ? pvv[q] : nv; }
  };
  auto pblock = [&]() AINL {
    unsigned spins = 0;
    while (!pok() && !expired(spins)) { __builtin_amdgcn_s_sleep(2); FENCE(); preload(); }
  };
  auto psum = [&]() AINL {
    f4 mv;
#pragma unroll
    for (int e = 0; e < 4; ++e) mv[e] = (__uint_as_float(pvv[0][e] & 0xffffff00u) + __uint_as_float(pvv[1][e] & 0xffffff00u)) +
                                        (__uint_as_float(pvv[2][e] & 0xffffff00u) + __uint_as_float(pvv[3][e] & 0xffffff00u));
    float a0 = mv[0], a1 = mv[1], a2 = mv[2], a3 = mv[3]; rbf2(a0, a1); rbf2(a2, a3);
    return f4{a0, a1, a2, a3};
  };
  bool hdone = false;
  auto rprocess = [&]() AINL {
    const f4 mv = psum();
    const float xmean = __uint_as_float((unsigned)__builtin_amdgcn_readlane((int)pvv[0].x, 40) & 0xffff0000u);
    const float xrden = frcp(__uint_as_float((unsigned)__builtin_amdgcn_readlane((int)pvv[0].y, 40) & 0xffff0000u));
    float lnv[4] = {bflo(xh.x) - xmean, bfhi(xh.x) - xmean, bflo(xh.y) - xmean, bfhi(xh.y) - xmean};
    rbf2(lnv[0], lnv[1]); rbf2(lnv[2], lnv[3]);
#pragma unroll
    for (int e = 0; e < 4; ++e) lnv[e] *= xrden;
    rbf2(lnv[0], lnv[1]); rbf2(lnv[2], lnv[3]);
    float hv[4];
#pragma unroll
    for (int e = 0; e < 4; ++e) {
      const float ca = __uint_as_float(__builtin_amdgcn_ds_bpermute(((lane + 20) & 63) * 4, (int)__float_as_uint(mv[e])));
      hv[e] = rbf(lnv[e] * rbf(1.f + ca)) + mv[e];
    }
    if (lane < 20) lds_st<v2u>(L, L_HB + w * 160 + lane * 8, v2u{cvtpk(hv[0], hv[1]), cvtpk(hv[2], hv[3])});
    asm volatile("s_waitcnt lgkmcnt(0)" ::: "memory");
    if (lane == 0) __hip_atomic_fetch_add((LAS unsigned*)(L + L_FLAG), 1u, __ATOMIC_RELAXED, __HIP_MEMORY_SCOPE_WORKGROUP);
    STAMP(14);
  };
  auto mprocess = [&]() AINL {
    const f4 mv = psum();
    v4u msg;
#pragma unroll
    for (int e = 0; e < 4; ++e) msg[e] = (__float_as_uint(rbf(lane >= 10 ? 1.f + mv[e] : mv[e])) & 0xffff0000u) | ((T >> (8 * e)) & 0xffu);
    if (lane >= 5 && lane < 15) stsc(rsWS, WS_AGP + (b * 32 + h) * 256 + lane * 16, msg);
    else if (lane < 5) lds_st<f4>(L, L_GAB + lane * 16, mv);
    else if (lane < 20) lds_st<f4>(L, L_GAB + 80 + (lane - 15) * 16, mv);
    STAMP(17);
  };
  auto bfpack = [&](v4u v) AINL { return v2u{__builtin_amdgcn_perm(v.y, v.x, 0x07060302u), __builtin_amdgcn_perm(v.w, v.z, 0x07060302u)}; };
  auto hflag = [&]() AINL { FENCE(); return __builtin_amdgcn_readfirstlane((int)*(volatile LAS unsigned*)(L + L_FLAG)) == 8; };
  auto hstep = [&]() AINL {
    const int pi = w & 3, hf = w >> 2;
    unsigned X[5]; v4u Bp[10];
#pragma unroll
    for (int d = 0; d < 5; ++d) X[d] = lds_ld<unsigned>(L, L_HB + (2 * pi + (c >> 3)) * 160 + 40 * g + 20 * hf + d * 4);
#pragma unroll
    for (int i = 0; i < 10; ++i) Bp[i] = lds_ld<v4u>(L, L_WQL + (10 * hf + i) * 1024 + lane * 16);
    f4 a0 = {0.f, 0.f, 0.f, 0.f}, a1 = {0.f, 0.f, 0.f, 0.f};
#pragma unroll
    for (int i = 0; i < 10; i += 2) { a0 = mfma(diagA(X[i >> 1], S, 0), Bp[i], a0); a1 = mfma(diagA(X[i >> 1], S, 1), Bp[i + 1], a1); }
    a0 += a1;
    lds_st<f4>(L, L_QP8 + w * 1024 + lane * 16, a0);
    asm volatile("s_waitcnt lgkmcnt(0)" ::: "memory");
    unsigned old = 0;
    if (lane == 0) old = __hip_atomic_fetch_add((LAS unsigned*)(L + L_FLAG + 4 + pi * 4), 1u, __ATOMIC_RELAXED, __HIP_MEMORY_SCOPE_WORKGROUP);
    if (__builtin_amdgcn_readfirstlane((int)old) == 1) {
      FENCE();
      const f4 lo = lds_ld<f4>(L, L_QP8 + pi * 1024 + lane * 16), hi = lds_ld<f4>(L, L_QP8 + (pi + 4) * 1024 + lane * 16);
      stsc(rsWS, WS_QPM + (h * 8 + b) * 4096 + (2 * pi + (g >> 1)) * 512 + (2 * c + (g & 1)) * 16, tag4(lo + hi, T));
      STAMP(16);
    }
  };
  static_assert(a_kpos(RL) >= a_kpos(6) + 1 + TW, "wq parked before the first h arrival");
  static_assert(a_kpos(RL) >= a_kpos(RK + 3) + 1 + TW - 1 && RL <= HK && HK + (HN - 1) * HD < QK - 1 && RL < QK - 1 && QK + 3 < 31, "stage order");
  auto pnone = [&]() AINL {
#pragma unroll
    for (int q = 0; q < 4; ++q) po[q] = OOB;
  };
  sfor<NINIT, NA>([&](auto mc) AINL {
    constexpr int m = decltype(mc)::value;
    constexpr It it = a_item(m);
    constexpr int k = it.kind == 0 ? it.idx : -100;
    WAITW(TW - 1 + (m - (TW - DA_LO) < NINIT ? 1 : 0) + a_free_between(m - (TW - DA_LO), m - 1), DA_LO, DA_HI);
    {
      constexpr It ol = a_item(m - TW);
      if constexpr (ol.kind == 4) { if (ol.idx < 2 || w < 4) lds_st<v4u>(L, L_WQL + (w + 8 * ol.idx) * 1024 + lane * 16, WQ[ol.idx]); }
      if constexpr (!NOPARK && ol.kind == 0 && ol.idx >= NR) lds_st<v4u>(L, ksoff + (ol.idx - NR) * 1024, KL[(ol.idx - NR) % (TW + 1)]);
    }
    a_issue(mc);
    if constexpr (k == 9) { if (w == 4 && lane < 5) lds_st<v2u>(L, L_XR + lane * 8, xown); }
    KSTAMP(k);
    if constexpr (k == 10) asm volatile("" :: "v"(pg0), "v"(pg1), "v"(pg2), "v"(pg3), "v"(pg4), "v"(pg5));
    if constexpr (k == 8) STAMP(18);
    if constexpr (k == 16) STAMP(19);
    if constexpr (k == 24) STAMP(20);
    if constexpr (!NODUTY) {
    if constexpr (k == RL) { pblock(); rprocess(); }
    if constexpr (k >= HK && k < HK + (HN - 1) * HD && (k - HK) % HD == 0) { if (!hdone && hflag()) { hstep(); hdone = true; } }
    if constexpr (k == HK + (HN - 1) * HD) {
      if (!hdone) { unsigned spins = 0; while (!hflag() && !expired(spins)) __builtin_amdgcn_s_sleep(1); hstep(); }
    }
    if constexpr (k == QK - 1) {
#pragma unroll
      for (int q = 0; q < 4; ++q) {
        unsigned o = OOB;
        if (w == 5) { if (lane < 20) o = WS_PM + ((b * 4 + q) * 3840 + (2 + lane / 5) * 640 + 20 * h + 4 * (lane % 5)) * 4; }
        else if (w == 6) o = WS_QPM + ((h * 8 + 2 * q + (lane >> 5)) * 8 + b) * 512 + (lane & 31) * 16;
        else if (w == 4 && wg == 0) o = WS_ALIVE + (64 * q + lane) * 16;
        po[q] = o;
      }
      pmasks();
    }
    }
    SB();
  });
  STAMP(4);
  v4u W1[10], W2[10], VR[VRN];
  const unsigned offW1 = (mlp && !NOW) ? (unsigned)(80 * w + 40 * (c >> 3) + 10 * g) * 3456u + (64 * h + 8 * (c & 7)) * 2 : OOB;
  const unsigned offW2 = (mlp && !NOW) ? (unsigned)(64 * h + 8 * w + 2 * g) * 1280u + c * 16 : OOB;
  auto b_issue = [&](auto mc) AINL {
    constexpr It it = b_item(decltype(mc)::value);
    if constexpr (it.kind == 1) VR[it.idx % VRN] = ld<AUX_V>(rsV, offKV, it.idx * 8192);
    else if constexpr (it.kind == 2) ldwo(IC<it.idx>{});
    else W1[it.idx] = ld<AUX_W>(rsW1, offW1, it.idx * 3456);
    SB();
  };
#define VTICK(k) do { WAITW(TW - 1, DB_LO, DB_HI); b_issue(IC<(k)>{}); } while (0)
  sfor<0, VPRE>([&](auto kc) AINL { WAITW(TW - 1 + a_free_between(NA + decltype(kc)::value - (TW - DB_LO), NA - 1), DB_LO, DB_HI); b_issue(kc); });
  if (!NODUTY && w == 5) { pblock(); mprocess(); }
  if (!NODUTY && (w == 6 || w == 4)) {
    pblock();
    if (w == 6) {
      f4 q4;
#pragma unroll
      for (int e = 0; e < 4; ++e) q4[e] = x32_sum((__uint_as_float(pvv[0][e] & 0xffffff00u) + __uint_as_float(pvv[1][e] & 0xffffff00u)) +
                                                  (__uint_as_float(pvv[2][e] & 0xffffff00u) + __uint_as_float(pvv[3][e] & 0xffffff00u)));
      if (lane < 32) lds_st<v2u>(L, L_QB + lane * 8, v2u{cvtpk(q4[0], q4[1]), cvtpk(q4[2], q4[3])});
    } else {
      if (wg == 0 && lane == 0) __builtin_amdgcn_raw_buffer_store_b32(T, rsWS, WS_EPOCH, 0, AUX_SC1);
    }
  }
  BAR();
  STAMP(5);
  const v4u qr = lds_ld<v4u>(L, L_QB + c * 16);
  STAMP(6);
  auto kdot = [&](v4u k) AINL { return dot2(k.w, qr[3], dot2(k.z, qr[2], dot2(k.y, qr[1], dot2(k.x, qr[0], 0.f)))); };
  auto treduce = [&](float (&xs)[16]) AINL {
    float x[8];
#pragma unroll
    for (int i = 0; i < 8; ++i) { const float lo = xs[i] + dppf<XOR8>(xs[i]), hi = xs[i + 8] + dppf<XOR8>(xs[i + 8]); x[i] = (c & 8) ? hi : lo; }
#pragma unroll
    for (int i = 0; i < 4; ++i) { const float lo = x[i] + dppf<XOR7>(x[i]), hi = x[i + 4] + dppf<XOR7>(x[i + 4]); x[i] = (c & 4) ? hi : lo; }
#pragma unroll
    for (int i = 0; i < 2; ++i) { const float lo = x[i] + dppf<XOR2>(x[i]), hi = x[i + 2] + dppf<XOR2>(x[i + 2]); x[i] = (c & 2) ? hi : lo; }
    const float lo = x[0] + dppf<XOR1>(x[0]), hi = x[1] + dppf<XOR1>(x[1]);
    return (c & 1) ? hi : lo;
  };
  constexpr float C2 = 0.08838834764831845f * 1.4426950408889634f;
  float mw, fw = 0.f;
  unsigned ppk;
  {
    float xs[16];
#pragma unroll
    for (int j = 0; j < 16; ++j) xs[j] = kdot(KB[j]);
    SB();
    VTICK(VPRE);
    const float s0 = treduce(xs);
    SB();
    VTICK(VPRE + 1);
#pragma unroll
    for (int j = 0; j <= LASTPARK - NR; ++j) xs[j] = kdot(lds_ld<v4u>(L, ksoff + j * 1024));
#pragma unroll
    for (int j = LASTPARK - NR + 1; j < 16; ++j) xs[j] = kdot(KL[j % (TW + 1)]);
    SB();
    sfor<VPRE + 2, (VPRE + 4 < VRN + WEARLY ? VPRE + 4 : VRN + WEARLY)>([&](auto kc) AINL { WAITW(TW - 1, DB_LO, DB_HI); b_issue(kc); });
    const float s1 = treduce(xs);
    SB();
    sfor<(VPRE + 4 < VRN + WEARLY ? VPRE + 4 : VRN + WEARLY), VRN + WEARLY>([&](auto kc) AINL { WAITW(TW - 1, DB_LO, DB_HI); b_issue(kc); });
    mw = wave_max(fmaxf(s0, s1));
    const float p0 = __builtin_amdgcn_exp2f((s0 - mw) * C2), p1 = __builtin_amdgcn_exp2f((s1 - mw) * C2);
    const float lw = wave_sum(p0 + p1);
    ppk = cvtpk(p0, p1);
    if (lane == 0) lds_st<v2u>(L, L_ML + w * 8, v2u{__float_as_uint(mw), __float_as_uint(lw)});
  }
  STAMP(15);
  f4 oacc = {0.f, 0.f, 0.f, 0.f};
  unsigned touch0 = 0, touch1 = 0;
  auto pvstep = [&](auto jc) AINL {
    constexpr int j = decltype(jc)::value;
    const unsigned X = dppu<0x150 + (j & 15)>(ppk);
    oacc = mfma(diagA(X, S, j >> 4), VR[j % VRN], oacc);
    if constexpr (j == 4) {
#pragma unroll
      for (int q = 0; q < 4; ++q) { const int idx = 64 * q + lane; po[q] = ((w == 1 || w == 2) && mlp && idx < 160) ? WS_AGP + (b * 32 + idx / 5) * 256 + (5 * w + idx % 5) * 16 : OOB; }
      pmasks(); pissue();
    }
    if constexpr (j == 14) {
      if (!NODUTY && (w == 1 || w == 2)) {
        pblock();
#pragma unroll
        for (int n = 0; n < 3; ++n) {
          const int idx = 64 * n + lane;
          if (po[n] != OOB) lds_st<v2u>(L, (w == 1 ? L_SB : L_CB) + (20 * (idx / 5) + 4 * (idx % 5)) * 2, bfpack(pvv[n]));
        }
      }
    }
    if constexpr (j == 8) {
      unsigned o0 = OOB, o1 = OOB;
      if (w == 4) {
        const int u = lane & 31; const int wgu = (b << 5) | ((u & 3) << 3) | (u >> 2);
        o0 = (lane < 32 ? WS_OUT1 : WS_OUT2) + wgu * 4096 + h * 128;
        o1 = lane < 32 ? WS_AG + (b * 32 + lane) * 128 : OOB;
      } else if (w == 0) { o0 = (lane < 32 ? WS_OUT1 : WS_OUT2) + wg * 4096 + (lane & 31) * 128; }
      touch0 = __builtin_amdgcn_raw_buffer_load_b32(rsWS, (int)o0, 0, AUX_SC1);
      touch1 = __builtin_amdgcn_raw_buffer_load_b32(rsWS, (int)o1, 0, AUX_SC1);
    }
    if constexpr (j == 16) { asm volatile("" :: "v"(touch0), "v"(touch1)); }
    if constexpr (j == 10) {
      BAR();
      float M = -INFINITY;
      v2u ml[8];
#pragma unroll
      for (int ww = 0; ww < 8; ++ww) { ml[ww] = lds_ld<v2u>(L, L_ML + ww * 8); M = fmaxf(M, __uint_as_float(ml[ww].x)); }
      float Ls = 0.f;
#pragma unroll
      for (int ww = 0; ww < 8; ++ww) Ls += __builtin_amdgcn_exp2f((__uint_as_float(ml[ww].x) - M) * C2) * __uint_as_float(ml[ww].y);
      fw = __builtin_amdgcn_exp2f((mw - M) * C2) * frcp(Ls);
      PIN(fw);
    }
  };
  sfor<VRN + WEARLY, NB>([&](auto mc) AINL {
    constexpr int m = decltype(mc)::value;
    constexpr It it = b_item(m);
    if constexpr (it.kind == 1) pvstep(IC<it.idx - VRN>{});
    if constexpr (m == NBV) sfor<32 - VRN, 22>([&](auto jc) AINL { pvstep(jc); SB(); });
    if constexpr (it.kind != 3) WAITW(TW - 1 + b_extra(m), DB_LO, DB_HI);
    else { pvstep(IC<22 + it.idx>{}); }
    b_issue(mc);
  });
  STAMP(7);
  {
    const f4 ov = oacc * fw;
    if (lane < 32) lds_st<f4>(L, L_OP + w * 512 + (8 * c + 4 * g) * 4, ov);
  }
  BAR();
  const int ypoff = L_YP + w * 2560 + (8 * c + 4 * (g & 1)) * 4;
  {
    const int kap = (c >> 1) & 3, sig = (c & 1) + 2 * (c >> 3);
    const int ko = (16 * w + 4 * g + kap) * 4;
    float o = lds_ld<float>(L, L_OP + (2 * sig) * 512 + ko) + lds_ld<float>(L, L_OP + (2 * sig + 1) * 512 + ko);
    o += dppf<XOR1>(o); o += dppf<XOR8>(o);
    const unsigned P = cvtpk(dppf<QB0>(o), dppf<QB2>(o)), Q = dppu<XOR7>(P);
    const unsigned X0 = (c & 4) ? Q : P, X1 = (c & 4) ? P : Q;
    f4 y[5];
    { const v4u A = diagA(X0, S, 0);
#pragma unroll
      for (int cb = 0; cb < 5; ++cb) y[cb] = mfma(A, WO[cb], f4{0.f, 0.f, 0.f, 0.f}); }
    { const v4u A = diagA(X0, S, 1);
#pragma unroll
      for (int cb = 0; cb < 5; ++cb) y[cb] = mfma(A, WO[5 + cb], y[cb]); }
    { const v4u A = diagA(X1, S, 0);
#pragma unroll
      for (int cb = 0; cb < 5; ++cb) y[cb] = mfma(A, WO[10 + cb], y[cb]); }
    { const v4u A = diagA(X1, S, 1);
#pragma unroll
      for (int cb = 0; cb < 5; ++cb) y[cb] = mfma(A, WO[15 + cb], y[cb]); }
    if (lane < 32) {
#pragma unroll
      for (int cb = 0; cb < 5; ++cb) lds_st<f4>(L, ypoff + cb * 512, y[cb]);
    }
  }
  BAR();
  STAMP(8);
  auto publish = [&](int wsoff) AINL {
    const int k = t & 7, j = t >> 3;
    const int o = L_YP + (20 * j + 4 * (k < 5 ? k : 0)) * 4;
    f4 s0 = lds_ld<f4>(L, o) + lds_ld<f4>(L, o + 2560), s1 = lds_ld<f4>(L, o + 2 * 2560) + lds_ld<f4>(L, o + 3 * 2560);
    f4 s2 = lds_ld<f4>(L, o + 4 * 2560) + lds_ld<f4>(L, o + 5 * 2560), s3 = lds_ld<f4>(L, o + 6 * 2560) + lds_ld<f4>(L, o + 7 * 2560);
    stsc(rsWS, wsoff + t * 16, tag4((s0 + s1) + (s2 + s3), T));
  };
  float x1v[4] = {0.f, 0.f, 0.f, 0.f};
  auto rs_gather = [&](int wsbase, int nprod, int delay_done) AINL -> f4 {
    const int pl = lane >> 3, k = lane & 7;
    unsigned off[4]; v4u v[4];
#pragma unroll
    for (int q = 0; q < 4; ++q) {
      const int u = 8 * q + pl;
      const int wgu = (b << 5) | ((u & 3) << 3) | (u >> 2);
      off[q] = (k < 5 && u < nprod) ? wsbase + wgu * 4096 + h * 128 + k * 16 : OOB;
      PIN(off[q]);
    }
    const unsigned long long m0 = ballot(off[0] != OOB), m3 = ballot(off[3] != OOB);
    __builtin_amdgcn_s_sleep(0);
    (void)delay_done;
#pragma unroll
    for (int q = 0; q < 4; ++q) v[q] = ldsc(rsWS, off[q]);
    unsigned spins = 0;
    for (;;) {
      const bool k0 = tagok4(v[0], T), k1 = tagok4(v[1], T), k2 = tagok4(v[2], T), k3 = tagok4(v[3], T);
      if ((ballot(k0) & ballot(k1) & ballot(k2) & m0) == m0 && (ballot(k3) & m3) == m3) break;
      if (expired(spins)) break;
      __builtin_amdgcn_s_sleep(POLL_SLEEP);
      FENCE();
      const v4u n0 = ldsc(rsWS, k0 ? OOB : off[0]), n1 = ldsc(rsWS, k1 ? OOB : off[1]), n2 = ldsc(rsWS, k2 ? OOB : off[2]), n3 = ldsc(rsWS, k3 ? OOB : off[3]);
      v[0] = k0 ? v[0] : n0; v[1] = k1 ? v[1] : n1; v[2] = k2 ? v[2] : n2; v[3] = k3 ? v[3] : n3;
    }
    f4 s;
#pragma unroll
    for (int e = 0; e < 4; ++e) {
      float a = (__uint_as_float(v[0][e] & 0xffffff00u) + __uint_as_float(v[1][e] & 0xffffff00u)) + (__uint_as_float(v[2][e] & 0xffffff00u) + __uint_as_float(v[3][e] & 0xffffff00u));
      a += dppf<XOR8>(a); s[e] = x32_sum(x16_sum(a));
    }
    return s;
  };
  if (w < 4) publish(WS_OUT1 + wg * 4096);
  if (w != 4) {
#pragma unroll
    for (int i = 0; i < 10; ++i) W2[i] = ld<AUX_W>(rsW2, offW2, (i / 5) * 1280 + (i % 5) * 256);
  }
  if (w == 4) {
    const v2u xr = lds_ld<v2u>(L, L_XR + (lane & 7) * 8); const f4 gg = lds_ld<f4>(L, L_GAB + (lane & 7) * 16);
    unsigned off[3]; v4u v[3]; float sbv[12], cbv[12]; int hoff[3];
#pragma unroll
    for (int q = 0; q < 3; ++q) {
      const int idx = 64 * q + lane, jj = idx / 5, k = idx % 5;
      off[q] = (idx < 160 && mlp) ? WS_AG + (b * 32 + jj) * 128 + k * 16 : OOB;
      const int col = idx < 160 ? 20 * jj + 4 * k : 0;
      const v2u s2 = lds_ld<v2u>(L, L_SB + col * 2), c2 = lds_ld<v2u>(L, L_CB + col * 2);
      sbv[4 * q] = bflo(s2.x); sbv[4 * q + 1] = bfhi(s2.x); sbv[4 * q + 2] = bflo(s2.y); sbv[4 * q + 3] = bfhi(s2.y);
      cbv[4 * q] = bflo(c2.x); cbv[4 * q + 1] = bfhi(c2.x); cbv[4 * q + 2] = bflo(c2.y); cbv[4 * q + 3] = bfhi(c2.y);
      hoff[q] = L_H2 + col * 2;
      PIN(off[q]); PIN(hoff[q]);
    }
#pragma unroll
    for (int i = 0; i < 12; ++i) { PIN(sbv[i]); PIN(cbv[i]); }
    float xx[4] = {bflo(xr.x), bfhi(xr.x), bflo(xr.y), bfhi(xr.y)};
    const unsigned long long am0 = ballot(off[0] != OOB), am2 = ballot(off[2] != OOB);
    __builtin_amdgcn_s_sleep(D_RS1);
    const f4 s = rs_gather(WS_OUT1, 32, 0);
    v4u m;
    {
      float a0 = s[0], a1 = s[1], a2 = s[2], a3 = s[3];
      rbf2(a0, a1); rbf2(a2, a3);
      a0 *= gg[0]; a1 *= gg[1]; a2 *= gg[2]; a3 *= gg[3];
      rbf2(a0, a1); rbf2(a2, a3);
      a0 += xx[0]; a1 += xx[1]; a2 += xx[2]; a3 += xx[3];
      const unsigned p01 = cvtpk(a0, a1), p23 = cvtpk(a2, a3);
      m = v4u{(p01 << 16) | (T & 0xffu), (p01 & 0xffff0000u) | ((T >> 8) & 0xffu), (p23 << 16) | ((T >> 16) & 0xffu), (p23 & 0xffff0000u) | (T >> 24)};
      x1v[0] = bflo(p01); x1v[1] = bfhi(p01); x1v[2] = bflo(p23); x1v[3] = bfhi(p23);
    }
    if (lane < 5) stsc(rsWS, WS_AG + (b * 32 + h) * 128 + lane * 16, m);
#pragma unroll
    for (int i = 0; i < 10; ++i) W2[i] = ld<AUX_W>(rsW2, offW2, (i / 5) * 1280 + (i % 5) * 256);
    STAMP(9);
    if (mlp) {
      __builtin_amdgcn_s_sleep(D_AG);
#pragma unroll
      for (int q = 0; q < 3; ++q) v[q] = ldsc(rsWS, off[q]);
      unsigned spins = 0;
      for (;;) {
        const bool k0 = tagok4(v[0], T), k1 = tagok4(v[1], T), k2 = tagok4(v[2], T);
        if ((ballot(k0) & ballot(k1) & am0) == am0 && (ballot(k2) & am2) == am2) break;
        if (expired(spins)) break;
        __builtin_amdgcn_s_sleep(POLL_SLEEP);
        FENCE();
        const v4u n0 = ldsc(rsWS, k0 ? OOB : off[0]), n1 = ldsc(rsWS, k1 ? OOB : off[1]), n2 = ldsc(rsWS, k2 ? OOB : off[2]);
        v[0] = k0 ? v[0] : n0; v[1] = k1 ? v[1] : n1; v[2] = k2 ? v[2] : n2;
      }
      STAMP(13);
      float xv[12]; float sm = 0.f;
#pragma unroll
      for (int q = 0; q < 3; ++q)
#pragma unroll
        for (int e = 0; e < 4; ++e) { xv[4 * q + e] = __uint_as_float(v[q][e] & 0xffff0000u); sm += xv[4 * q + e]; }
      const float mean = wave_sum(sm) * (1.f / 640.f);
      float q2 = 0.f, q3 = 0.f;
#pragma unroll
      for (int i = 0; i < 8; ++i) { const float dd = xv[i] - mean; q2 += dd * dd; }
#pragma unroll
      for (int i = 8; i < 12; ++i) { const float dd = xv[i] - mean; q3 += dd * dd; }
      q2 += lane < 32 ? q3 : 0.f;
      const float var = rbf(wave_sum(q2) * (1.f / 640.f));
      const float mb = rbf(mean), rden = frcp(rbf(__builtin_amdgcn_sqrtf(rbf(var + 1e-5f))));
#pragma unroll
      for (int q = 0; q < 3; ++q) {
        float a[4];
#pragma unroll
        for (int e = 0; e < 4; ++e) a[e] = xv[4 * q + e] - mb;
        rbf2(a[0], a[1]); rbf2(a[2], a[3]);
#pragma unroll
        for (int e = 0; e < 4; ++e) a[e] *= rden;
        rbf2(a[0], a[1]); rbf2(a[2], a[3]);
#pragma unroll
        for (int e = 0; e < 4; ++e) a[e] *= cbv[4 * q + e];
        rbf2(a[0], a[1]); rbf2(a[2], a[3]);
        const v2u o = {cvtpk(a[0] + sbv[4 * q], a[1] + sbv[4 * q + 1]), cvtpk(a[2] + sbv[4 * q + 2], a[3] + sbv[4 * q + 3])};
        if (q < 2 || lane < 32) lds_st<v2u>(L, hoff[q], o);
      }
    }
  }
  if (mlp) {
    BAR();
    STAMP(10);
    {
      unsigned X[5];
#pragma unroll
      for (int d = 0; d < 5; ++d) X[d] = lds_ld<unsigned>(L, L_H2 + 160 * w + 80 * (c >> 3) + 20 * g + d * 4);
      f4 a0 = {0.f, 0.f, 0.f, 0.f}, a1 = {0.f, 0.f, 0.f, 0.f};
#pragma unroll
      for (int p = 0; p < 10; p += 2) { a0 = mfma(diagA(X[p >> 1], S, 0), W1[p], a0); a1 = mfma(diagA(X[p >> 1], S, 1), W1[p + 1], a1); }
      a0 += a1;
      if ((g >> 1) == (c >> 3)) lds_st<f4>(L, L_HP + (2 * w + (c >> 3)) * 256 + (8 * (c & 7) + 4 * (g & 1)) * 4, a0);
    }
    BAR();
    {
      const int kap = (c >> 2) & 1, sig = (c & 3) + 4 * (c >> 3);
      const int n = (8 * w + 2 * g + kap) * 4;
      float hd = lds_ld<float>(L, L_HP + (2 * sig) * 256 + n) + lds_ld<float>(L, L_HP + (2 * sig + 1) * 256 + n);
      hd += dppf<XOR1>(hd); hd += dppf<XOR2>(hd); hd += dppf<XOR8>(hd);
      hd = fsilu(rbf(hd));
      const float ot = dppf<XOR7>(hd);
      const unsigned X = kap ? cvtpk(ot, hd) : cvtpk(hd, ot);
      const v4u A0 = diagA(X, S, 0), A1 = diagA(X, S, 1);
      f4 y[5];
#pragma unroll
      for (int cb = 0; cb < 5; ++cb) y[cb] = mfma(A0, W2[cb], f4{0.f, 0.f, 0.f, 0.f});
#pragma unroll
      for (int cb = 0; cb < 5; ++cb) y[cb] = mfma(A1, W2[5 + cb], y[cb]);
      if (lane < 32) {
#pragma unroll
        for (int cb = 0; cb < 5; ++cb) lds_st<f4>(L, ypoff + cb * 512, y[cb]);
      }
    }
    BAR();
    STAMP(11);
    if (w < 4) publish(WS_OUT2 + wg * 4096);
  }
  if (w == 4) {
    const f4 gg = lds_ld<f4>(L, L_GAB + 80 + (lane & 7) * 16);
    if (!mlp) __builtin_amdgcn_s_sleep(48);
    __builtin_amdgcn_s_sleep(D_RS2);
    const f4 s = rs_gather(WS_OUT2, 27, 0);
    float a0 = s[0], a1 = s[1], a2 = s[2], a3 = s[3];
    rbf2(a0, a1); rbf2(a2, a3);
    a0 *= gg[0]; a1 *= gg[1]; a2 *= gg[2]; a3 *= gg[3];
    rbf2(a0, a1); rbf2(a2, a3);
    if (lane < 5) __builtin_amdgcn_raw_buffer_store_b64(v2u{cvtpk(x1v[0] + a0, x1v[1] + a1), cvtpk(x1v[2] + a2, x1v[3] + a3)}, rsOUT, b * 1280 + 40 * h + lane * 8, 0, AUX_SC1);
    STAMP(12);
  }
}

extern "C" void kernel_launch(void* const* d_in, const int* in_sizes, int n_in, void* d_out, int out_size, void* d_ws, size_t ws_size,
                              hipStream_t stream) {
  fused_kernel<<<256, 512, 0, stream>>>(d_in[0], d_in[1], d_in[2], d_in[3], d_in[4], d_in[6], d_in[7], d_in[8], d_in[9], d_out, d_ws);
}
```

```cpp
#include <hip/hip_runtime.h>
#include <hip/hip_bf16.h>
#include <stdint.h>
#include <type_traits>

typedef unsigned v4u __attribute__((ext_vector_type(4)));
typedef unsigned v2u __attribute__((ext_vector_type(2)));
typedef float f4 __attribute__((ext_vector_type(4)));
typedef __bf16 bf8 __attribute__((ext_vector_type(8)));
typedef __bf16 bf2 __attribute__((ext_vector_type(2)));
typedef short s4 __attribute__((ext_vector_type(4)));
#define LAS __attribute__((address_space(3)))
typedef __amdgpu_buffer_rsrc_t rsrc_t;

constexpr int AUX_K = 2;
constexpr int AUX_V = 2;
constexpr int AUX_W = 0;
constexpr int AUX_NT = 2, AUX_SC1 = 16;
constexpr unsigned OOB = 0x80000000u;

constexpr int WS_EPOCH = 0;
constexpr int WS_ALIVE = 4096;
constexpr int WS_XST = 16384;
constexpr int WS_AGP = 65536;
constexpr int WS_AG = 131072;
constexpr int WS_PM = 262144;
constexpr int WS_OUT1 = 1 << 20;
constexpr int WS_OUT2 = 2 << 20;
constexpr int WS_QPM = 3 << 20;
constexpr int WS_BYTES = 8 << 20;

constexpr int TW = 6;
constexpr int DA_LO = 1;
constexpr int DA_HI = 1;
constexpr int DB_LO = 1;
constexpr int DB_HI = 0;
constexpr int VRN = 14;
constexpr int VPRE = 9;
constexpr int NR = 16;
constexpr int RK = 12;
constexpr int RL = 19;
constexpr int HK = 19;
constexpr int HD = 2;
constexpr int HN = 3;
constexpr int QK = 25;
struct It { int kind, idx; };
constexpr int WOA = 6;
constexpr int NA = 3 + 32 + WOA + 8;
constexpr It a_item(int m) {
  int pos = 0, wn = 0;
  for (int k = 0; k < 32; ++k) {
    if (pos == m) return It{0, k};
    ++pos;
    if (k >= 4 && k < 7) { if (pos == m) return It{4, k - 4}; ++pos; }
    else if (k >= RK && k < RK + 4) { if (pos == m) return It{5, k - RK}; ++pos; }
    else if (k >= QK && k < QK + 4) { if (pos == m) return It{6, k - QK}; ++pos; }
    else if (k >= 7 && k < 31) {
      int idx = 0; for (int u = 7; u < k; ++u) if (!(u >= RK && u < RK + 4) && !(u >= QK && u < QK + 4)) ++idx;
      if (idx * WOA / 16 != (idx + 1) * WOA / 16 && wn < WOA) { if (pos == m) return It{2, wn}; ++pos; ++wn; }
    }
  }
  return It{-1, 0};
}
static_assert(a_item(NA - 1).kind == 0 && a_item(NA - 1).idx == 31, "phase A list");
constexpr int a_free_between(int lo, int hi) { int n = 0; for (int m = lo; m <= hi; ++m) if (m >= 0 && m < NA && a_item(m).kind == 6) ++n; return n; }
constexpr int a_kpos(int k) { for (int m = 0; m < NA; ++m) if (a_item(m).kind == 0 && a_item(m).idx == k) return m; return NA; }
constexpr int WEARLY = 2;
constexpr int WLAST = 27;
constexpr int NBV = 32 + 20 - WOA, NB = NBV + 10;
constexpr It b_item(int m) {
  if (m < VRN) return It{1, m};
  if (m < VRN + WEARLY) return It{2, WOA + m - VRN};
  if (m >= NBV) return It{3, m - NBV};
  int pos = VRN + WEARLY, wn = WOA + WEARLY;
  for (int v = VRN; v < 32; ++v) {
    if (pos == m) return It{1, v};
    ++pos;
    int target = WOA + WEARLY + ((20 - WOA - WEARLY) * (v - VRN + 1) + (WLAST - VRN)) / (WLAST - VRN + 1);
    if (target > 20) target = 20;
    while (wn < target) { if (pos == m) return It{2, wn}; ++pos; ++wn; }
  }
  return It{-1, 0};
}
static_assert(b_item(NBV - 1).kind == 1 && b_item(NBV - 1).idx == 31, "phase B list");
constexpr int b_vpos(int k) { for (int m = 0; m < NB; ++m) if (b_item(m).kind == 1 && b_item(m).idx == k) return m; return NB; }
constexpr int b_extra(int m) { int n = 0; const int p4 = b_vpos(4 + VRN), p8 = b_vpos(8 + VRN); if (m >= p4 && m - p4 < TW) n += 4; if (m >= p8 && m - p8 < TW) n += 2; return n; }
constexpr int a_lastpark_f() { int k = NR - 1; for (int m = 0; m + TW < NA; ++m) if (a_item(m).kind == 0 && a_item(m).idx > k) k = a_item(m).idx; return k; }
constexpr int LASTPARK = a_lastpark_f();
constexpr int NLDS = LASTPARK - NR + 1;


constexpr int L_WL = 0;
constexpr int L_SC = 20480;
constexpr int SC_STRIDE = 336;
constexpr int L_PART = L_SC + 2688;
constexpr int L_WQL = 0;
constexpr int L_QP8 = 20480;
constexpr int L_HB = 32768;
constexpr int L_SB = L_HB + 1280;
constexpr int L_CB = L_SB + 1280;
constexpr int L_GAB = L_CB + 1280;
constexpr int L_XR = L_GAB + 160;
constexpr int L_ML = L_XR + 64;
constexpr int L_FLAG = L_ML + 64;
constexpr int L_QB = L_FLAG + 32;
constexpr int L_H2 = L_QB + 256;
constexpr int L_OP = L_H2 + 1280;
constexpr int L_HP = L_OP + 4096;
constexpr int L_KS = L_HP + 4096;
constexpr int L_YP = L_KS;
constexpr int L_END = L_KS + (8 * NLDS * 1024 > 20480 ? 8 * NLDS * 1024 : 20480);
static_assert(L_QP8 + 8192 <= 32768 && L_PART + 4096 <= 32768 && L_HB % 16 == 0 && L_QB % 16 == 0 && L_OP % 16 == 0 && L_KS % 16 == 0 && L_GAB % 16 == 0 && L_H2 % 16 == 0 && L_END <= 163840, "lds map");

constexpr int D_RS1 = 8;
constexpr int D_AG = 10;
constexpr int D_RS2 = 8;
constexpr int POLL_SLEEP = 1;
constexpr int NI0 = 4;
constexpr unsigned long long WAIT_TICKS = 1000000000ull;

__device__ __forceinline__ float bflo(unsigned d) { return __uint_as_float(d << 16); }
__device__ __forceinline__ float bfhi(unsigned d) { return __uint_as_float(d & 0xffff0000u); }
__device__ __forceinline__ unsigned cvtpk(float lo, float hi) {
  bf2 r = {(__bf16)lo, (__bf16)hi}; return __builtin_bit_cast(unsigned, r);
}
__device__ __forceinline__ float rbf(float f) { return bflo(cvtpk(f, 0.f)); }
__device__ __forceinline__ void rbf2(float& a, float& b) { const unsigned p = cvtpk(a, b); a = bflo(p); b = bfhi(p); }
__device__ __forceinline__ float frcp(float x) { return __builtin_amdgcn_rcpf(x); }
__device__ __forceinline__ float fsilu(float a) { return a * frcp(1.f + __builtin_amdgcn_exp2f(a * -1.4426950408889634f)); }
template <int CTRL> __device__ __forceinline__ float dppf(float x) {
  return __builtin_bit_cast(float, __builtin_amdgcn_update_dpp(0, __builtin_bit_cast(int, x), CTRL, 0xf, 0xf, true));
}
template <int CTRL> __device__ __forceinline__ unsigned dppu(unsigned x) { return (unsigned)__builtin_amdgcn_update_dpp(0, (int)x, CTRL, 0xf, 0xf, true); }
constexpr int XOR1 = 0xB1, XOR2 = 0x4E, XOR7 = 0x141, XOR8 = 0x128, QB0 = 0x00, QB2 = 0xAA;
__device__ __forceinline__ float x16_sum(float x) { auto s = __builtin_amdgcn_permlane16_swap(__float_as_uint(x), __float_as_uint(x), false, false); return __uint_as_float(s[0]) + __uint_as_float(s[1]); }
__device__ __forceinline__ float x32_sum(float x) { auto s = __builtin_amdgcn_permlane32_swap(__float_as_uint(x), __float_as_uint(x), false, false); return __uint_as_float(s[0]) + __uint_as_float(s[1]); }
__device__ __forceinline__ float x16_max(float x) { auto s = __builtin_amdgcn_permlane16_swap(__float_as_uint(x), __float_as_uint(x), false, false); return fmaxf(__uint_as_float(s[0]), __uint_as_float(s[1])); }
__device__ __forceinline__ float x32_max(float x) { auto s = __builtin_amdgcn_permlane32_swap(__float_as_uint(x), __float_as_uint(x), false, false); return fmaxf(__uint_as_float(s[0]), __uint_as_float(s[1])); }
__device__ __forceinline__ float wave_sum(float x) {
  x += dppf<XOR1>(x); x += dppf<XOR2>(x); x += dppf<XOR7>(x); x += dppf<XOR8>(x); return x32_sum(x16_sum(x));
}
__device__ __forceinline__ float wave_max(float x) {
  x = fmaxf(x, dppf<XOR1>(x)); x = fmaxf(x, dppf<XOR2>(x)); x = fmaxf(x, dppf<XOR7>(x)); x = fmaxf(x, dppf<XOR8>(x)); return x32_max(x16_max(x));
}
__device__ __forceinline__ f4 mfma(v4u a, v4u b, f4 c) {
  return __builtin_amdgcn_mfma_f32_16x16x32_bf16(__builtin_bit_cast(bf8, a), __builtin_bit_cast(bf8, b), c, 0, 0, 0);
}
__device__ __forceinline__ float dot2(unsigned a, unsigned b, float c) {
  return __builtin_amdgcn_fdot2_f32_bf16(__builtin_bit_cast(bf2, a), __builtin_bit_cast(bf2, b), c, false);
}
__device__ __forceinline__ rsrc_t mkrs(const void* p, unsigned bytes) { return __builtin_amdgcn_make_buffer_rsrc((void*)p, 0, (int)bytes, 0x00020000); }
template <int AUX> __device__ __forceinline__ v4u ld(rsrc_t r, unsigned voff, unsigned soff) { return __builtin_amdgcn_raw_buffer_load_b128(r, (int)voff, (int)soff, AUX); }
__device__ __forceinline__ v4u ldsc(rsrc_t r, unsigned voff) { return __builtin_amdgcn_raw_buffer_load_b128(r, (int)voff, 0, AUX_SC1); }
__device__ __forceinline__ void stsc(rsrc_t r, unsigned voff, v4u v) { __builtin_amdgcn_raw_buffer_store_b128(v, r, (int)voff, 0, AUX_SC1); }
#define WAITVM(n) asm volatile("s_waitcnt vmcnt(%0)" ::"n"((n) > 63 ? 63 : (n)) : "memory")
#define WAITW(n, lo, hi) do { if ((lo) == (hi)) WAITVM((n) - (lo)); else if (w < 4) WAITVM((n) - (lo)); else WAITVM((n) - (hi)); } while (0)
#define BAR() asm volatile("s_waitcnt lgkmcnt(0)\n\ts_barrier" ::: "memory")
#define SB() __builtin_amdgcn_sched_barrier(0)
#define FENCE() asm volatile("" ::: "memory")
#define PIN(x) asm volatile("" : "+v"(x))

template <typename T> __device__ __forceinline__ T lds_ld(LAS unsigned char* L, int off) { return *(LAS T*)(L + off); }
template <typename T> __device__ __forceinline__ void lds_st(LAS unsigned char* L, int off, T v) { *(LAS T*)(L + off) = v; }
template <int I, int N, typename F> __device__ __forceinline__ void sfor(F&& f) {
  if constexpr (I < N) { f(std::integral_constant<int, I>{}); sfor<I + 1, N>(f); }
}
template <int I> using IC = std::integral_constant<int, I>;
#define AINL __attribute__((always_inline))

struct Sel { unsigned s[2][4]; };
__device__ __forceinline__ v4u diagA(unsigned X, const Sel& S, int par) {
  v4u a;
#pragma unroll
  for (int d = 0; d < 4; ++d) a[d] = __builtin_amdgcn_perm(X, X, S.s[par][d]);
  return a;
}
__device__ __forceinline__ v4u tag4(f4 v, unsigned T) {
  v4u r;
#pragma unroll
  for (int e = 0; e < 4; ++e) r[e] = ((__float_as_uint(v[e]) + 0x80u) & 0xffffff00u) | ((T >> (8 * e)) & 0xffu);
  return r;
}
__device__ __forceinline__ bool tagok4(v4u v, unsigned T) {
  const unsigned lo = __builtin_amdgcn_perm(v.y, v.x, 0x0c0c0400u), hi = __builtin_amdgcn_perm(v.w, v.z, 0x04000c0cu);
  return (lo | hi) == T;
}
__device__ __forceinline__ unsigned long long ballot(bool p) { return __builtin_amdgcn_ballot_w64(p); }


__global__ void __launch_bounds__(512, 2)
fused_kernel(const void* px, const void* pk, const void* pv, const void* pwq,
             const void* pwo, const void* pw1, const void* pw2, const void* pc,
             const void* pwm, void* pout, void* pws) {
  __shared__ __attribute__((aligned(16))) unsigned char smem[L_END];
  LAS unsigned char* L = (LAS unsigned char*)smem;
  const int t = threadIdx.x, lane = t & 63, w = __builtin_amdgcn_readfirstlane(t >> 6);
  const int wg = blockIdx.x, b = (wg >> 2) & 7, h = (wg >> 5) * 4 + (wg & 3);
  const int g = lane >> 4, c = lane & 15;
  const bool mlp = h < 27;
  asm volatile("" :: "s"(px), "s"(pk), "s"(pv), "s"(pwq), "s"(pwo), "s"(pw1), "s"(pw2), "s"(pc), "s"(pwm), "s"(pout), "s"(pws));
  const unsigned long long t_start = __builtin_amdgcn_s_memrealtime();
  auto expired = [&](unsigned& spins) AINL { return (++spins & 255u) == 0u && __builtin_amdgcn_s_memrealtime() - t_start > WAIT_TICKS; };
  const rsrc_t rsWS = mkrs(pws, WS_BYTES);
  const rsrc_t rsK = mkrs((const char*)pk + ((size_t)(b * 32 + h) * 4096 + 3072) * 256, 262144);
  const rsrc_t rsV = mkrs((const char*)pv + ((size_t)(b * 32 + h) * 4096 + 3072) * 256, 262144);
  const rsrc_t rsWQ = mkrs(pwq, 640 * 4096 * 2), rsWO = mkrs(pwo, 4096 * 640 * 2), rsW1 = mkrs(pw1, 640 * 1728 * 2), rsW2 = mkrs(pw2, 1728 * 640 * 2);
  const rsrc_t rsWM = mkrs(pwm, 640 * 3840 * 2), rsC = mkrs(pc, 10240), rsX = mkrs(px, 10240), rsOUT = mkrs(pout, 10240);

  unsigned po[4]; v4u pvv[4];
  v4u WQ[3], WO[20], KB[NR], KL[TW + 1];
  const unsigned offWQ = (unsigned)(80 * b + 20 * g + w) * 8192u + h * 256 + c * 16;
  const unsigned offWO = (unsigned)(h * 128 + 16 * w + 4 * g) * 1280u + c * 16;
  const unsigned offKV = w * 1024 + lane * 16;
  const int ksoff = L_KS + w * (NLDS * 1024) + lane * 16;
  auto ldwo = [&](auto ic) AINL { constexpr int i = decltype(ic)::value; WO[i] = ld<AUX_W>(rsWO, offWO, (i / 5) * 1280 + (i % 5) * 256); };
  auto a_issue = [&](auto mc) AINL {
    constexpr It it = a_item(decltype(mc)::value);
    if constexpr (it.kind == 4) WQ[it.idx] = ld<AUX_NT>(rsWQ, (it.idx < 2 || w < 4) ? offWQ : OOB, it.idx * 8 * 8192);
    else if constexpr (it.kind == 2) ldwo(IC<it.idx>{});
    else if constexpr (it.kind == 5 || it.kind == 6) pvv[it.idx] = ldsc(rsWS, po[it.idx]);
    else if constexpr (it.idx < NR) KB[it.idx] = ld<AUX_K>(rsK, offKV, it.idx * 8192);
    else KL[(it.idx - NR) % (TW + 1)] = ld<AUX_K>(rsK, offKV, it.idx * 8192);
  };
  constexpr int NINIT0 = NI0;
  sfor<0, NINIT0>(a_issue);
  SB();
  const bool role = wg < 240;
  const int ta = wg % 60, kq = wg / 60;
  v4u cv0 = ld<AUX_NT>(rsC, (role && t < 160) ? (t / 20) * 1280 + kq * 320 + (t % 20) * 16 : OOB, 0);
  const unsigned wmoff = (unsigned)(160 * kq + 8 * w + (lane >> 3)) * 7680u + ta * 128 + (lane & 7) * 16;
  v4u wm0 = ld<AUX_NT>(rsWM, role ? wmoff : OOB, 0);
  v4u wm1 = ld<AUX_NT>(rsWM, role ? wmoff : OOB, 64 * 7680);
  v4u wm2 = ld<AUX_NT>(rsWM, (role && w < 4) ? wmoff : OOB, 128 * 7680);
  unsigned Eraw = 0;
  if (w == 0) Eraw = __builtin_amdgcn_raw_buffer_load_b32(rsWS, WS_EPOCH, 0, AUX_SC1);
  unsigned pg0 = 0, pg1 = 0, pg2 = 0, pg3 = 0, pg4 = 0, pg5 = 0;
  if (w == 7) {
    pg0 = __builtin_amdgcn_raw_buffer_load_b32(rsV, lane < 2 ? lane * 262140 : OOB, 0, AUX_NT);
    pg1 = __builtin_amdgcn_raw_buffer_load_b32(rsWO, lane < 2 ? h * 163840 + lane * 163836 : OOB, 0, AUX_W);
    pg2 = __builtin_amdgcn_raw_buffer_load_b32(rsW1, lane < 8 ? lane * 80 * 3456 + h * 128 : OOB, 0, AUX_W);
    pg3 = __builtin_amdgcn_raw_buffer_load_b32(rsW2, lane < 2 ? h * 81920 + lane * 81916 : OOB, 0, AUX_W);
    pg4 = __builtin_amdgcn_raw_buffer_load_b32(rsWS, lane < 16 ? lane * 262144 + 1024 : OOB, 0, AUX_SC1);
    pg5 = __builtin_amdgcn_raw_buffer_load_b32(rsOUT, lane == 0 ? b * 1280 : OOB, 0, AUX_SC1);
  }
  if (t < 8) lds_st<unsigned>(L, L_FLAG + t * 4, 0u);
  SB();
  constexpr int NINIT1 = NI0 + 2, NINIT2 = NI0 + 3, NINIT3 = NI0 + 5, NINIT = NI0 + 6;
  static_assert(NINIT >= TW && a_item(NINIT - 1).idx < NR && NINIT - TW <= 5, "no parking during P1; the wq pieces (positions 5, 7, 9) are parked in the main loop");
  if (w == 7) WAITVM(6); else if (w == 0) WAITVM(1); else WAITVM(0);
  sfor<NINIT0, NINIT1>(a_issue);
  SB();
  if (w == 0 && lane == 0) lds_st<unsigned>(L, L_ML, Eraw + 1u);
  if (wg >= 240 && wg < 248 && w == 0) {
    const unsigned T = (unsigned)__builtin_amdgcn_readfirstlane((int)Eraw) + 1u;
    const v4u xa = ld<AUX_NT>(rsX, (wg - 240) * 1280 + lane * 16, 0);
    const unsigned xb = __builtin_amdgcn_raw_buffer_load_b32(rsX, (wg - 240) * 1280 + 1024 + lane * 4, 0, AUX_NT);
    float xv[10];
#pragma unroll
    for (int d = 0; d < 4; ++d) { xv[2 * d] = bflo(xa[d]); xv[2 * d + 1] = bfhi(xa[d]); }
    xv[8] = bflo(xb); xv[9] = bfhi(xb);
    const float sm = ((xv[0] + xv[1]) + (xv[2] + xv[3])) + ((xv[4] + xv[5]) + (xv[6] + xv[7])) + (xv[8] + xv[9]);
    const float mean = wave_sum(sm) * (1.f / 640.f);
    float q2 = 0.f;
#pragma unroll
    for (int d = 0; d < 10; ++d) { const float dd = xv[d] - mean; q2 = fmaf(dd, dd, q2); }
    const float var = rbf(wave_sum(q2) * (1.f / 640.f));
    const float mb = rbf(mean), den = rbf(__builtin_amdgcn_sqrtf(rbf(var + 1e-5f)));
    if (lane == 0) stsc(rsWS, WS_XST + (wg - 240) * 16, v4u{__float_as_uint(mb) | (T & 0xffu), __float_as_uint(den) | ((T >> 8) & 0xffu), (T >> 16) & 0xffu, T >> 24});
  }
  {
    auto silu8 = [&](v4u v) AINL {
      v4u r;
#pragma unroll
      for (int d = 0; d < 4; ++d) r[d] = cvtpk(fsilu(bflo(v[d])), fsilu(bfhi(v[d])));
      return r;
    };
    if (t < 160) lds_st<v4u>(L, L_SC + (t / 20) * SC_STRIDE + (t % 20) * 16, silu8(cv0));
    lds_st<v4u>(L, L_WL + w * 1024 + lane * 16, wm0);
    lds_st<v4u>(L, L_WL + (w + 8) * 1024 + lane * 16, wm1);
    if (w < 4) lds_st<v4u>(L, L_WL + (w + 16) * 1024 + lane * 16, wm2);
  }
  SB();
  sfor<NINIT1, NINIT2>(a_issue);
  v2u xh = __builtin_amdgcn_raw_buffer_load_b64(rsX, lane < 20 ? w * 1280 + 160 * b + lane * 8 : OOB, 0, AUX_NT);
  v2u xown = __builtin_amdgcn_raw_buffer_load_b64(rsX, (w == 4 && lane < 5) ? b * 1280 + 40 * h + lane * 8 : OOB, 0, AUX_NT);
  SB();
  BAR();
  const unsigned T = (unsigned)__builtin_amdgcn_readfirstlane((int)lds_ld<unsigned>(L, L_ML));
  sfor<NINIT2, NINIT3>([&](auto mc) AINL { WAITVM(TW - 2 + 2); a_issue(mc); SB(); });
  {
    f4 acc = {0.f, 0.f, 0.f, 0.f};
    const int tl = w & 3, s0_ = (w >> 2) * 3;
    const int trbase = L_WL + (8 * g + ((lane & 15) >> 2)) * 128 + tl * 32 + (lane & 3) * 8;
#pragma unroll
    for (int n = 0; n < 3; ++n) {
      if (n < 2 || w < 4) {
        const int s = s0_ + n;
        const v4u a = lds_ld<v4u>(L, L_SC + (c & 7) * SC_STRIDE + s * 64 + g * 16);
        const s4 b0 = __builtin_amdgcn_ds_read_tr16_b64_v4i16((LAS s4*)(L + trbase + s * 4096));
        const s4 b1 = __builtin_amdgcn_ds_read_tr16_b64_v4i16((LAS s4*)(L + trbase + s * 4096 + 512));
        const v2u u0 = __builtin_bit_cast(v2u, b0), u1 = __builtin_bit_cast(v2u, b1);
        acc = mfma(a, v4u{u0.x, u0.y, u1.x, u1.y}, acc);
      }
    }
    if (g < 2) {
#pragma unroll
      for (int r = 0; r < 4; ++r) lds_st<float>(L, L_PART + (w >> 2) * 2048 + (4 * g + r) * 256 + (16 * tl + c) * 4, acc[r]);
    }
  }
  sfor<NINIT3, NINIT>([&](auto mc) AINL { WAITVM(TW - 2 + 2); a_issue(mc); SB(); });
  BAR();
  {
    const f4 s = lds_ld<f4>(L, L_PART + (t & 127) * 16) + lds_ld<f4>(L, L_PART + 2048 + (t & 127) * 16);
    unsigned o = OOB;
    if (t < 128) { if (role) o = WS_PM + (((t >> 4) * 4 + kq) * 3840 + ta * 64) * 4 + (t & 15) * 16; }
    else if (t == 128) o = WS_ALIVE + wg * 16;
    stsc(rsWS, o, tag4(s, T));
  }
  Sel S;
  {
    const int e = c & 7;
#pragma unroll
    for (int par = 0; par < 2; ++par)
#pragma unroll
      for (int d = 0; d < 4; ++d) {
        const unsigned lo = (unsigned)(2 * par) | ((unsigned)(2 * par + 1) << 8);
        S.s[par][d] = (d == (e >> 1)) ? ((e & 1) ? (0x0c0cu | (lo << 16)) : (0x0c0c0000u | lo)) : 0x0c0c0c0cu;
      }
  }
  unsigned long long pm0, pm1, pm2, pm3;
  auto pmasks = [&]() AINL { pm0 = ballot(po[0] != OOB); pm1 = ballot(po[1] != OOB); pm2 = ballot(po[2] != OOB); pm3 = ballot(po[3] != OOB); };
#pragma unroll
  for (int q = 0; q < 4; ++q) po[q] = lane < 40 ? WS_PM + ((w * 4 + q) * 3840 + (lane / 20) * 640 + 80 * b + 4 * (lane % 20)) * 4 : OOB;
  if (lane == 40) po[0] = WS_XST + w * 16;
  pmasks();
  auto pissue = [&]() AINL {
#pragma unroll
    for (int q = 0; q < 4; ++q) pvv[q] = ldsc(rsWS, po[q]);
  };
  auto pok = [&]() AINL {
    return (ballot(tagok4(pvv[0], T)) & pm0) == pm0 && (ballot(tagok4(pvv[1], T)) & pm1) == pm1 && (ballot(tagok4(pvv[2], T)) & pm2) == pm2 && (ballot(tagok4(pvv[3], T)) & pm3) == pm3;
  };
  auto preload = [&]() AINL {
#pragma unroll
    for (int q = 0; q < 4; ++q) { const bool k1 = tagok4(pvv[q], T); const v4u nv = ldsc(rsWS, k1 ? OOB : po[q]); pvv[q] = k1 ? pvv[q] : nv; }
  };
  auto pblock = [&]() AINL {
    unsigned spins = 0;
    while (!pok() && !expired(spins)) { __builtin_amdgcn_s_sleep(2); FENCE(); preload(); }
  };
  auto psum = [&]() AINL {
    f4 mv;
#pragma unroll
    for (int e = 0; e < 4; ++e) mv[e] = (__uint_as_float(pvv[0][e] & 0xffffff00u) + __uint_as_float(pvv[1][e] & 0xffffff00u)) +
                                        (__uint_as_float(pvv[2][e] & 0xffffff00u) + __uint_as_float(pvv[3][e] & 0xffffff00u));
    float a0 = mv[0], a1 = mv[1], a2 = mv[2], a3 = mv[3]; rbf2(a0, a1); rbf2(a2, a3);
    return f4{a0, a1, a2, a3};
  };
  bool hdone = false;
  auto rprocess = [&]() AINL {
    const f4 mv = psum();
    const float xmean = __uint_as_float((unsigned)__builtin_amdgcn_readlane((int)pvv[0].x, 40) & 0xffff0000u);
    const float xrden = frcp(__uint_as_float((unsigned)__builtin_amdgcn_readlane((int)pvv[0].y, 40) & 0xffff0000u));
    float lnv[4] = {bflo(xh.x) - xmean, bfhi(xh.x) - xmean, bflo(xh.y) - xmean, bfhi(xh.y) - xmean};
    rbf2(lnv[0], lnv[1]); rbf2(lnv[2], lnv[3]);
#pragma unroll
    for (int e = 0; e < 4; ++e) lnv[e] *= xrden;
    rbf2(lnv[0], lnv[1]); rbf2(lnv[2], lnv[3]);
    float hv[4];
#pragma unroll
    for (int e = 0; e < 4; ++e) {
      const float ca = __uint_as_float(__builtin_amdgcn_ds_bpermute(((lane + 20) & 63) * 4, (int)__float_as_uint(mv[e])));
      hv[e] = rbf(lnv[e] * rbf(1.f + ca)) + mv[e];
    }
    if (lane < 20) lds_st<v2u>(L, L_HB + w * 160 + lane * 8, v2u{cvtpk(hv[0], hv[1]), cvtpk(hv[2], hv[3])});
    asm volatile("s_waitcnt lgkmcnt(0)" ::: "memory");
    if (lane == 0) __hip_atomic_fetch_add((LAS unsigned*)(L + L_FLAG), 1u, __ATOMIC_RELAXED, __HIP_MEMORY_SCOPE_WORKGROUP);
  };
  auto mprocess = [&]() AINL {
    const f4 mv = psum();
    v4u msg;
#pragma unroll
    for (int e = 0; e < 4; ++e) msg[e] = (__float_as_uint(rbf(lane >= 10 ? 1.f + mv[e] : mv[e])) & 0xffff0000u) | ((T >> (8 * e)) & 0xffu);
    if (lane >= 5 && lane < 15) stsc(rsWS, WS_AGP + (b * 32 + h) * 256 + lane * 16, msg);
    else if (lane < 5) lds_st<f4>(L, L_GAB + lane * 16, mv);
    else if (lane < 20) lds_st<f4>(L, L_GAB + 80 + (lane - 15) * 16, mv);
  };
  auto bfpack = [&](v4u v) AINL { return v2u{__builtin_amdgcn_perm(v.y, v.x, 0x07060302u), __builtin_amdgcn_perm(v.w, v.z, 0x07060302u)}; };
  auto hflag = [&]() AINL { FENCE(); return __builtin_amdgcn_readfirstlane((int)*(volatile LAS unsigned*)(L + L_FLAG)) == 8; };
  auto hstep = [&]() AINL {
    FENCE();
    const int pi = w & 3, hf = w >> 2;
    unsigned X[5]; v4u Bp[10];
#pragma unroll
    for (int d = 0; d < 5; ++d) X[d] = lds_ld<unsigned>(L, L_HB + (2 * pi + (c >> 3)) * 160 + 40 * g + 20 * hf + d * 4);
#pragma unroll
    for (int i = 0; i < 10; ++i) Bp[i] = lds_ld<v4u>(L, L_WQL + (10 * hf + i) * 1024 + lane * 16);
    f4 a0 = {0.f, 0.f, 0.f, 0.f}, a1 = {0.f, 0.f, 0.f, 0.f};
#pragma unroll
    for (int i = 0; i < 10; i += 2) { a0 = mfma(diagA(X[i >> 1], S, 0), Bp[i], a0); a1 = mfma(diagA(X[i >> 1], S, 1), Bp[i + 1], a1); }
    a0 += a1;
    lds_st<f4>(L, L_QP8 + w * 1024 + lane * 16, a0);
    asm volatile("s_waitcnt lgkmcnt(0)" ::: "memory");
    unsigned old = 0;
    if (lane == 0) old = __hip_atomic_fetch_add((LAS unsigned*)(L + L_FLAG + 4 + pi * 4), 1u, __ATOMIC_RELAXED, __HIP_MEMORY_SCOPE_WORKGROUP);
    if (__builtin_amdgcn_readfirstlane((int)old) == 1) {
      FENCE();
      const f4 lo = lds_ld<f4>(L, L_QP8 + pi * 1024 + lane * 16), hi = lds_ld<f4>(L, L_QP8 + (pi + 4) * 1024 + lane * 16);
      stsc(rsWS, WS_QPM + (h * 8 + b) * 4096 + (2 * pi + (g >> 1)) * 512 + (2 * c + (g & 1)) * 16, tag4(lo + hi, T));
    }
  };
  static_assert(a_kpos(RL) >= a_kpos(6) + 1 + TW, "wq parked before the first h arrival");
  static_assert(a_kpos(RL) >= a_kpos(RK + 3) + 1 + TW - 1 && RL <= HK && HK + (HN - 1) * HD < QK - 1 && RL < QK - 1 && QK + 3 < 31, "stage order");
  sfor<NINIT, NA>([&](auto mc) AINL {
    constexpr int m = decltype(mc)::value;
    constexpr It it = a_item(m);
    constexpr int k = it.kind == 0 ? it.idx : -100;
    WAITW(TW - 1 + (m - (TW - DA_LO) < NINIT ? 1 : 0) + a_free_between(m - (TW - DA_LO), m - 1), DA_LO, DA_HI);
    {
      constexpr It ol = a_item(m - TW);
      if constexpr (ol.kind == 4) { if (ol.idx < 2 || w < 4) lds_st<v4u>(L, L_WQL + (w + 8 * ol.idx) * 1024 + lane * 16, WQ[ol.idx]); }
      if constexpr (ol.kind == 0 && ol.idx >= NR) lds_st<v4u>(L, ksoff + (ol.idx - NR) * 1024, KL[(ol.idx - NR) % (TW + 1)]);
    }
    a_issue(mc);
    if constexpr (k == 9) { if (w == 4 && lane < 5) lds_st<v2u>(L, L_XR + lane * 8, xown); }
    if constexpr (k == 10) asm volatile("" :: "v"(pg0), "v"(pg1), "v"(pg2), "v"(pg3), "v"(pg4), "v"(pg5));
    if constexpr (k == RL) { pblock(); rprocess(); }
    if constexpr (k >= HK && k < HK + (HN - 1) * HD && (k - HK) % HD == 0) { if (!hdone && hflag()) { hstep(); hdone = true; } }
    if constexpr (k == HK + (HN - 1) * HD) {
      if (!hdone) { unsigned spins = 0; while (!hflag() && !expired(spins)) __builtin_amdgcn_s_sleep(1); hstep(); }
    }
    if constexpr (k == QK - 1) {
#pragma unroll
      for (int q = 0; q < 4; ++q) {
        unsigned o = OOB;
        if (w == 5) { if (lane < 20) o = WS_PM + ((b * 4 + q) * 3840 + (2 + lane / 5) * 640 + 20 * h + 4 * (lane % 5)) * 4; }
        else if (w == 6) o = WS_QPM + ((h * 8 + 2 * q + (lane >> 5)) * 8 + b) * 512 + (lane & 31) * 16;
        else if (w == 4 && wg == 0) o = WS_ALIVE + (64 * q + lane) * 16;
        po[q] = o;
      }
      pmasks();
    }
    SB();
  });
  v4u W1[10], W2[10], VR[VRN];
  const unsigned offW1 = mlp ? (unsigned)(80 * w + 40 * (c >> 3) + 10 * g) * 3456u + (64 * h + 8 * (c & 7)) * 2 : OOB;
  const unsigned offW2 = mlp ? (unsigned)(64 * h + 8 * w + 2 * g) * 1280u + c * 16 : OOB;
  auto b_issue = [&](auto mc) AINL {
    constexpr It it = b_item(decltype(mc)::value);
    if constexpr (it.kind == 1) VR[it.idx % VRN] = ld<AUX_V>(rsV, offKV, it.idx * 8192);
    else if constexpr (it.kind == 2) ldwo(IC<it.idx>{});
    else W1[it.idx] = ld<AUX_W>(rsW1, offW1, it.idx * 3456);
    SB();
  };
#define VTICK(k) do { WAITW(TW - 1, DB_LO, DB_HI); b_issue(IC<(k)>{}); } while (0)
  sfor<0, VPRE>([&](auto kc) AINL { WAITW(TW - 1 + a_free_between(NA + decltype(kc)::value - (TW - DB_LO), NA - 1), DB_LO, DB_HI); b_issue(kc); });
  if (w == 5) { pblock(); mprocess(); }
  if ((w == 6 || w == 4)) {
    pblock();
    if (w == 6) {
      f4 q4;
#pragma unroll
      for (int e = 0; e < 4; ++e) q4[e] = x32_sum((__uint_as_float(pvv[0][e] & 0xffffff00u) + __uint_as_float(pvv[1][e] & 0xffffff00u)) +
                                                  (__uint_as_float(pvv[2][e] & 0xffffff00u) + __uint_as_float(pvv[3][e] & 0xffffff00u)));
      if (lane < 32) lds_st<v2u>(L, L_QB + lane * 8, v2u{cvtpk(q4[0], q4[1]), cvtpk(q4[2], q4[3])});
    } else {
      if (wg == 0 && lane == 0) __builtin_amdgcn_raw_buffer_store_b32(T, rsWS, WS_EPOCH, 0, AUX_SC1);
    }
  }
  BAR();
  const v4u qr = lds_ld<v4u>(L, L_QB + c * 16);
  auto kdot = [&](v4u k) AINL { return dot2(k.w, qr[3], dot2(k.z, qr[2], dot2(k.y, qr[1], dot2(k.x, qr[0], 0.f)))); };
  auto treduce = [&](float (&xs)[16]) AINL {
    float x[8];
#pragma unroll
    for (int i = 0; i < 8; ++i) { const float lo = xs[i] + dppf<XOR8>(xs[i]), hi = xs[i + 8] + dppf<XOR8>(xs[i + 8]); x[i] = (c & 8) ? hi : lo; }
#pragma unroll
    for (int i = 0; i < 4; ++i) { const float lo = x[i] + dppf<XOR7>(x[i]), hi = x[i + 4] + dppf<XOR7>(x[i + 4]); x[i] = (c & 4) ? hi : lo; }
#pragma unroll
    for (int i = 0; i < 2; ++i) { const float lo = x[i] + dppf<XOR2>(x[i]), hi = x[i + 2] + dppf<XOR2>(x[i + 2]); x[i] = (c & 2) ? hi : lo; }
    const float lo = x[0] + dppf<XOR1>(x[0]), hi = x[1] + dppf<XOR1>(x[1]);
    return (c & 1) ? hi : lo;
  };
  constexpr float C2 = 0.08838834764831845f * 1.4426950408889634f;
  float mw, fw = 0.f;
  unsigned ppk;
  {
    float xs[16];
#pragma unroll
    for (int j = 0; j < 16; ++j) xs[j] = kdot(KB[j]);
    SB();
    VTICK(VPRE);
    const float s0 = treduce(xs);
    SB();
    VTICK(VPRE + 1);
#pragma unroll
    for (int j = 0; j <= LASTPARK - NR; ++j) xs[j] = kdot(lds_ld<v4u>(L, ksoff + j * 1024));
#pragma unroll
    for (int j = LASTPARK - NR + 1; j < 16; ++j) xs[j] = kdot(KL[j % (TW + 1)]);
    SB();
    sfor<VPRE + 2, (VPRE + 4 < VRN + WEARLY ? VPRE + 4 : VRN + WEARLY)>([&](auto kc) AINL { WAITW(TW - 1, DB_LO, DB_HI); b_issue(kc); });
    const float s1 = treduce(xs);
    SB();
    sfor<(VPRE + 4 < VRN + WEARLY ? VPRE + 4 : VRN + WEARLY), VRN + WEARLY>([&](auto kc) AINL { WAITW(TW - 1, DB_LO, DB_HI); b_issue(kc); });
    mw = wave_max(fmaxf(s0, s1));
    const float p0 = __builtin_amdgcn_exp2f((s0 - mw) * C2), p1 = __builtin_amdgcn_exp2f((s1 - mw) * C2);
    const float lw = wave_sum(p0 + p1);
    ppk = cvtpk(p0, p1);
    if (lane == 0) lds_st<v2u>(L, L_ML + w * 8, v2u{__float_as_uint(mw), __float_as_uint(lw)});
  }
  f4 oacc = {0.f, 0.f, 0.f, 0.f};
  unsigned touch0 = 0, touch1 = 0;
  auto pvstep = [&](auto jc) AINL {
    constexpr int j = decltype(jc)::value;
    const unsigned X = dppu<0x150 + (j & 15)>(ppk);
    oacc = mfma(diagA(X, S, j >> 4), VR[j % VRN], oacc);
    if constexpr (j == 4) {
#pragma unroll
      for (int q = 0; q < 4; ++q) { const int idx = 64 * q + lane; po[q] = ((w == 1 || w == 2) && mlp && idx < 160) ? WS_AGP + (b * 32 + idx / 5) * 256 + (5 * w + idx % 5) * 16 : OOB; }
      pmasks(); pissue();
    }
    if constexpr (j == 14) {
      if ((w == 1 || w == 2)) {
        pblock();
#pragma unroll
        for (int n = 0; n < 3; ++n) {
          const int idx = 64 * n + lane;
          if (po[n] != OOB) lds_st<v2u>(L, (w == 1 ? L_SB : L_CB) + (20 * (idx / 5) + 4 * (idx % 5)) * 2, bfpack(pvv[n]));
        }
      }
    }
    if constexpr (j == 8) {
      unsigned o0 = OOB, o1 = OOB;
      if (w == 4) {
        const int u = lane & 31; const int wgu = ((u >> 2) << 5) | (b << 2) | (u & 3);
        o0 = (lane < 32 ? WS_OUT1 : WS_OUT2) + wgu * 4096 + h * 128;
        o1 = lane < 32 ? WS_AG + (b * 32 + lane) * 128 : OOB;
      } else if (w == 0) { o0 = (lane < 32 ? WS_OUT1 : WS_OUT2) + wg * 4096 + (lane & 31) * 128; }
      touch0 = __builtin_amdgcn_raw_buffer_load_b32(rsWS, (int)o0, 0, AUX_SC1);
      touch1 = __builtin_amdgcn_raw_buffer_load_b32(rsWS, (int)o1, 0, AUX_SC1);
    }
    if constexpr (j == 16) { asm volatile("" :: "v"(touch0), "v"(touch1)); }
    if constexpr (j == 10) {
      BAR();
      float M = -INFINITY;
      v2u ml[8];
#pragma unroll
      for (int ww = 0; ww < 8; ++ww) { ml[ww] = lds_ld<v2u>(L, L_ML + ww * 8); M = fmaxf(M, __uint_as_float(ml[ww].x)); }
      float Ls = 0.f;
#pragma unroll
      for (int ww = 0; ww < 8; ++ww) Ls += __builtin_amdgcn_exp2f((__uint_as_float(ml[ww].x) - M) * C2) * __uint_as_float(ml[ww].y);
      fw = __builtin_amdgcn_exp2f((mw - M) * C2) * frcp(Ls);
      PIN(fw);
    }
  };
  sfor<VRN + WEARLY, NB>([&](auto mc) AINL {
    constexpr int m = decltype(mc)::value;
    constexpr It it = b_item(m);
    if constexpr (it.kind == 1) pvstep(IC<it.idx - VRN>{});
    if constexpr (m == NBV) sfor<32 - VRN, 22>([&](auto jc) AINL { pvstep(jc); SB(); });
    if constexpr (it.kind != 3) WAITW(TW - 1 + b_extra(m), DB_LO, DB_HI);
    else { pvstep(IC<22 + it.idx>{}); }
    b_issue(mc);
  });
  {
    const f4 ov = oacc * fw;
    if (lane < 32) lds_st<f4>(L, L_OP + w * 512 + (8 * c + 4 * g) * 4, ov);
  }
  BAR();
  const int ypoff = L_YP + w * 2560 + (8 * c + 4 * (g & 1)) * 4;
  {
    const int kap = (c >> 1) & 3, sig = (c & 1) + 2 * (c >> 3);
    const int ko = (16 * w + 4 * g + kap) * 4;
    float o = lds_ld<float>(L, L_OP + (2 * sig) * 512 + ko) + lds_ld<float>(L, L_OP + (2 * sig + 1) * 512 + ko);
    o += dppf<XOR1>(o); o += dppf<XOR8>(o);
    const unsigned P = cvtpk(dppf<QB0>(o), dppf<QB2>(o)), Q = dppu<XOR7>(P);
    const unsigned X0 = (c & 4) ? Q : P, X1 = (c & 4) ? P : Q;
    f4 y[5];
    { const v4u A = diagA(X0, S, 0);
#pragma unroll
      for (int cb = 0; cb < 5; ++cb) y[cb] = mfma(A, WO[cb], f4{0.f, 0.f, 0.f, 0.f}); }
    { const v4u A = diagA(X0, S, 1);
#pragma unroll
      for (int cb = 0; cb < 5; ++cb) y[cb] = mfma(A, WO[5 + cb], y[cb]); }
    { const v4u A = diagA(X1, S, 0);
#pragma unroll
      for (int cb = 0; cb < 5; ++cb) y[cb] = mfma(A, WO[10 + cb], y[cb]); }
    { const v4u A = diagA(X1, S, 1);
#pragma unroll
      for (int cb = 0; cb < 5; ++cb) y[cb] = mfma(A, WO[15 + cb], y[cb]); }
    if (lane < 32) {
#pragma unroll
      for (int cb = 0; cb < 5; ++cb) lds_st<f4>(L, ypoff + cb * 512, y[cb]);
    }
  }
  BAR();
  auto publish = [&](int wsoff) AINL {
    const int k = t & 7, j = t >> 3;
    const int o = L_YP + (20 * j + 4 * (k < 5 ? k : 0)) * 4;
    f4 s0 = lds_ld<f4>(L, o) + lds_ld<f4>(L, o + 2560), s1 = lds_ld<f4>(L, o + 2 * 2560) + lds_ld<f4>(L, o + 3 * 2560);
    f4 s2 = lds_ld<f4>(L, o + 4 * 2560) + lds_ld<f4>(L, o + 5 * 2560), s3 = lds_ld<f4>(L, o + 6 * 2560) + lds_ld<f4>(L, o + 7 * 2560);
    stsc(rsWS, wsoff + t * 16, tag4((s0 + s1) + (s2 + s3), T));
  };
  float x1v[4] = {0.f, 0.f, 0.f, 0.f};
  auto rs_gather = [&](int wsbase, int nprod) AINL -> f4 {
    const int pl = lane >> 3, k = lane & 7;
    unsigned off[4]; v4u v[4];
#pragma unroll
    for (int q = 0; q < 4; ++q) {
      const int u = 8 * q + pl;
      const int wgu = ((u >> 2) << 5) | (b << 2) | (u & 3);
      off[q] = (k < 5 && u < nprod) ? wsbase + wgu * 4096 + h * 128 + k * 16 : OOB;
      PIN(off[q]);
    }
    const unsigned long long m0 = ballot(off[0] != OOB), m3 = ballot(off[3] != OOB);
#pragma unroll
    for (int q = 0; q < 4; ++q) v[q] = ldsc(rsWS, off[q]);
    unsigned spins = 0;
    for (;;) {
      const bool k0 = tagok4(v[0], T), k1 = tagok4(v[1], T), k2 = tagok4(v[2], T), k3 = tagok4(v[3], T);
      if ((ballot(k0) & ballot(k1) & ballot(k2) & m0) == m0 && (ballot(k3) & m3) == m3) break;
      if (expired(spins)) break;
      __builtin_amdgcn_s_sleep(POLL_SLEEP);
      FENCE();
      const v4u n0 = ldsc(rsWS, k0 ? OOB : off[0]), n1 = ldsc(rsWS, k1 ? OOB : off[1]), n2 = ldsc(rsWS, k2 ? OOB : off[2]), n3 = ldsc(rsWS, k3 ? OOB : off[3]);
      v[0] = k0 ? v[0] : n0; v[1] = k1 ? v[1] : n1; v[2] = k2 ? v[2] : n2; v[3] = k3 ? v[3] : n3;
    }
    f4 s;
#pragma unroll
    for (int e = 0; e < 4; ++e) {
      float a = (__uint_as_float(v[0][e] & 0xffffff00u) + __uint_as_float(v[1][e] & 0xffffff00u)) + (__uint_as_float(v[2][e] & 0xffffff00u) + __uint_as_float(v[3][e] & 0xffffff00u));
      a += dppf<XOR8>(a); s[e] = x32_sum(x16_sum(a));
    }
    return s;
  };
  if (w < 4) publish(WS_OUT1 + wg * 4096);
  if (w != 4) {
#pragma unroll
    for (int i = 0; i < 10; ++i) W2[i] = ld<AUX_W>(rsW2, offW2, (i / 5) * 1280 + (i % 5) * 256);
  }
  if (w == 4) {
    const v2u xr = lds_ld<v2u>(L, L_XR + (lane & 7) * 8); const f4 gg = lds_ld<f4>(L, L_GAB + (lane & 7) * 16);
    unsigned off[3]; v4u v[3]; float sbv[12], cbv[12]; int hoff[3];
#pragma unroll
    for (int q = 0; q < 3; ++q) {
      const int idx = 64 * q + lane, jj = idx / 5, k = idx % 5;
      off[q] = (idx < 160 && mlp) ? WS_AG + (b * 32 + jj) * 128 + k * 16 : OOB;
      const int col = idx < 160 ? 20 * jj + 4 * k : 0;
      const v2u s2 = lds_ld<v2u>(L, L_SB + col * 2), c2 = lds_ld<v2u>(L, L_CB + col * 2);
      sbv[4 * q] = bflo(s2.x); sbv[4 * q + 1] = bfhi(s2.x); sbv[4 * q + 2] = bflo(s2.y); sbv[4 * q + 3] = bfhi(s2.y);
      cbv[4 * q] = bflo(c2.x); cbv[4 * q + 1] = bfhi(c2.x); cbv[4 * q + 2] = bflo(c2.y); cbv[4 * q + 3] = bfhi(c2.y);
      hoff[q] = L_H2 + col * 2;
      PIN(off[q]); PIN(hoff[q]);
    }
#pragma unroll
    for (int i = 0; i < 12; ++i) { PIN(sbv[i]); PIN(cbv[i]); }
    float xx[4] = {bflo(xr.x), bfhi(xr.x), bflo(xr.y), bfhi(xr.y)};
    const unsigned long long am0 = ballot(off[0] != OOB), am2 = ballot(off[2] != OOB);
    __builtin_amdgcn_s_sleep(D_RS1);
    const f4 s = rs_gather(WS_OUT1, 32);
    v4u m;
    {
      float a0 = s[0], a1 = s[1], a2 = s[2], a3 = s[3];
      rbf2(a0, a1); rbf2(a2, a3);
      a0 *= gg[0]; a1 *= gg[1]; a2 *= gg[2]; a3 *= gg[3];
      rbf2(a0, a1); rbf2(a2, a3);
      a0 += xx[0]; a1 += xx[1]; a2 += xx[2]; a3 += xx[3];
      const unsigned p01 = cvtpk(a0, a1), p23 = cvtpk(a2, a3);
      m = v4u{(p01 << 16) | (T & 0xffu), (p01 & 0xffff0000u) | ((T >> 8) & 0xffu), (p23 << 16) | ((T >> 16) & 0xffu), (p23 & 0xffff0000u) | (T >> 24)};
      x1v[0] = bflo(p01); x1v[1] = bfhi(p01); x1v[2] = bflo(p23); x1v[3] = bfhi(p23);
    }
    if (lane < 5) stsc(rsWS, WS_AG + (b * 32 + h) * 128 + lane * 16, m);
#pragma unroll
    for (int i = 0; i < 10; ++i) W2[i] = ld<AUX_W>(rsW2, offW2, (i / 5) * 1280 + (i % 5) * 256);
    if (mlp) {
      __builtin_amdgcn_s_sleep(D_AG);
#pragma unroll
      for (int q = 0; q < 3; ++q) v[q] = ldsc(rsWS, off[q]);
      unsigned spins = 0;
      for (;;) {
        const bool k0 = tagok4(v[0], T), k1 = tagok4(v[1], T), k2 = tagok4(v[2], T);
        if ((ballot(k0) & ballot(k1) & am0) == am0 && (ballot(k2) & am2) == am2) break;
        if (expired(spins)) break;
        __builtin_amdgcn_s_sleep(POLL_SLEEP);
        FENCE();
        const v4u n0 = ldsc(rsWS, k0 ? OOB : off[0]), n1 = ldsc(rsWS, k1 ? OOB : off[1]), n2 = ldsc(rsWS, k2 ? OOB : off[2]);
        v[0] = k0 ? v[0] : n0; v[1] = k1 ? v[1] : n1; v[2] = k2 ? v[2] : n2;
      }
      float xv[12]; float sm = 0.f;
#pragma unroll
      for (int q = 0; q < 3; ++q)
#pragma unroll
        for (int e = 0; e < 4; ++e) { xv[4 * q + e] = __uint_as_float(v[q][e] & 0xffff0000u); sm += xv[4 * q + e]; }
      const float mean = wave_sum(sm) * (1.f / 640.f);
      float q2 = 0.f, q3 = 0.f;
#pragma unroll
      for (int i = 0; i < 8; ++i) { const float dd = xv[i] - mean; q2 += dd * dd; }
#pragma unroll
      for (int i = 8; i < 12; ++i) { const float dd = xv[i] - mean; q3 += dd * dd; }
      q2 += lane < 32 ? q3 : 0.f;
      const float var = rbf(wave_sum(q2) * (1.f / 640.f));
      const float mb = rbf(mean), rden = frcp(rbf(__builtin_amdgcn_sqrtf(rbf(var + 1e-5f))));
#pragma unroll
      for (int q = 0; q < 3; ++q) {
        float a[4];
#pragma unroll
        for (int e = 0; e < 4; ++e) a[e] = xv[4 * q + e] - mb;
        rbf2(a[0], a[1]); rbf2(a[2], a[3]);
#pragma unroll
        for (int e = 0; e < 4; ++e) a[e] *= rden;
        rbf2(a[0], a[1]); rbf2(a[2], a[3]);
#pragma unroll
        for (int e = 0; e < 4; ++e) a[e] *= cbv[4 * q + e];
        rbf2(a[0], a[1]); rbf2(a[2], a[3]);
        const v2u o = {cvtpk(a[0] + sbv[4 * q], a[1] + sbv[4 * q + 1]), cvtpk(a[2] + sbv[4 * q + 2], a[3] + sbv[4 * q + 3])};
        if (q < 2 || lane < 32) lds_st<v2u>(L, hoff[q], o);
      }
    }
  }
  if (mlp) {
    BAR();
    {
      unsigned X[5];
#pragma unroll
      for (int d = 0; d < 5; ++d) X[d] = lds_ld<unsigned>(L, L_H2 + 160 * w + 80 * (c >> 3) + 20 * g + d * 4);
      f4 a0 = {0.f, 0.f, 0.f, 0.f}, a1 = {0.f, 0.f, 0.f, 0.f};
#pragma unroll
      for (int p = 0; p < 10; p += 2) { a0 = mfma(diagA(X[p >> 1], S, 0), W1[p], a0); a1 = mfma(diagA(X[p >> 1], S, 1), W1[p + 1], a1); }
      a0 += a1;
      if ((g >> 1) == (c >> 3)) lds_st<f4>(L, L_HP + (2 * w + (c >> 3)) * 256 + (8 * (c & 7) + 4 * (g & 1)) * 4, a0);
    }
    BAR();
    {
      const int kap = (c >> 2) & 1, sig = (c & 3) + 4 * (c >> 3);
      const int n = (8 * w + 2 * g + kap) * 4;
      float hd = lds_ld<float>(L, L_HP + (2 * sig) * 256 + n) + lds_ld<float>(L, L_HP + (2 * sig + 1) * 256 + n);
      hd += dppf<XOR1>(hd); hd += dppf<XOR2>(hd); hd += dppf<XOR8>(hd);
      hd = fsilu(rbf(hd));
      const float ot = dppf<XOR7>(hd);
      const unsigned X = kap ? cvtpk(ot, hd) : cvtpk(hd, ot);
      const v4u A0 = diagA(X, S, 0), A1 = diagA(X, S, 1);
      f4 y[5];
#pragma unroll
      for (int cb = 0; cb < 5; ++cb) y[cb] = mfma(A0, W2[cb], f4{0.f, 0.f, 0.f, 0.f});
#pragma unroll
      for (int cb = 0; cb < 5; ++cb) y[cb] = mfma(A1, W2[5 + cb], y[cb]);
      if (lane < 32) {
#pragma unroll
        for (int cb = 0; cb < 5; ++cb) lds_st<f4>(L, ypoff + cb * 512, y[cb]);
      }
    }
    BAR();
    if (w < 4) publish(WS_OUT2 + wg * 4096);
  }
  if (w == 4) {
    const f4 gg = lds_ld<f4>(L, L_GAB + 80 + (lane & 7) * 16);
    if (!mlp) __builtin_amdgcn_s_sleep(48);
    __builtin_amdgcn_s_sleep(D_RS2);
    const f4 s = rs_gather(WS_OUT2, 27);
    float a0 = s[0], a1 = s[1], a2 = s[2], a3 = s[3];
    rbf2(a0, a1); rbf2(a2, a3);
    a0 *= gg[0]; a1 *= gg[1]; a2 *= gg[2]; a3 *= gg[3];
    rbf2(a0, a1); rbf2(a2, a3);
    if (lane < 5) __builtin_amdgcn_raw_buffer_store_b64(v2u{cvtpk(x1v[0] + a0, x1v[1] + a1), cvtpk(x1v[2] + a2, x1v[3] + a3)}, rsOUT, b * 1280 + 40 * h + lane * 8, 0, AUX_SC1);
  }
}

extern "C" void kernel_launch(void* const* d_in, const int* in_sizes, int n_in, void* d_out, int out_size, void* d_ws, size_t ws_size,
                              hipStream_t stream) {
  fused_kernel<<<256, 512, 0, stream>>>(d_in[0], d_in[1], d_in[2], d_in[3], d_in[4], d_in[6], d_in[7], d_in[8], d_in[9], d_out, d_ws);
}
```

```cpp
#include <hip/hip_runtime.h>
#include <hip/hip_bf16.h>
#include <stdint.h>
#include <type_traits>

typedef unsigned v4u __attribute__((ext_vector_type(4)));
typedef unsigned v2u __attribute__((ext_vector_type(2)));
typedef float f4 __attribute__((ext_vector_type(4)));
typedef __bf16 bf8 __attribute__((ext_vector_type(8)));
typedef __bf16 bf2 __attribute__((ext_vector_type(2)));
typedef short s4 __attribute__((ext_vector_type(4)));
#define LAS __attribute__((address_space(3)))
typedef __amdgpu_buffer_rsrc_t rsrc_t;

constexpr int AUX_K = 2;
constexpr int AUX_V = 2;
constexpr int AUX_W = 0;
constexpr int AUX_NT = 2, AUX_SC1 = 16;
constexpr unsigned OOB = 0x80000000u;

constexpr int WS_EPOCH = 0;
constexpr int WS_ALIVE = 4096;
constexpr int WS_XST = 16384;
constexpr int WS_AGP = 65536;
constexpr int WS_AG = 131072;
constexpr int WS_PM = 262144;
constexpr int WS_OUT1 = 1 << 20;
constexpr int WS_OUT2 = 2 << 20;
constexpr int WS_QPM = 3 << 20;
constexpr int WS_BYTES = 8 << 20;

constexpr int TW = 6;
constexpr int DA_LO = 1;
constexpr int DA_HI = 1;
constexpr int DB_LO = 1;
constexpr int DB_HI = 0;
constexpr int VRN = 14;
constexpr int VPRE = 9;
constexpr int NR = 16;
constexpr int RK = 12;
constexpr int RL = 19;
constexpr int HK = 19;
constexpr int HD = 2;
constexpr int HN = 3;
constexpr int QK = 25;
struct It { int kind, idx; };
constexpr int WOA = 6;
constexpr int NA = 3 + 32 + WOA + 8;
constexpr It a_item(int m) {
  int pos = 0, wn = 0;
  for (int k = 0; k < 32; ++k) {
    if (pos == m) return It{0, k};
    ++pos;
    if (k >= 4 && k < 7) { if (pos == m) return It{4, k - 4}; ++pos; }
    else if (k >= RK && k < RK + 4) { if (pos == m) return It{5, k - RK}; ++pos; }
    else if (k >= QK && k < QK + 4) { if (pos == m) return It{6, k - QK}; ++pos; }
    else if (k >= 7 && k < 31) {
      int idx = 0; for (int u = 7; u < k; ++u) if (!(u >= RK && u < RK + 4) && !(u >= QK && u < QK + 4)) ++idx;
      if (idx * WOA / 16 != (idx + 1) * WOA / 16 && wn < WOA) { if (pos == m) return It{2, wn}; ++pos; ++wn; }
    }
  }
  return It{-1, 0};
}
static_assert(a_item(NA - 1).kind == 0 && a_item(NA - 1).idx == 31, "phase A list");
constexpr int a_free_between(int lo, int hi) { int n = 0; for (int m = lo; m <= hi; ++m) if (m >= 0 && m < NA && a_item(m).kind == 6) ++n; return n; }
constexpr int a_kpos(int k) { for (int m = 0; m < NA; ++m) if (a_item(m).kind == 0 && a_item(m).idx == k) return m; return NA; }
constexpr int WEARLY = 2;
constexpr int WLAST = 27;
constexpr int NBV = 32 + 20 - WOA, NB = NBV + 10;
constexpr It b_item(int m) {
  if (m < VRN) return It{1, m};
  if (m < VRN + WEARLY) return It{2, WOA + m - VRN};
  if (m >= NBV) return It{3, m - NBV};
  int pos = VRN + WEARLY, wn = WOA + WEARLY;
  for (int v = VRN; v < 32; ++v) {
    if (pos == m) return It{1, v};
    ++pos;
    int target = WOA + WEARLY + ((20 - WOA - WEARLY) * (v - VRN + 1) + (WLAST - VRN)) / (WLAST - VRN + 1);
    if (target > 20) target = 20;
    while (wn < target) { if (pos == m) return It{2, wn}; ++pos; ++wn; }
  }
  return It{-1, 0};
}
static_assert(b_item(NBV - 1).kind == 1 && b_item(NBV - 1).idx == 31, "phase B list");
constexpr int b_vpos(int k) { for (int m = 0; m < NB; ++m) if (b_item(m).kind == 1 && b_item(m).idx == k) return m; return NB; }
constexpr int b_extra(int m) { int n = 0; const int p4 = b_vpos(4 + VRN), p8 = b_vpos(8 + VRN); if (m >= p4 && m - p4 < TW) n += 4; if (m >= p8 && m - p8 < TW) n += 2; return n; }
constexpr int a_lastpark_f() { int k = NR - 1; for (int m = 0; m + TW < NA; ++m) if (a_item(m).kind == 0 && a_item(m).idx > k) k = a_item(m).idx; return k; }
constexpr int LASTPARK = a_lastpark_f();
constexpr int NLDS = LASTPARK - NR + 1;


constexpr int L_WL = 0;
constexpr int L_SC = 20480;
constexpr int SC_STRIDE = 336;
constexpr int L_PART = L_SC + 2688;
constexpr int L_WQL = 0;
constexpr int L_QP8 = 20480;
constexpr int L_HB = 32768;
constexpr int L_SB = L_HB + 1280;
constexpr int L_CB = L_SB + 1280;
constexpr int L_GAB = L_CB + 1280;
constexpr int L_XR = L_GAB + 160;
constexpr int L_ML = L_XR + 64;
constexpr int L_FLAG = L_ML + 64;
constexpr int L_QB = L_FLAG + 32;
constexpr int L_H2 = L_QB + 256;
constexpr int L_OP = L_H2 + 1280;
constexpr int L_HP = L_OP + 4096;
constexpr int L_KS = L_HP + 4096;
constexpr int L_YP = L_KS;
constexpr int L_END = L_KS + (8 * NLDS * 1024 > 20480 ? 8 * NLDS * 1024 : 20480);
static_assert(L_QP8 + 8192 <= 32768 && L_PART + 4096 <= 32768 && L_HB % 16 == 0 && L_QB % 16 == 0 && L_OP % 16 == 0 && L_KS % 16 == 0 && L_GAB % 16 == 0 && L_H2 % 16 == 0 && L_END <= 163840, "lds map");

constexpr int D_RS1 = 8;
constexpr int D_AG = 10;
constexpr int D_RS2 = 8;
constexpr int POLL_SLEEP = 1;
constexpr int NI0 = 4;
constexpr unsigned long long WAIT_TICKS = 1000000000ull;

__device__ __forceinline__ float bflo(unsigned d) { return __uint_as_float(d << 16); }
__device__ __forceinline__ float bfhi(unsigned d) { return __uint_as_float(d & 0xffff0000u); }
__device__ __forceinline__ unsigned cvtpk(float lo, float hi) {
  bf2 r = {(__bf16)lo, (__bf16)hi}; return __builtin_bit_cast(unsigned, r);
}
__device__ __forceinline__ float rbf(float f) { return bflo(cvtpk(f, 0.f)); }
__device__ __forceinline__ void rbf2(float& a, float& b) { const unsigned p = cvtpk(a, b); a = bflo(p); b = bfhi(p); }
__device__ __forceinline__ float frcp(float x) { return __builtin_amdgcn_rcpf(x); }
__device__ __forceinline__ float fsilu(float a) { return a * frcp(1.f + __builtin_amdgcn_exp2f(a * -1.4426950408889634f)); }
template <int CTRL> __device__ __forceinline__ float dppf(float x) {
  return __builtin_bit_cast(float, __builtin_amdgcn_update_dpp(0, __builtin_bit_cast(int, x), CTRL, 0xf, 0xf, true));
}
template <int CTRL> __device__ __forceinline__ unsigned dppu(unsigned x) { return (unsigned)__builtin_amdgcn_update_dpp(0, (int)x, CTRL, 0xf, 0xf, true); }
constexpr int XOR1 = 0xB1, XOR2 = 0x4E, XOR7 = 0x141, XOR8 = 0x128, QB0 = 0x00, QB2 = 0xAA;
__device__ __forceinline__ float x16_sum(float x) { auto s = __builtin_amdgcn_permlane16_swap(__float_as_uint(x), __float_as_uint(x), false, false); return __uint_as_float(s[0]) + __uint_as_float(s[1]); }
__device__ __forceinline__ float x32_sum(float x) { auto s = __builtin_amdgcn_permlane32_swap(__float_as_uint(x), __float_as_uint(x), false, false); return __uint_as_float(s[0]) + __uint_as_float(s[1]); }
__device__ __forceinline__ float x16_max(float x) { auto s = __builtin_amdgcn_permlane16_swap(__float_as_uint(x), __float_as_uint(x), false, false); return fmaxf(__uint_as_float(s[0]), __uint_as_float(s[1])); }
__device__ __forceinline__ float x32_max(float x) { auto s = __builtin_amdgcn_permlane32_swap(__float_as_uint(x), __float_as_uint(x), false, false); return fmaxf(__uint_as_float(s[0]), __uint_as_float(s[1])); }
__device__ __forceinline__ float wave_sum(float x) {
  x += dppf<XOR1>(x); x += dppf<XOR2>(x); x += dppf<XOR7>(x); x += dppf<XOR8>(x); return x32_sum(x16_sum(x));
}
__device__ __forceinline__ float wave_max(float x) {
  x = fmaxf(x, dppf<XOR1>(x)); x = fmaxf(x, dppf<XOR2>(x)); x = fmaxf(x, dppf<XOR7>(x)); x = fmaxf(x, dppf<XOR8>(x)); return x32_max(x16_max(x));
}
__device__ __forceinline__ f4 mfma(v4u a, v4u b, f4 c) {
  return __builtin_amdgcn_mfma_f32_16x16x32_bf16(__builtin_bit_cast(bf8, a), __builtin_bit_cast(bf8, b), c, 0, 0, 0);
}
__device__ __forceinline__ float dot2(unsigned a, unsigned b, float c) {
  return __builtin_amdgcn_fdot2_f32_bf16(__builtin_bit_cast(bf2, a), __builtin_bit_cast(bf2, b), c, false);
}
__device__ __forceinline__ rsrc_t mkrs(const void* p, unsigned bytes) { return __builtin_amdgcn_make_buffer_rsrc((void*)p, 0, (int)bytes, 0x00020000); }
template <int AUX> __device__ __forceinline__ v4u ld(rsrc_t r, unsigned voff, unsigned soff) { return __builtin_amdgcn_raw_buffer_load_b128(r, (int)voff, (int)soff, AUX); }
__device__ __forceinline__ v4u ldsc(rsrc_t r, unsigned voff) { return __builtin_amdgcn_raw_buffer_load_b128(r, (int)voff, 0, AUX_SC1); }
__device__ __forceinline__ void stsc(rsrc_t r, unsigned voff, v4u v) { __builtin_amdgcn_raw_buffer_store_b128(v, r, (int)voff, 0, AUX_SC1); }
#define WAITVM(n) asm volatile("s_waitcnt vmcnt(%0)" ::"n"((n) > 63 ? 63 : (n)) : "memory")
#define WAITW(n, lo, hi) do { if ((lo) == (hi)) WAITVM((n) - (lo)); else if (w < 4) WAITVM((n) - (lo)); else WAITVM((n) - (hi)); } while (0)
#define BAR() asm volatile("s_waitcnt lgkmcnt(0)\n\ts_barrier" ::: "memory")
#define SB() __builtin_amdgcn_sched_barrier(0)
#define FENCE() asm volatile("" ::: "memory")
#define PIN(x) asm volatile("" : "+v"(x))

template <typename T> __device__ __forceinline__ T lds_ld(LAS unsigned char* L, int off) { return *(LAS T*)(L + off); }
template <typename T> __device__ __forceinline__ void lds_st(LAS unsigned char* L, int off, T v) { *(LAS T*)(L + off) = v; }
template <int I, int N, typename F> __device__ __forceinline__ void sfor(F&& f) {
  if constexpr (I < N) { f(std::integral_constant<int, I>{}); sfor<I + 1, N>(f); }
}
template <int I> using IC = std::integral_constant<int, I>;
#define AINL __attribute__((always_inline))

struct Sel { unsigned s[2][4]; };
__device__ __forceinline__ v4u diagA(unsigned X, const Sel& S, int par) {
  v4u a;
#pragma unroll
  for (int d = 0; d < 4; ++d) a[d] = __builtin_amdgcn_perm(X, X, S.s[par][d]);
  return a;
}
__device__ __forceinline__ v4u tag4(f4 v, unsigned T) {
  v4u r;
#pragma unroll
  for (int e = 0; e < 4; ++e) r[e] = ((__float_as_uint(v[e]) + 0x80u) & 0xffffff00u) | ((T >> (8 * e)) & 0xffu);
  return r;
}
__device__ __forceinline__ bool tagok4(v4u v, unsigned T) {
  const unsigned lo = __builtin_amdgcn_perm(v.y, v.x, 0x0c0c0400u), hi = __builtin_amdgcn_perm(v.w, v.z, 0x04000c0cu);
  return (lo | hi) == T;
}
__device__ __forceinline__ unsigned long long ballot(bool p) { return __builtin_amdgcn_ballot_w64(p); }


__global__ void __launch_bounds__(512, 2)
fused_kernel(const void* px, const void* pk, const void* pv, const void* pwq,
             const void* pwo, const void* pw1, const void* pw2, const void* pc,
             const void* pwm, void* pout, void* pws) {
  __shared__ __attribute__((aligned(16))) unsigned char smem[L_END];
  LAS unsigned char* L = (LAS unsigned char*)smem;
  const int t = threadIdx.x, lane = t & 63, w = __builtin_amdgcn_readfirstlane(t >> 6);
  const int wg = blockIdx.x, b = (wg >> 2) & 7, h = (wg >> 5) * 4 + (wg & 3);
  const int g = lane >> 4, c = lane & 15;
  const bool mlp = h < 27;
  asm volatile("" :: "s"(px), "s"(pk), "s"(pv), "s"(pwq), "s"(pwo), "s"(pw1), "s"(pw2), "s"(pc), "s"(pwm), "s"(pout), "s"(pws));
  const unsigned long long t_start = __builtin_amdgcn_s_memrealtime();
  auto expired = [&](unsigned& spins) AINL { return (++spins & 255u) == 0u && __builtin_amdgcn_s_memrealtime() - t_start > WAIT_TICKS; };
  const rsrc_t rsWS = mkrs(pws, WS_BYTES);
  const rsrc_t rsK = mkrs((const char*)pk + ((size_t)(b * 32 + h) * 4096 + 3072) * 256, 262144);
  const rsrc_t rsV = mkrs((const char*)pv + ((size_t)(b * 32 + h) * 4096 + 3072) * 256, 262144);
  const rsrc_t rsWQ = mkrs(pwq, 640 * 4096 * 2), rsWO = mkrs(pwo, 4096 * 640 * 2), rsW1 = mkrs(pw1, 640 * 1728 * 2), rsW2 = mkrs(pw2, 1728 * 640 * 2);
  const rsrc_t rsWM = mkrs(pwm, 640 * 3840 * 2), rsC = mkrs(pc, 10240), rsX = mkrs(px, 10240), rsOUT = mkrs(pout, 10240);

  unsigned po[4]; v4u pvv[4];
  v4u WQ[3], WO[20], KB[NR], KL[TW + 1];
  const unsigned offWQ = (unsigned)(80 * b + 20 * g + w) * 8192u + h * 256 + c * 16;
  const unsigned offWO = (unsigned)(h * 128 + 16 * w + 4 * g) * 1280u + c * 16;
  const unsigned offKV = w * 1024 + lane * 16;
  const int ksoff = L_KS + w * (NLDS * 1024) + lane * 16;
  auto ldwo = [&](auto ic) AINL { constexpr int i = decltype(ic)::value; WO[i] = ld<AUX_W>(rsWO, offWO, (i / 5) * 1280 + (i % 5) * 256); };
  auto a_issue = [&](auto mc) AINL {
    constexpr It it = a_item(decltype(mc)::value);
    if constexpr (it.kind == 4) WQ[it.idx] = ld<AUX_NT>(rsWQ, (it.idx < 2 || w < 4) ? offWQ : OOB, it.idx * 8 * 8192);
    else if constexpr (it.kind == 2) ldwo(IC<it.idx>{});
    else if constexpr (it.kind == 5 || it.kind == 6) pvv[it.idx] = ldsc(rsWS, po[it.idx]);
    else if constexpr (it.idx < NR) KB[it.idx] = ld<AUX_K>(rsK, offKV, it.idx * 8192);
    else KL[(it.idx - NR) % (TW + 1)] = ld<AUX_K>(rsK, offKV, it.idx * 8192);
  };
  constexpr int NINIT0 = NI0;
  sfor<0, NINIT0>(a_issue);
  SB();
  const bool role = wg < 240;
  const int ta = wg % 60, kq = wg / 60;
  v4u cv0 = ld<AUX_NT>(rsC, (role && t < 160) ? (t / 20) * 1280 + kq * 320 + (t % 20) * 16 : OOB, 0);
  const unsigned wmoff = (unsigned)(160 * kq + 8 * w + (lane >> 3)) * 7680u + ta * 128 + (lane & 7) * 16;
  v4u wm0 = ld<AUX_NT>(rsWM, role ? wmoff : OOB, 0);
  v4u wm1 = ld<AUX_NT>(rsWM, role ? wmoff : OOB, 64 * 7680);
  v4u wm2 = ld<AUX_NT>(rsWM, (role && w < 4) ? wmoff : OOB, 128 * 7680);
  unsigned Eraw = 0;
  if (w == 0) Eraw = __builtin_amdgcn_raw_buffer_load_b32(rsWS, WS_EPOCH, 0, AUX_SC1);
  unsigned pg0 = 0, pg1 = 0, pg2 = 0, pg3 = 0, pg4 = 0, pg5 = 0, pg6 = 0, pg7 = 0;
  if (w == 7) {
    pg0 = __builtin_amdgcn_raw_buffer_load_b32(rsV, lane < 2 ? lane * 262140 : OOB, 0, AUX_NT);
    pg1 = __builtin_amdgcn_raw_buffer_load_b32(rsWO, lane < 2 ? h * 163840 + lane * 163836 : OOB, 0, AUX_W);
    pg2 = __builtin_amdgcn_raw_buffer_load_b32(rsW1, lane < 8 ? lane * 80 * 3456 + h * 128 : OOB, 0, AUX_W);
    pg3 = __builtin_amdgcn_raw_buffer_load_b32(rsW2, lane < 2 ? h * 81920 + lane * 81916 : OOB, 0, AUX_W);
    pg4 = __builtin_amdgcn_raw_buffer_load_b32(rsWS, lane < 16 ? lane * 262144 + 1024 : OOB, 0, AUX_SC1);
    pg5 = __builtin_amdgcn_raw_buffer_load_b32(rsOUT, lane == 0 ? b * 1280 : OOB, 0, AUX_SC1);
    pg6 = __builtin_amdgcn_raw_buffer_load_b32(rsX, lane == 0 ? b * 1280 : OOB, 0, AUX_NT);
    pg7 = __builtin_amdgcn_raw_buffer_load_b32(rsWQ, lane < 2 ? (80 * b + 79 * lane) * 8192 + h * 256 : OOB, 0, AUX_NT);
  }
  if (t < 8) lds_st<unsigned>(L, L_FLAG + t * 4, 0u);
  SB();
  constexpr int NINIT1 = NI0 + 2, NINIT2 = NI0 + 3, NINIT3 = NI0 + 5, NINIT = NI0 + 6;
  static_assert(NINIT >= TW && a_item(NINIT - 1).idx < NR && NINIT - TW <= 5, "no parking during P1; the wq pieces (positions 5, 7, 9) are parked in the main loop");
  if (w == 7) WAITVM(8); else if (w == 0) WAITVM(1); else WAITVM(0);
  sfor<NINIT0, NINIT1>(a_issue);
  SB();
  if (w == 0 && lane == 0) lds_st<unsigned>(L, L_ML, Eraw + 1u);
  if (wg >= 240 && wg < 248 && w == 0) {
    const unsigned T = (unsigned)__builtin_amdgcn_readfirstlane((int)Eraw) + 1u;
    const v4u xa = ld<AUX_NT>(rsX, (wg - 240) * 1280 + lane * 16, 0);
    const unsigned xb = __builtin_amdgcn_raw_buffer_load_b32(rsX, (wg - 240) * 1280 + 1024 + lane * 4, 0, AUX_NT);
    float xv[10];
#pragma unroll
    for (int d = 0; d < 4; ++d) { xv[2 * d] = bflo(xa[d]); xv[2 * d + 1] = bfhi(xa[d]); }
    xv[8] = bflo(xb); xv[9] = bfhi(xb);
    const float sm = ((xv[0] + xv[1]) + (xv[2] + xv[3])) + ((xv[4] + xv[5]) + (xv[6] + xv[7])) + (xv[8] + xv[9]);
    const float mean = wave_sum(sm) * (1.f / 640.f);
    float q2 = 0.f;
#pragma unroll
    for (int d = 0; d < 10; ++d) { const float dd = xv[d] - mean; q2 = fmaf(dd, dd, q2); }
    const float var = rbf(wave_sum(q2) * (1.f / 640.f));
    const float mb = rbf(mean), den = rbf(__builtin_amdgcn_sqrtf(rbf(var + 1e-5f)));
    if (lane == 0) stsc(rsWS, WS_XST + (wg - 240) * 16, v4u{__float_as_uint(mb) | (T & 0xffu), __float_as_uint(den) | ((T >> 8) & 0xffu), (T >> 16) & 0xffu, T >> 24});
  }
  {
    auto silu8 = [&](v4u v) AINL {
      v4u r;
#pragma unroll
      for (int d = 0; d < 4; ++d) r[d] = cvtpk(fsilu(bflo(v[d])), fsilu(bfhi(v[d])));
      return r;
    };
    if (t < 160) lds_st<v4u>(L, L_SC + (t / 20) * SC_STRIDE + (t % 20) * 16, silu8(cv0));
    lds_st<v4u>(L, L_WL + w * 1024 + lane * 16, wm0);
    lds_st<v4u>(L, L_WL + (w + 8) * 1024 + lane * 16, wm1);
    if (w < 4) lds_st<v4u>(L, L_WL + (w + 16) * 1024 + lane * 16, wm2);
  }
  SB();
  sfor<NINIT1, NINIT2>(a_issue);
  v2u xh = __builtin_amdgcn_raw_buffer_load_b64(rsX, lane < 20 ? w * 1280 + 160 * b + lane * 8 : OOB, 0, AUX_NT);
  v2u xown = __builtin_amdgcn_raw_buffer_load_b64(rsX, (w == 4 && lane < 5) ? b * 1280 + 40 * h + lane * 8 : OOB, 0, AUX_NT);
  SB();
  BAR();
  const unsigned T = (unsigned)__builtin_amdgcn_readfirstlane((int)lds_ld<unsigned>(L, L_ML));
  sfor<NINIT2, NINIT3>([&](auto mc) AINL { WAITVM(TW - 2 + 2); a_issue(mc); SB(); });
  {
    f4 acc = {0.f, 0.f, 0.f, 0.f};
    const int tl = w & 3, s0_ = (w >> 2) * 3;
    const int trbase = L_WL + (8 * g + ((lane & 15) >> 2)) * 128 + tl * 32 + (lane & 3) * 8;
#pragma unroll
    for (int n = 0; n < 3; ++n) {
      if (n < 2 || w < 4) {
        const int s = s0_ + n;
        const v4u a = lds_ld<v4u>(L, L_SC + (c & 7) * SC_STRIDE + s * 64 + g * 16);
        const s4 b0 = __builtin_amdgcn_ds_read_tr16_b64_v4i16((LAS s4*)(L + trbase + s * 4096));
        const s4 b1 = __builtin_amdgcn_ds_read_tr16_b64_v4i16((LAS s4*)(L + trbase + s * 4096 + 512));
        const v2u u0 = __builtin_bit_cast(v2u, b0), u1 = __builtin_bit_cast(v2u, b1);
        acc = mfma(a, v4u{u0.x, u0.y, u1.x, u1.y}, acc);
      }
    }
    if (g < 2) {
#pragma unroll
      for (int r = 0; r < 4; ++r) lds_st<float>(L, L_PART + (w >> 2) * 2048 + (4 * g + r) * 256 + (16 * tl + c) * 4, acc[r]);
    }
  }
  sfor<NINIT3, NINIT>([&](auto mc) AINL { WAITVM(TW - 2 + 2); a_issue(mc); SB(); });
  BAR();
  {
    const f4 s = lds_ld<f4>(L, L_PART + (t & 127) * 16) + lds_ld<f4>(L, L_PART + 2048 + (t & 127) * 16);
    unsigned o = OOB;
    if (t < 128) { if (role) o = WS_PM + (((t >> 4) * 4 + kq) * 3840 + ta * 64) * 4 + (t & 15) * 16; }
    else if (t == 128) o = WS_ALIVE + wg * 16;
    stsc(rsWS, o, tag4(s, T));
  }
  Sel S;
  {
    const int e = c & 7;
#pragma unroll
    for (int par = 0; par < 2; ++par)
#pragma unroll
      for (int d = 0; d < 4; ++d) {
        const unsigned lo = (unsigned)(2 * par) | ((unsigned)(2 * par + 1) << 8);
        S.s[par][d] = (d == (e >> 1)) ? ((e & 1) ? (0x0c0cu | (lo << 16)) : (0x0c0c0000u | lo)) : 0x0c0c0c0cu;
      }
  }
  unsigned long long pm0, pm1, pm2, pm3;
  auto pmasks = [&]() AINL { pm0 = ballot(po[0] != OOB); pm1 = ballot(po[1] != OOB); pm2 = ballot(po[2] != OOB); pm3 = ballot(po[3] != OOB); };
#pragma unroll
  for (int q = 0; q < 4; ++q) po[q] = lane < 40 ? WS_PM + ((w * 4 + q) * 3840 + (lane / 20) * 640 + 80 * b + 4 * (lane % 20)) * 4 : OOB;
  if (lane == 40) po[0] = WS_XST + w * 16;
  pmasks();
  auto pissue = [&]() AINL {
#pragma unroll
    for (int q = 0; q < 4; ++q) pvv[q] = ldsc(rsWS, po[q]);
  };
  auto pok = [&]() AINL {
    return (ballot(tagok4(pvv[0], T)) & pm0) == pm0 && (ballot(tagok4(pvv[1], T)) & pm1) == pm1 && (ballot(tagok4(pvv[2], T)) & pm2) == pm2 && (ballot(tagok4(pvv[3], T)) & pm3) == pm3;
  };
  auto preload = [&]() AINL {
#pragma unroll
    for (int q = 0; q < 4; ++q) { const bool k1 = tagok4(pvv[q], T); const v4u nv = ldsc(rsWS, k1 ? OOB : po[q]); pvv[q] = k1 ? pvv[q] : nv; }
  };
  auto pblock = [&]() AINL {
    unsigned spins = 0;
    while (!pok() && !expired(spins)) { __builtin_amdgcn_s_sleep(2); FENCE(); preload(); }
  };
  auto psum = [&]() AINL {
    f4 mv;
#pragma unroll
    for (int e = 0; e < 4; ++e) mv[e] = (__uint_as_float(pvv[0][e] & 0xffffff00u) + __uint_as_float(pvv[1][e] & 0xffffff00u)) +
                                        (__uint_as_float(pvv[2][e] & 0xffffff00u) + __uint_as_float(pvv[3][e] & 0xffffff00u));
    float a0 = mv[0], a1 = mv[1], a2 = mv[2], a3 = mv[3]; rbf2(a0, a1); rbf2(a2, a3);
    return f4{a0, a1, a2, a3};
  };
  bool hdone = false;
  auto rprocess = [&]() AINL {
    const f4 mv = psum();
    const float xmean = __uint_as_float((unsigned)__builtin_amdgcn_readlane((int)pvv[0].x, 40) & 0xffff0000u);
    const float xrden = frcp(__uint_as_float((unsigned)__builtin_amdgcn_readlane((int)pvv[0].y, 40) & 0xffff0000u));
    float lnv[4] = {bflo(xh.x) - xmean, bfhi(xh.x) - xmean, bflo(xh.y) - xmean, bfhi(xh.y) - xmean};
    rbf2(lnv[0], lnv[1]); rbf2(lnv[2], lnv[3]);
#pragma unroll
    for (int e = 0; e < 4; ++e) lnv[e] *= xrden;
    rbf2(lnv[0], lnv[1]); rbf2(lnv[2], lnv[3]);
    float hv[4];
#pragma unroll
    for (int e = 0; e < 4; ++e) {
      const float ca = __uint_as_float(__builtin_amdgcn_ds_bpermute(((lane + 20) & 63) * 4, (int)__float_as_uint(mv[e])));
      hv[e] = rbf(lnv[e] * rbf(1.f + ca)) + mv[e];
    }
    if (lane < 20) lds_st<v2u>(L, L_HB + w * 160 + lane * 8, v2u{cvtpk(hv[0], hv[1]), cvtpk(hv[2], hv[3])});
    asm volatile("s_waitcnt lgkmcnt(0)" ::: "memory");
    if (lane == 0) __hip_atomic_fetch_add((LAS unsigned*)(L + L_FLAG), 1u, __ATOMIC_RELAXED, __HIP_MEMORY_SCOPE_WORKGROUP);
  };
  auto mprocess = [&]() AINL {
    const f4 mv = psum();
    v4u msg;
#pragma unroll
    for (int e = 0; e < 4; ++e) msg[e] = (__float_as_uint(rbf(lane >= 10 ? 1.f + mv[e] : mv[e])) & 0xffff0000u) | ((T >> (8 * e)) & 0xffu);
    if (lane >= 5 && lane < 15) stsc(rsWS, WS_AGP + (b * 32 + h) * 256 + lane * 16, msg);
    else if (lane < 5) lds_st<f4>(L, L_GAB + lane * 16, mv);
    else if (lane < 20) lds_st<f4>(L, L_GAB + 80 + (lane - 15) * 16, mv);
  };
  auto bfpack = [&](v4u v) AINL { return v2u{__builtin_amdgcn_perm(v.y, v.x, 0x07060302u), __builtin_amdgcn_perm(v.w, v.z, 0x07060302u)}; };
  auto hflag = [&]() AINL { FENCE(); return __builtin_amdgcn_readfirstlane((int)*(volatile LAS unsigned*)(L + L_FLAG)) == 8; };
  auto hstep = [&]() AINL {
    FENCE();
    const int pi = w & 3, hf = w >> 2;
    unsigned X[5]; v4u Bp[10];
#pragma unroll
    for (int d = 0; d < 5; ++d) X[d] = lds_ld<unsigned>(L, L_HB + (2 * pi + (c >> 3)) * 160 + 40 * g + 20 * hf + d * 4);
#pragma unroll
    for (int i = 0; i < 10; ++i) Bp[i] = lds_ld<v4u>(L, L_WQL + (10 * hf + i) * 1024 + lane * 16);
    f4 a0 = {0.f, 0.f, 0.f, 0.f}, a1 = {0.f, 0.f, 0.f, 0.f};
#pragma unroll
    for (int i = 0; i < 10; i += 2) { a0 = mfma(diagA(X[i >> 1], S, 0), Bp[i], a0); a1 = mfma(diagA(X[i >> 1], S, 1), Bp[i + 1], a1); }
    a0 += a1;
    lds_st<f4>(L, L_QP8 + w * 1024 + lane * 16, a0);
    asm volatile("s_waitcnt lgkmcnt(0)" ::: "memory");
    unsigned old = 0;
    if (lane == 0) old = __hip_atomic_fetch_add((LAS unsigned*)(L + L_FLAG + 4 + pi * 4), 1u, __ATOMIC_RELAXED, __HIP_MEMORY_SCOPE_WORKGROUP);
    if (__builtin_amdgcn_readfirstlane((int)old) == 1) {
      FENCE();
      const f4 lo = lds_ld<f4>(L, L_QP8 + pi * 1024 + lane * 16), hi = lds_ld<f4>(L, L_QP8 + (pi + 4) * 1024 + lane * 16);
      stsc(rsWS, WS_QPM + (h * 8 + b) * 4096 + (2 * pi + (g >> 1)) * 512 + (2 * c + (g & 1)) * 16, tag4(lo + hi, T));
    }
  };
  static_assert(a_kpos(RL) >= a_kpos(6) + 1 + TW, "wq parked before the first h arrival");
  static_assert(a_kpos(RL) >= a_kpos(RK + 3) + 1 + TW - 1 && RL <= HK && HK + (HN - 1) * HD < QK - 1 && RL < QK - 1 && QK + 3 < 31, "stage order");
  sfor<NINIT, NA>([&](auto mc) AINL {
    constexpr int m = decltype(mc)::value;
    constexpr It it = a_item(m);
    constexpr int k = it.kind == 0 ? it.idx : -100;
    WAITW(TW - 1 + (m - (TW - DA_LO) < NINIT ? 1 : 0) + a_free_between(m - (TW - DA_LO), m - 1), DA_LO, DA_HI);
    {
      constexpr It ol = a_item(m - TW);
      if constexpr (ol.kind == 4) { if (ol.idx < 2 || w < 4) lds_st<v4u>(L, L_WQL + (w + 8 * ol.idx) * 1024 + lane * 16, WQ[ol.idx]); }
      if constexpr (ol.kind == 0 && ol.idx >= NR) lds_st<v4u>(L, ksoff + (ol.idx - NR) * 1024, KL[(ol.idx - NR) % (TW + 1)]);
    }
    a_issue(mc);
    if constexpr (k == 9) { if (w == 4 && lane < 5) lds_st<v2u>(L, L_XR + lane * 8, xown); }
    if constexpr (k == 10) asm volatile("" :: "v"(pg0), "v"(pg1), "v"(pg2), "v"(pg3), "v"(pg4), "v"(pg5), "v"(pg6), "v"(pg7));
    if constexpr (k == RL) { pblock(); rprocess(); }
    if constexpr (k >= HK && k < HK + (HN - 1) * HD && (k - HK) % HD == 0) { if (!hdone && hflag()) { hstep(); hdone = true; } }
    if constexpr (k == HK + (HN - 1) * HD) {
      if (!hdone) { unsigned spins = 0; while (!hflag() && !expired(spins)) __builtin_amdgcn_s_sleep(1); hstep(); }
    }
    if constexpr (k == QK - 1) {
#pragma unroll
      for (int q = 0; q < 4; ++q) {
        unsigned o = OOB;
        if (w == 5) { if (lane < 20) o = WS_PM + ((b * 4 + q) * 3840 + (2 + lane / 5) * 640 + 20 * h + 4 * (lane % 5)) * 4; }
        else if (w == 6) o = WS_QPM + ((h * 8 + 2 * q + (lane >> 5)) * 8 + b) * 512 + (lane & 31) * 16;
        else if (w == 4 && wg == 0) o = WS_ALIVE + (64 * q + lane) * 16;
        po[q] = o;
      }
      pmasks();
    }
    SB();
  });
  v4u W1[10], W2[10], VR[VRN];
  const unsigned offW1 = mlp ? (unsigned)(80 * w + 40 * (c >> 3) + 10 * g) * 3456u + (64 * h + 8 * (c & 7)) * 2 : OOB;
  const unsigned offW2 = mlp ? (unsigned)(64 * h + 8 * w + 2 * g) * 1280u + c * 16 : OOB;
  auto b_issue = [&](auto mc) AINL {
    constexpr It it = b_item(decltype(mc)::value);
    if constexpr (it.kind == 1) VR[it.idx % VRN] = ld<AUX_V>(rsV, offKV, it.idx * 8192);
    else if constexpr (it.kind == 2) ldwo(IC<it.idx>{});
    else W1[it.idx] = ld<AUX_W>(rsW1, offW1, it.idx * 3456);
    SB();
  };
#define VTICK(k) do { WAITW(TW - 1, DB_LO, DB_HI); b_issue(IC<(k)>{}); } while (0)
  sfor<0, VPRE>([&](auto kc) AINL { WAITW(TW - 1 + a_free_between(NA + decltype(kc)::value - (TW - DB_LO), NA - 1), DB_LO, DB_HI); b_issue(kc); });
  if (w == 5) { pblock(); mprocess(); }
  if ((w == 6 || w == 4)) {
    pblock();
    if (w == 6) {
      f4 q4;
#pragma unroll
      for (int e = 0; e < 4; ++e) q4[e] = x32_sum((__uint_as_float(pvv[0][e] & 0xffffff00u) + __uint_as_float(pvv[1][e] & 0xffffff00u)) +
                                                  (__uint_as_float(pvv[2][e] & 0xffffff00u) + __uint_as_float(pvv[3][e] & 0xffffff00u)));
      if (lane < 32) lds_st<v2u>(L, L_QB + lane * 8, v2u{cvtpk(q4[0], q4[1]), cvtpk(q4[2], q4[3])});
    } else {
      if (wg == 0 && lane == 0) __builtin_amdgcn_raw_buffer_store_b32(T, rsWS, WS_EPOCH, 0, AUX_SC1);
    }
  }
  BAR();
  const v4u qr = lds_ld<v4u>(L, L_QB + c * 16);
  auto kdot = [&](v4u k) AINL { return dot2(k.w, qr[3], dot2(k.z, qr[2], dot2(k.y, qr[1], dot2(k.x, qr[0], 0.f)))); };
  auto treduce = [&](float (&xs)[16]) AINL {
    float x[8];
#pragma unroll
    for (int i = 0; i < 8; ++i) { const float lo = xs[i] + dppf<XOR8>(xs[i]), hi = xs[i + 8] + dppf<XOR8>(xs[i + 8]); x[i] = (c & 8) ? hi : lo; }
#pragma unroll
    for (int i = 0; i < 4; ++i) { const float lo = x[i] + dppf<XOR7>(x[i]), hi = x[i + 4] + dppf<XOR7>(x[i + 4]); x[i] = (c & 4) ? hi : lo; }
#pragma unroll
    for (int i = 0; i < 2; ++i) { const float lo = x[i] + dppf<XOR2>(x[i]), hi = x[i + 2] + dppf<XOR2>(x[i + 2]); x[i] = (c & 2) ? hi : lo; }
    const float lo = x[0] + dppf<XOR1>(x[0]), hi = x[1] + dppf<XOR1>(x[1]);
    return (c & 1) ? hi : lo;
  };
  constexpr float C2 = 0.08838834764831845f * 1.4426950408889634f;
  float mw, fw = 0.f;
  unsigned ppk;
  {
    float xs[16];
#pragma unroll
    for (int j = 0; j < 16; ++j) xs[j] = kdot(KB[j]);
    SB();
    VTICK(VPRE);
    const float s0 = treduce(xs);
    SB();
    VTICK(VPRE + 1);
#pragma unroll
    for (int j = 0; j <= LASTPARK - NR; ++j) xs[j] = kdot(lds_ld<v4u>(L, ksoff + j * 1024));
#pragma unroll
    for (int j = LASTPARK - NR + 1; j < 16; ++j) xs[j] = kdot(KL[j % (TW + 1)]);
    SB();
    sfor<VPRE + 2, (VPRE + 4 < VRN + WEARLY ? VPRE + 4 : VRN + WEARLY)>([&](auto kc) AINL { WAITW(TW - 1, DB_LO, DB_HI); b_issue(kc); });
    const float s1 = treduce(xs);
    SB();
    sfor<(VPRE + 4 < VRN + WEARLY ? VPRE + 4 : VRN + WEARLY), VRN + WEARLY>([&](auto kc) AINL { WAITW(TW - 1, DB_LO, DB_HI); b_issue(kc); });
    mw = wave_max(fmaxf(s0, s1));
    const float p0 = __builtin_amdgcn_exp2f((s0 - mw) * C2), p1 = __builtin_amdgcn_exp2f((s1 - mw) * C2);
    const float lw = wave_sum(p0 + p1);
    ppk = cvtpk(p0, p1);
    if (lane == 0) lds_st<v2u>(L, L_ML + w * 8, v2u{__float_as_uint(mw), __float_as_uint(lw)});
  }
  f4 oacc = {0.f, 0.f, 0.f, 0.f};
  unsigned touch0 = 0, touch1 = 0;
  auto pvstep = [&](auto jc) AINL {
    constexpr int j = decltype(jc)::value;
    const unsigned X = dppu<0x150 + (j & 15)>(ppk);
    oacc = mfma(diagA(X, S, j >> 4), VR[j % VRN], oacc);
    if constexpr (j == 4) {
#pragma unroll
      for (int q = 0; q < 4; ++q) { const int idx = 64 * q + lane; po[q] = ((w == 1 || w == 2) && mlp && idx < 160) ? WS_AGP + (b * 32 + idx / 5) * 256 + (5 * w + idx % 5) * 16 : OOB; }
      pmasks(); pissue();
    }
    if constexpr (j == 14) {
      if ((w == 1 || w == 2)) {
        pblock();
#pragma unroll
        for (int n = 0; n < 3; ++n) {
          const int idx = 64 * n + lane;
          if (po[n] != OOB) lds_st<v2u>(L, (w == 1 ? L_SB : L_CB) + (20 * (idx / 5) + 4 * (idx % 5)) * 2, bfpack(pvv[n]));
        }
      }
    }
    if constexpr (j == 8) {
      unsigned o0 = OOB, o1 = OOB;
      if (w == 4) {
        const int u = lane & 31; const int wgu = ((u >> 2) << 5) | (b << 2) | (u & 3);
        o0 = (lane < 32 ? WS_OUT1 : WS_OUT2) + wgu * 4096 + h * 128;
        o1 = lane < 32 ? WS_AG + (b * 32 + lane) * 128 : OOB;
      } else if (w == 0) { o0 = (lane < 32 ? WS_OUT1 : WS_OUT2) + wg * 4096 + (lane & 31) * 128; }
      touch0 = __builtin_amdgcn_raw_buffer_load_b32(rsWS, (int)o0, 0, AUX_SC1);
      touch1 = __builtin_amdgcn_raw_buffer_load_b32(rsWS, (int)o1, 0, AUX_SC1);
    }
    if constexpr (j == 16) { asm volatile("" :: "v"(touch0), "v"(touch1)); }
    if constexpr (j == 10) {
      BAR();
      float M = -INFINITY;
      v2u ml[8];
#pragma unroll
      for (int ww = 0; ww < 8; ++ww) { ml[ww] = lds_ld<v2u>(L, L_ML + ww * 8); M = fmaxf(M, __uint_as_float(ml[ww].x)); }
      float Ls = 0.f;
#pragma unroll
      for (int ww = 0; ww < 8; ++ww) Ls += __builtin_amdgcn_exp2f((__uint_as_float(ml[ww].x) - M) * C2) * __uint_as_float(ml[ww].y);
      fw = __builtin_amdgcn_exp2f((mw - M) * C2) * frcp(Ls);
      PIN(fw);
    }
  };
  sfor<VRN + WEARLY, NB>([&](auto mc) AINL {
    constexpr int m = decltype(mc)::value;
    constexpr It it = b_item(m);
    if constexpr (it.kind == 1) pvstep(IC<it.idx - VRN>{});
    if constexpr (m == NBV) sfor<32 - VRN, 22>([&](auto jc) AINL { pvstep(jc); SB(); });
    if constexpr (it.kind != 3) WAITW(TW - 1 + b_extra(m), DB_LO, DB_HI);
    else { pvstep(IC<22 + it.idx>{}); }
    b_issue(mc);
  });
  {
    const f4 ov = oacc * fw;
    if (lane < 32) lds_st<f4>(L, L_OP + w * 512 + (8 * c + 4 * g) * 4, ov);
  }
  BAR();
  const int ypoff = L_YP + w * 2560 + (8 * c + 4 * (g & 1)) * 4;
  {
    const int kap = (c >> 1) & 3, sig = (c & 1) + 2 * (c >> 3);
    const int ko = (16 * w + 4 * g + kap) * 4;
    float o = lds_ld<float>(L, L_OP + (2 * sig) * 512 + ko) + lds_ld<float>(L, L_OP + (2 * sig + 1) * 512 + ko);
    o += dppf<XOR1>(o); o += dppf<XOR8>(o);
    const unsigned P = cvtpk(dppf<QB0>(o), dppf<QB2>(o)), Q = dppu<XOR7>(P);
    const unsigned X0 = (c & 4) ? Q : P, X1 = (c & 4) ? P : Q;
    f4 y[5];
    { const v4u A = diagA(X0, S, 0);
#pragma unroll
      for (int cb = 0; cb < 5; ++cb) y[cb] = mfma(A, WO[cb], f4{0.f, 0.f, 0.f, 0.f}); }
    { const v4u A = diagA(X0, S, 1);
#pragma unroll
      for (int cb = 0; cb < 5; ++cb) y[cb] = mfma(A, WO[5 + cb], y[cb]); }
    { const v4u A = diagA(X1, S, 0);
#pragma unroll
      for (int cb = 0; cb < 5; ++cb) y[cb] = mfma(A, WO[10 + cb], y[cb]); }
    { const v4u A = diagA(X1, S, 1);
#pragma unroll
      for (int cb = 0; cb < 5; ++cb) y[cb] = mfma(A, WO[15 + cb], y[cb]); }
    if (lane < 32) {
#pragma unroll
      for (int cb = 0; cb < 5; ++cb) lds_st<f4>(L, ypoff + cb * 512, y[cb]);
    }
  }
  BAR();
  auto publish = [&](int wsoff) AINL {
    const int k = t & 7, j = t >> 3;
    const int o = L_YP + (20 * j + 4 * (k < 5 ? k : 0)) * 4;
    f4 s0 = lds_ld<f4>(L, o) + lds_ld<f4>(L, o + 2560), s1 = lds_ld<f4>(L, o + 2 * 2560) + lds_ld<f4>(L, o + 3 * 2560);
    f4 s2 = lds_ld<f4>(L, o + 4 * 2560) + lds_ld<f4>(L, o + 5 * 2560), s3 = lds_ld<f4>(L, o + 6 * 2560) + lds_ld<f4>(L, o + 7 * 2560);
    stsc(rsWS, wsoff + t * 16, tag4((s0 + s1) + (s2 + s3), T));
  };
  float x1v[4] = {0.f, 0.f, 0.f, 0.f};
  auto rs_gather = [&](int wsbase, int nprod) AINL -> f4 {
    const int pl = lane >> 3, k = lane & 7;
    unsigned off[4]; v4u v[4];
#pragma unroll
    for (int q = 0; q < 4; ++q) {
      const int u = 8 * q + pl;
      const int wgu = ((u >> 2) << 5) | (b << 2) | (u & 3);
      off[q] = (k < 5 && u < nprod) ? wsbase + wgu * 4096 + h * 128 + k * 16 : OOB;
      PIN(off[q]);
    }
    const unsigned long long m0 = ballot(off[0] != OOB), m3 = ballot(off[3] != OOB);
#pragma unroll
    for (int q = 0; q < 4; ++q) v[q] = ldsc(rsWS, off[q]);
    unsigned spins = 0;
    for (;;) {
      const bool k0 = tagok4(v[0], T), k1 = tagok4(v[1], T), k2 = tagok4(v[2], T), k3 = tagok4(v[3], T);
      if ((ballot(k0) & ballot(k1) & ballot(k2) & m0) == m0 && (ballot(k3) & m3) == m3) break;
      if (expired(spins)) break;
      __builtin_amdgcn_s_sleep(POLL_SLEEP);
      FENCE();
      const v4u n0 = ldsc(rsWS, k0 ? OOB : off[0]), n1 = ldsc(rsWS, k1 ? OOB : off[1]), n2 = ldsc(rsWS, k2 ? OOB : off[2]), n3 = ldsc(rsWS, k3 ? OOB : off[3]);
      v[0] = k0 ? v[0] : n0; v[1] = k1 ? v[1] : n1; v[2] = k2 ? v[2] : n2; v[3] = k3 ? v[3] : n3;
    }
    f4 s;
#pragma unroll
    for (int e = 0; e < 4; ++e) {
      float a = (__uint_as_float(v[0][e] & 0xffffff00u) + __uint_as_float(v[1][e] & 0xffffff00u)) + (__uint_as_float(v[2][e] & 0xffffff00u) + __uint_as_float(v[3][e] & 0xffffff00u));
      a += dppf<XOR8>(a); s[e] = x32_sum(x16_sum(a));
    }
    return s;
  };
  if (w < 4) publish(WS_OUT1 + wg * 4096);
  if (w != 4) {
#pragma unroll
    for (int i = 0; i < 10; ++i) W2[i] = ld<AUX_W>(rsW2, offW2, (i / 5) * 1280 + (i % 5) * 256);
  }
  if (w == 4) {
    const v2u xr = lds_ld<v2u>(L, L_XR + (lane & 7) * 8); const f4 gg = lds_ld<f4>(L, L_GAB + (lane & 7) * 16);
    unsigned off[3]; v4u v[3]; float sbv[12], cbv[12]; int hoff[3];
#pragma unroll
    for (int q = 0; q < 3; ++q) {
      const int idx = 64 * q + lane, jj = idx / 5, k = idx % 5;
      off[q] = (idx < 160 && mlp) ? WS_AG + (b * 32 + jj) * 128 + k * 16 : OOB;
      const int col = idx < 160 ? 20 * jj + 4 * k : 0;
      const v2u s2 = lds_ld<v2u>(L, L_SB + col * 2), c2 = lds_ld<v2u>(L, L_CB + col * 2);
      sbv[4 * q] = bflo(s2.x); sbv[4 * q + 1] = bfhi(s2.x); sbv[4 * q + 2] = bflo(s2.y); sbv[4 * q + 3] = bfhi(s2.y);
      cbv[4 * q] = bflo(c2.x); cbv[4 * q + 1] = bfhi(c2.x); cbv[4 * q + 2] = bflo(c2.y); cbv[4 * q + 3] = bfhi(c2.y);
      hoff[q] = L_H2 + col * 2;
      PIN(off[q]); PIN(hoff[q]);
    }
#pragma unroll
    for (int i = 0; i < 12; ++i) { PIN(sbv[i]); PIN(cbv[i]); }
    float xx[4] = {bflo(xr.x), bfhi(xr.x), bflo(xr.y), bfhi(xr.y)};
    const unsigned long long am0 = ballot(off[0] != OOB), am2 = ballot(off[2] != OOB);
    __builtin_amdgcn_s_sleep(D_RS1);
    const f4 s = rs_gather(WS_OUT1, 32);
    v4u m;
    {
      float a0 = s[0], a1 = s[1], a2 = s[2], a3 = s[3];
      rbf2(a0, a1); rbf2(a2, a3);
      a0 *= gg[0]; a1 *= gg[1]; a2 *= gg[2]; a3 *= gg[3];
      rbf2(a0, a1); rbf2(a2, a3);
      a0 += xx[0]; a1 += xx[1]; a2 += xx[2]; a3 += xx[3];
      const unsigned p01 = cvtpk(a0, a1), p23 = cvtpk(a2, a3);
      m = v4u{(p01 << 16) | (T & 0xffu), (p01 & 0xffff0000u) | ((T >> 8) & 0xffu), (p23 << 16) | ((T >> 16) & 0xffu), (p23 & 0xffff0000u) | (T >> 24)};
      x1v[0] = bflo(p01); x1v[1] = bfhi(p01); x1v[2] = bflo(p23); x1v[3] = bfhi(p23);
    }
    if (lane < 5) stsc(rsWS, WS_AG + (b * 32 + h) * 128 + lane * 16, m);
#pragma unroll
    for (int i = 0; i < 10; ++i) W2[i] = ld<AUX_W>(rsW2, offW2, (i / 5) * 1280 + (i % 5) * 256);
    if (mlp) {
      __builtin_amdgcn_s_sleep(D_AG);
#pragma unroll
      for (int q = 0; q < 3; ++q) v[q] = ldsc(rsWS, off[q]);
      unsigned spins = 0;
      for (;;) {
        const bool k0 = tagok4(v[0], T), k1 = tagok4(v[1], T), k2 = tagok4(v[2], T);
        if ((ballot(k0) & ballot(k1) & am0) == am0 && (ballot(k2) & am2) == am2) break;
        if (expired(spins)) break;
        __builtin_amdgcn_s_sleep(POLL_SLEEP);
        FENCE();
        const v4u n0 = ldsc(rsWS, k0 ? OOB : off[0]), n1 = ldsc(rsWS, k1 ? OOB : off[1]), n2 = ldsc(rsWS, k2 ? OOB : off[2]);
        v[0] = k0 ? v[0] : n0; v[1] = k1 ? v[1] : n1; v[2] = k2 ? v[2] : n2;
      }
      float xv[12]; float sm = 0.f;
#pragma unroll
      for (int q = 0; q < 3; ++q)
#pragma unroll
        for (int e = 0; e < 4; ++e) { xv[4 * q + e] = __uint_as_float(v[q][e] & 0xffff0000u); sm += xv[4 * q + e]; }
      const float mean = wave_sum(sm) * (1.f / 640.f);
      float q2 = 0.f, q3 = 0.f;
#pragma unroll
      for (int i = 0; i < 8; ++i) { const float dd = xv[i] - mean; q2 += dd * dd; }
#pragma unroll
      for (int i = 8; i < 12; ++i) { const float dd = xv[i] - mean; q3 += dd * dd; }
      q2 += lane < 32 ? q3 : 0.f;
      const float var = rbf(wave_sum(q2) * (1.f / 640.f));
      const float mb = rbf(mean), rden = frcp(rbf(__builtin_amdgcn_sqrtf(rbf(var + 1e-5f))));
#pragma unroll
      for (int q = 0; q < 3; ++q) {
        float a[4];
#pragma unroll
        for (int e = 0; e < 4; ++e) a[e] = xv[4 * q + e] - mb;
        rbf2(a[0], a[1]); rbf2(a[2], a[3]);
#pragma unroll
        for (int e = 0; e < 4; ++e) a[e] *= rden;
        rbf2(a[0], a[1]); rbf2(a[2], a[3]);
#pragma unroll
        for (int e = 0; e < 4; ++e) a[e] *= cbv[4 * q + e];
        rbf2(a[0], a[1]); rbf2(a[2], a[3]);
        const v2u o = {cvtpk(a[0] + sbv[4 * q], a[1] + sbv[4 * q + 1]), cvtpk(a[2] + sbv[4 * q + 2], a[3] + sbv[4 * q + 3])};
        if (q < 2 || lane < 32) lds_st<v2u>(L, hoff[q], o);
      }
    }
  }
  if (mlp) {
    BAR();
    {
      unsigned X[5];
#pragma unroll
      for (int d = 0; d < 5; ++d) X[d] = lds_ld<unsigned>(L, L_H2 + 160 * w + 80 * (c >> 3) + 20 * g + d * 4);
      f4 a0 = {0.f, 0.f, 0.f, 0.f}, a1 = {0.f, 0.f, 0.f, 0.f};
#pragma unroll
      for (int p = 0; p < 10; p += 2) { a0 = mfma(diagA(X[p >> 1], S, 0), W1[p], a0); a1 = mfma(diagA(X[p >> 1], S, 1), W1[p + 1], a1); }
      a0 += a1;
      if ((g >> 1) == (c >> 3)) lds_st<f4>(L, L_HP + (2 * w + (c >> 3)) * 256 + (8 * (c & 7) + 4 * (g & 1)) * 4, a0);
    }
    BAR();
    {
      const int kap = (c >> 2) & 1, sig = (c & 3) + 4 * (c >> 3);
      const int n = (8 * w + 2 * g + kap) * 4;
      float hd = lds_ld<float>(L, L_HP + (2 * sig) * 256 + n) + lds_ld<float>(L, L_HP + (2 * sig + 1) * 256 + n);
      hd += dppf<XOR1>(hd); hd += dppf<XOR2>(hd); hd += dppf<XOR8>(hd);
      hd = fsilu(rbf(hd));
      const float ot = dppf<XOR7>(hd);
      const unsigned X = kap ? cvtpk(ot, hd) : cvtpk(hd, ot);
      const v4u A0 = diagA(X, S, 0), A1 = diagA(X, S, 1);
      f4 y[5];
#pragma unroll
      for (int cb = 0; cb < 5; ++cb) y[cb] = mfma(A0, W2[cb], f4{0.f, 0.f, 0.f, 0.f});
#pragma unroll
      for (int cb = 0; cb < 5; ++cb) y[cb] = mfma(A1, W2[5 + cb], y[cb]);
      if (lane < 32) {
#pragma unroll
        for (int cb = 0; cb < 5; ++cb) lds_st<f4>(L, ypoff + cb * 512, y[cb]);
      }
    }
    BAR();
    if (w < 4) publish(WS_OUT2 + wg * 4096);
  }
  if (w == 4) {
    const f4 gg = lds_ld<f4>(L, L_GAB + 80 + (lane & 7) * 16);
    if (!mlp) __builtin_amdgcn_s_sleep(48);
    __builtin_amdgcn_s_sleep(D_RS2);
    const f4 s = rs_gather(WS_OUT2, 27);
    float a0 = s[0], a1 = s[1], a2 = s[2], a3 = s[3];
    rbf2(a0, a1); rbf2(a2, a3);
    a0 *= gg[0]; a1 *= gg[1]; a2 *= gg[2]; a3 *= gg[3];
    rbf2(a0, a1); rbf2(a2, a3);
    if (lane < 5) __builtin_amdgcn_raw_buffer_store_b64(v2u{cvtpk(x1v[0] + a0, x1v[1] + a1), cvtpk(x1v[2] + a2, x1v[3] + a3)}, rsOUT, b * 1280 + 40 * h + lane * 8, 0, AUX_SC1);
  }
}

extern "C" void kernel_launch(void* const* d_in, const int* in_sizes, int n_in, void* d_out, int out_size, void* d_ws, size_t ws_size,
                              hipStream_t stream) {
  fused_kernel<<<256, 512, 0, stream>>>(d_in[0], d_in[1], d_in[2], d_in[3], d_in[4], d_in[6], d_in[7], d_in[8], d_in[9], d_out, d_ws);
}
```
